```python
import torch

def _softmax(x, axis=-1):
    return torch.softmax(x, dim=axis)
def _gelu(x):
    return torch.nn.functional.gelu(x, approximate="tanh")
def _silu(x):
    return torch.nn.functional.silu(x)
def _sigmoid(x):
    return torch.sigmoid(x)
def _layernorm(x, axis=-1, eps=1e-5):
    m = x.mean(dim=axis, keepdim=True)
    v = x.var(dim=axis, keepdim=True, unbiased=False)
    return (x - m) / torch.sqrt(v + eps)
def _scatter_add(tgt, idx, src, axis=0):
    return tgt.clone().index_add_(axis, idx, src)
def _segment_sum(src, seg, n):
    out = torch.zeros((n,) + src.shape[1:], dtype=src.dtype)
    return out.index_add_(0, seg, src)
def _pad(x, pw):
    flat = tuple(v for pair in reversed(pw) for v in pair)
    return torch.nn.functional.pad(x, flat)

kernel_name = 'concat_heads__gqa_block_cached__gqa_block_cached__near_ridge'

def setup_inputs():

    _w = (lambda a: (lambda t: t.to(torch.bfloat16) if t.is_floating_point() else t)(torch.as_tensor(a)))
    def _A_setup():
        B, Sq, D = 8, 192, 512
        Hq, Hkv, Sk, Dh = 32, 32, 1024, 128
        assert Hq % Hkv == 0, (Hq, Hkv)
        return {
            "x": _w((lambda *s: torch.randn(*s, dtype=torch.bfloat16) * 0.8)(B, Sq, D)),
            "k_cache": _w((lambda *s: torch.randn(*s, dtype=torch.bfloat16) * 0.8)(B, Hkv, Sk, Dh)),
            "v_cache": _w((lambda *s: torch.randn(*s, dtype=torch.bfloat16) * 0.05)(B, Hkv, Sk, Dh)),
            "wq": _w((lambda *s: torch.randn(*s, dtype=torch.bfloat16) * 0.8)(D, Hq * Dh) * (D ** -0.5)),
            "wo": _w((lambda *s: torch.randn(*s, dtype=torch.bfloat16) * 0.05)(Hq * Dh, D) * ((Hq * Dh) ** -0.5)),
        }
    _A_in = _A_setup()

    def _B_setup():
        B, Sq, D = 8, 192, 512
        Hq, Hkv, Sk, Dh = 32, 32, 1024, 128
        assert Hq % Hkv == 0, (Hq, Hkv)
        return {
            "x": _w((lambda *s: torch.randn(*s, dtype=torch.bfloat16) * 0.8)(B, Sq, D)),
            "k_cache": _w((lambda *s: torch.randn(*s, dtype=torch.bfloat16) * 0.8)(B, Hkv, Sk, Dh)),
            "v_cache": _w((lambda *s: torch.randn(*s, dtype=torch.bfloat16) * 0.05)(B, Hkv, Sk, Dh)),
            "wq": _w((lambda *s: torch.randn(*s, dtype=torch.bfloat16) * 0.8)(D, Hq * Dh) * (D ** -0.5)),
            "wo": _w((lambda *s: torch.randn(*s, dtype=torch.bfloat16) * 0.05)(Hq * Dh, D) * ((Hq * Dh) ** -0.5)),
        }
    _B_in = _B_setup()

    return {
        "A_x": _A_in["x"],
        "A_k_cache": _A_in["k_cache"],
        "A_v_cache": _A_in["v_cache"],
        "A_wq": _A_in["wq"],
        "A_wo": _A_in["wo"],
        "B_x": _B_in["x"],
        "B_k_cache": _B_in["k_cache"],
        "B_v_cache": _B_in["v_cache"],
        "B_wq": _B_in["wq"],
        "B_wo": _B_in["wo"],
    }

def reference(A_x, A_k_cache, A_v_cache, A_wq, A_wo, B_x, B_k_cache, B_v_cache, B_wq, B_wo):
    def _A_ref(x, k_cache, v_cache, wq, wo):
        B, Sq, D = x.shape
        Hq, Dh = 32, 128
        Hkv = k_cache.shape[1]
        rep = Hq // Hkv
        q = (lambda x, *d: x.permute(*d))((x @ wq).reshape(B, Sq, Hq, Dh), 0, 2, 1, 3)
        k = (lambda x, n, axis: torch.repeat_interleave(x, n, dim=axis))(k_cache, rep, axis=1)
        v = (lambda x, n, axis: torch.repeat_interleave(x, n, dim=axis))(v_cache, rep, axis=1)
        scores = torch.einsum("bhqd,bhkd->bhqk", q, k) * (Dh ** -0.5)
        p = _softmax(scores, axis=-1)
        o = torch.einsum("bhqk,bhkd->bhqd", p, v)
        return (lambda x, *d: x.permute(*d))(o, 0, 2, 1, 3).reshape(B, Sq, Hq * Dh) @ wo

    def _B_ref(x, k_cache, v_cache, wq, wo):
        B, Sq, D = x.shape
        Hq, Dh = 32, 128
        Hkv = k_cache.shape[1]
        rep = Hq // Hkv
        q = (lambda x, *d: x.permute(*d))((x @ wq).reshape(B, Sq, Hq, Dh), 0, 2, 1, 3)
        k = (lambda x, n, axis: torch.repeat_interleave(x, n, dim=axis))(k_cache, rep, axis=1)
        v = (lambda x, n, axis: torch.repeat_interleave(x, n, dim=axis))(v_cache, rep, axis=1)
        scores = torch.einsum("bhqd,bhkd->bhqk", q, k) * (Dh ** -0.5)
        p = _softmax(scores, axis=-1)
        o = torch.einsum("bhqk,bhkd->bhqd", p, v)
        return (lambda x, *d: x.permute(*d))(o, 0, 2, 1, 3).reshape(B, Sq, Hq * Dh) @ wo

    a = _A_ref(A_x, A_k_cache, A_v_cache, A_wq, A_wo)
    b = _B_ref(B_x, B_k_cache, B_v_cache, B_wq, B_wo)
    return torch.concatenate([a, b], axis=-1)
```

```cpp
#include <hip/hip_runtime.h>
#include <hip/hip_bf16.h>
typedef unsigned short u16;
__device__ __forceinline__ float bf2f(u16 b) { return __uint_as_float((unsigned)b << 16); }
__device__ __forceinline__ u16 f2bf(float f) { unsigned u = __float_as_uint(f); u += 0x7fff + ((u >> 16) & 1); return (u16)(u >> 16); }
__global__ void n_qproj(const u16* x, const u16* wq, float* q) {
  int row = blockIdx.y, n = blockIdx.x * 256 + threadIdx.x; float s = 0;
  for (int c = 0; c < 512; ++c) s += bf2f(x[row * 512 + c]) * bf2f(wq[c * 4096 + n]);
  q[(size_t)row * 4096 + n] = bf2f(f2bf(s));
}
__global__ void n_attn(const float* q, const u16* k, const u16* v, float* o) {
  __shared__ float sc[1024]; __shared__ float qs[128]; __shared__ float red[128];
  int row = blockIdx.y, h = blockIdx.x, b = row / 192, t = threadIdx.x;
  qs[t] = q[(size_t)row * 4096 + h * 128 + t]; __syncthreads();
  const u16* kb = k + ((size_t)(b * 32 + h)) * 1024 * 128; const u16* vb = v + ((size_t)(b * 32 + h)) * 1024 * 128;
  float mx = -1e30f;
  for (int j = t; j < 1024; j += 128) { float s = 0; for (int d = 0; d < 128; ++d) s += qs[d] * bf2f(kb[j * 128 + d]); s *= 0.08838834764831845f; sc[j] = s; mx = fmaxf(mx, s); }
  red[t] = mx; __syncthreads(); for (int i = 64; i > 0; i >>= 1) { if (t < i) red[t] = fmaxf(red[t], red[t + i]); __syncthreads(); } mx = red[0]; __syncthreads();
  float l = 0; for (int j = t; j < 1024; j += 128) { float e = expf(sc[j] - mx); sc[j] = e; l += e; }
  red[t] = l; __syncthreads(); for (int i = 64; i > 0; i >>= 1) { if (t < i) red[t] += red[t + i]; __syncthreads(); } l = red[0];
  float a = 0; for (int j = 0; j < 1024; ++j) a += sc[j] * bf2f(vb[j * 128 + t]);
  o[(size_t)row * 4096 + h * 128 + t] = bf2f(f2bf(a / l));
}
__global__ void n_oproj(const float* o, const u16* wo, u16* out, int br) {
  int row = blockIdx.y, n = blockIdx.x * 256 + threadIdx.x; float s = 0;
  for (int c = 0; c < 4096; ++c) s += o[(size_t)row * 4096 + c] * bf2f(wo[c * 512 + n]);
  out[(size_t)row * 1024 + br * 512 + n] = f2bf(s);
}
extern "C" void kernel_launch(void* const* d_in, const int* in_sizes, int n_in, void* d_out, int out_size, void* d_ws, size_t ws_size, hipStream_t stream) {
  float* q = (float*)d_ws; float* o = q + (size_t)1536 * 4096;
  for (int br = 0; br < 2; ++br) {
    const u16* x = (const u16*)d_in[br * 5 + 0]; const u16* k = (const u16*)d_in[br * 5 + 1]; const u16* v = (const u16*)d_in[br * 5 + 2];
    const u16* wq = (const u16*)d_in[br * 5 + 3]; const u16* wo = (const u16*)d_in[br * 5 + 4];
    n_qproj<<<dim3(16, 1536), 256, 0, stream>>>(x, wq, q);
    n_attn<<<dim3(32, 1536), 128, 0, stream>>>(q, k, v, o);
    n_oproj<<<dim3(2, 1536), 256, 0, stream>>>(o, wo, (u16*)d_out, br);
  }
}
```

```cpp
#include <hip/hip_runtime.h>
#include <hip/hip_bf16.h>

__global__ __launch_bounds__(256, 1) void attn_kernel(const char* __restrict__ x0, const char* __restrict__ k0, const char* __restrict__ v0, const char* __restrict__ wq0,
                                                      const char* __restrict__ x1, const char* __restrict__ k1, const char* __restrict__ v1, const char* __restrict__ wq1,
                                                      char* ws, const char* __restrict__ wo0, const char* __restrict__ wo1, char* __restrict__ out) {
  extern __shared__ __attribute__((aligned(16))) char lds[];
  const unsigned wid = __builtin_amdgcn_readfirstlane(threadIdx.x >> 6);
  const unsigned bid = blockIdx.x, xcd = bid & 7u, idx = bid >> 3;
  asm volatile("s_getpc_b64 s[66:67]\n\tv_mbcnt_lo_u32_b32 v235, -1, 0\n\tv_and_b32 v235, 3, v235\n\tv_lshlrev_b32 v235, 7, v235\n\tv_add_u32 v235, %0, v235\n\t"
               "v_min_u32 v235, 42880, v235\n\tglobal_load_dword v240, v235, s[66:67]\n\t" :: "s"((idx * 4u + wid) << 9) : "memory", "s66", "s67", "v235", "v240");
  __builtin_amdgcn_sched_barrier(0);
  const unsigned br = xcd >> 2, pair = 4u * (xcd & 3u) + (idx & 3u), b = idx >> 2;
  const unsigned head = 2u * pair + (wid >> 1);
  char* Ows = ws + 65536;
  const char* kvs = (wid & 1u) ? (br ? v1 : v0) : (br ? k1 : k0);
  const char* kvb = kvs + (size_t)(b * 32u + head) * 262144u - 1024;
  const char* xb = (br ? x1 : x0) + (size_t)b * 196608u;
  const char* wb = (br ? wq1 : wq0) + pair * 512u - 3072;
  char* ob = Ows + ((size_t)(((br * 8u + b) * 2u + (wid & 1u)) * 32u + head)) * 24576u;
  const unsigned ldsb = (unsigned)(size_t)lds;
  const char* wo = br ? wo1 : wo0;
  const unsigned woff = (((xcd & 3u) * 32u + idx) * 4u + wid) * 8192u;
  const unsigned rt = 4u * (xcd & 3u) + (idx >> 3), ct = idx & 7u;
  const char* ol = Ows + (size_t)(br * 16u + rt) * 786432u + (size_t)wid * 196608u;
  const char* wl = wo + (size_t)wid * 1048576u + ct * 128u;
  char* op = out + (size_t)rt * 96u * 2048u + br * 1024u + ct * 128u;
  char* myflag = ws + bid * 128u;
  const char* myacks = ws + 32768u + bid * 64u;
  const unsigned p0 = (br * 4u + wid) + 32u * (rt >> 1);
  const char* pf = ws + p0 * 128u;
  char* ackdst = ws + 32768u + p0 * 64u + ((rt & 1u) * 8u + ct) * 4u;
  const char* khead = (br ? k1 : k0) + (size_t)(b * 32u + head) * 262144u;
  const unsigned krot = 8u * (idx >> 3);
  asm volatile(
".p2align 3\n\t"
"s_mov_b64 s[44:45], %2\n\t"
"s_mov_b64 s[40:41], %1\n\t"
"s_mov_b32 s52, %4\n\t"
"s_mov_b32 s53, %5\n\t"
"s_mov_b32 s46, 4189696\n\t"
"s_mov_b32 s83, %8\n\t"
"s_and_b32 s41, s41, 0xffff\n\t"
"s_and_b32 s45, s45, 0xffff\n\t"
"s_mov_b32 s42, 196608\n\t"
"s_mov_b32 s43, 0x20000\n\t"
"s_mov_b32 s47, 0x20000\n\t"
"v_mbcnt_lo_u32_b32 v252, -1, 0\n\t"
"v_mbcnt_hi_u32_b32 v252, -1, v252\n\t"
"v_bfe_u32 v254, v252, 2, 3\n\t"
"v_lshrrev_b32_e32 v253, 5, v252\n\t"
"v_and_b32_e32 v255, 3, v252\n\t"
"v_lshlrev_b32_e32 v253, 6, v253\n\t"
"v_lshl_add_u32 v241, v255, 4, v253\n\t"
"v_lshl_add_u32 v249, v254, 13, v241\n\t"
"v_add_u32_e32 v246, 3072, v249\n\t"
"v_add_u32_e32 v247, 2176, v249\n\t"
"v_add_u32_e32 v248, 1280, v249\n\t"
"v_add_u32_e32 v249, 384, v249\n\t"
"s_lshr_b32 s71, s83, 2\n\t"
"s_and_b32 s72, s83, 3\n\t"
"s_lshl_b32 s72, s72, 1\n\t"
"s_add_u32 s71, s71, s72\n\t"
"s_sub_u32 s72, s71, 7\n\t"
"s_cselect_b32 s71, s71, s72\n\t"
"s_add_u32 s80, s71, 1\n\t"
"s_mov_b32 s71, 0\n\t"
"s_lshl_b32 s73, s71, 7\n\t"
"s_lshl_b32 s74, s71, 19\n\t"
"s_lshl_b32 s72, s52, 16\n\t"
"s_add_u32 s74, s74, s72\n\t"
"s_lshl_b32 s76, s52, 12\n\t"
"s_add_u32 s76, s76, s53\n\t"
"s_add_u32 m0, s76, 65536\n\t"
"s_nop 0\n\t"
"buffer_load_dwordx4 v246, s[44:47], s74 offen offset:0 lds\n\t"
"buffer_load_dwordx4 v247, s[44:47], s74 offen offset:1024 lds\n\t"
"buffer_load_dwordx4 v248, s[44:47], s74 offen offset:2048 lds\n\t"
"buffer_load_dwordx4 v249, s[44:47], s74 offen offset:3072 lds\n\t"
"s_add_u32 s74, s74, 262144\n\t"
"s_mul_i32 s71, s52, 49152\n\t"
"v_lshl_add_u32 v242, v254, 10, v241\n\t"
"v_add_u32_e32 v242, s71, v242\n\t"
"v_xor_b32_e64 v243, 32, v242\n\t"
"v_add_u32_e32 v244, 14336, v242\n\t"
"v_add_u32_e32 v245, 21504, v243\n\t"
"v_add_u32_e32 v243, 7168, v243\n\t"
"s_mul_i32 s75, s52, 6144\n\t"
"s_add_u32 s75, s75, s53\n\t"
"s_add_u32 m0, s75, 114688\n\t"
"s_add_u32 s77, s73, 0x8000\n\t"
"buffer_load_dwordx4 v242, s[40:43], s73 offen offset:0 lds\n\t"
"buffer_load_dwordx4 v243, s[40:43], s73 offen offset:1024 lds\n\t"
"buffer_load_dwordx4 v244, s[40:43], s73 offen offset:2048 lds\n\t"
"buffer_load_dwordx4 v245, s[40:43], s73 offen offset:3072 lds\n\t"
"s_add_u32 m0, s75, 118784\n\t"
"s_add_u32 s73, s73, 0x80\n\t"
"buffer_load_dwordx4 v242, s[40:43], s77 offen offset:0 lds\n\t"
"buffer_load_dwordx4 v243, s[40:43], s77 offen offset:1024 lds\n\t"
"s_lshl_b32 s73, s80, 7\n\t"
"s_add_u32 m0, s76, 32768\n\t"
"s_nop 0\n\t"
"buffer_load_dwordx4 v246, s[44:47], s74 offen offset:0 lds\n\t"
"buffer_load_dwordx4 v247, s[44:47], s74 offen offset:1024 lds\n\t"
"buffer_load_dwordx4 v248, s[44:47], s74 offen offset:2048 lds\n\t"
"buffer_load_dwordx4 v249, s[44:47], s74 offen offset:3072 lds\n\t"
"s_lshl_b32 s74, s80, 19\n\t"
"s_lshl_b32 s69, s52, 16\n\t"
"s_add_u32 s74, s74, s69\n\t"
"s_mov_b64 s[36:37], %0\n\t"
"s_mov_b64 s[48:49], %3\n\t"
"s_mov_b64 s[78:79], %7\n\t"
"s_and_b32 s37, s37, 0xffff\n\t"
"s_and_b32 s49, s49, 0xffff\n\t"
"s_mov_b32 s38, 263168\n\t"
"s_mov_b32 s50, 24576\n\t"
"s_mov_b32 s39, 0x20000\n\t"
"s_mov_b32 s51, 0x20000\n\t"
"s_lshr_b32 s81, s52, 1\n\t"
"s_and_b32 s82, s52, 1\n\t"
"v_lshl_add_u32 v223, v254, 8, v241\n\t"
"v_lshlrev_b32_e32 v253, 1, v252\n\t"
"v_and_b32_e32 v253, 32, v253\n\t"
"s_mul_i32 s71, s82, -1\n\t"
"v_and_b32_e32 v253, s71, v253\n\t"
"v_xor_b32_e32 v223, v223, v253\n\t"
"s_lshl_b32 s71, s82, 5\n\t"
"s_xor_b32 s71, s71, 32\n\t"
"v_xor_b32_e64 v225, s71, v223\n\t"
"v_add_u32_e32 v224, 128, v223\n\t"
"v_add_u32_e32 v226, 128, v225\n\t"
"v_add_u32_e32 v223, 1024, v223\n\t"
"v_add_u32_e32 v225, 1024, v225\n\t"
"s_mul_i32 s58, s82, 81920\n\t"
"s_lshl_b32 s72, s81, 13\n\t"
"s_add_u32 s72, s72, s53\n\t"
"s_add_u32 s58, s58, s72\n\t"
"s_add_u32 s57, s58, 65536\n\t"
"s_mov_b32 s56, s58\n\t"
"s_mov_b32 s54, 0\n\t"
"s_movk_i32 s55, 0x1000\n\t"
"v_bfe_u32 v253, v252, 3, 1\n\t"
"v_lshrrev_b32_e32 v254, 4, v252\n\t"
"v_lshlrev_b32_e32 v241, 1, v253\n\t"
"v_xor_b32_e32 v241, v241, v254\n\t"
"v_lshlrev_b32_e32 v241, 4, v241\n\t"
"v_and_b32_e64 v220, 7, v252\n\t"
"v_lshl_add_u32 v241, v220, 6, v241\n\t"
"v_lshl_add_u32 v220, v253, 11, v241\n\t"
"v_add_u32_e64 v220, s72, v220\n\t"
"v_lshl_add_u32 v250, v253, 10, v241\n\t"
"s_mul_i32 s71, s82, 12288\n\t"
"s_add_u32 s71, s71, s53\n\t"
"s_add_u32 s71, s71, 114688\n\t"
"v_add_u32_e32 v250, s71, v250\n\t"
"v_bfe_u32 v253, v252, 2, 2\n\t"
"v_lshlrev_b32_e64 v253, 6, v253\n\t"
"v_lshl_add_u32 v251, v255, 4, v253\n\t"
"v_lshl_add_u32 v251, v254, 12, v251\n\t"
"s_lshl_b32 s71, s81, 11\n\t"
"s_add_u32 s71, s71, s53\n\t"
"s_add_u32 s71, s71, 32768\n\t"
"v_add_u32_e64 v251, s71, v251\n\t"
"v_lshl_add_u32 v221, v255, 3, v253\n\t"
"v_lshrrev_b32_e64 v241, 5, v252\n\t"
"v_lshl_add_u32 v221, v241, 11, v221\n\t"
"v_and_b32_e64 v241, 1, v254\n\t"
"v_mul_u32_u24_e32 v241, 288, v241\n\t"
"v_add_u32_e32 v221, v221, v241\n\t"
"v_xor_b32_e32 v222, 32, v221\n\t"
"s_add_u32 s72, s72, 81920\n\t"
"v_add_u32_e32 v221, s72, v221\n\t"
"v_add_u32_e32 v222, s72, v222\n\t"
"v_mov_b32_e32 v216, 0\n\t"
"v_mov_b32_e32 v217, 0\n\t"
"v_mov_b32_e32 v218, 0\n\t"
"v_mov_b32_e32 v219, 0\n\t"
"v_mov_b32_e32 v227, 0\n\t"
"v_mov_b32_e32 v228, 0\n\t"
"v_mov_b32_e32 v229, 0\n\t"
"v_mov_b32_e32 v230, 0\n\t"
"v_mov_b32_e32 v231, 0\n\t"
"v_mov_b32_e32 v232, 0\n\t"
"v_mov_b32_e32 v208, 0\n\t"
"v_mov_b32_e32 v209, 0\n\t"
"v_mov_b32_e32 v210, 0\n\t"
"v_mov_b32_e32 v211, 0\n\t"
"v_mov_b32_e32 v212, 0\n\t"
"v_mov_b32_e32 v213, 0\n\t"
"v_mov_b32_e32 v214, 0\n\t"
"v_mov_b32_e32 v215, 0\n\t"
"v_mfma_f32_32x32x16_bf16 v[192:207], v[216:219], v[216:219], 0\n\t"
"v_mfma_f32_32x32x16_bf16 a[112:127], v[216:219], v[216:219], 0\n\t"
"v_mfma_f32_32x32x16_bf16 a[128:143], v[216:219], v[216:219], 0\n\t"
"v_mfma_f32_32x32x16_bf16 a[144:159], v[216:219], v[216:219], 0\n\t"
"v_mfma_f32_32x32x16_bf16 a[160:175], v[216:219], v[216:219], 0\n\t"
"v_mfma_f32_32x32x16_bf16 a[176:191], v[216:219], v[216:219], 0\n\t"
"v_mfma_f32_32x32x16_bf16 a[224:239], v[216:219], v[216:219], 0\n\t"
"v_mfma_f32_32x32x16_bf16 a[240:255], v[216:219], v[216:219], 0\n\t"
"s_mov_b32 s64, 0x3e0293ee\n\t"
"s_mov_b32 s65, 0x3e0293ee\n\t"
"s_mov_b32 s60, 0xff800000\n\t"
"s_mov_b32 s61, 0xff800000\n\t"
"s_mov_b32 s59, 0\n\t"
"s_mov_b32 s68, 0\n\t"
"s_mov_b32 s70, 0\n\t"
"s_add_u32 m0, s76, 49152\n\t"
"s_nop 0\n\t"
"buffer_load_dwordx4 v246, s[44:47], s74 offen offset:0 lds\n\t"
"buffer_load_dwordx4 v247, s[44:47], s74 offen offset:1024 lds\n\t"
"buffer_load_dwordx4 v248, s[44:47], s74 offen offset:2048 lds\n\t"
"buffer_load_dwordx4 v249, s[44:47], s74 offen offset:3072 lds\n\t"
"s_add_u32 s74, s74, 262144\n\t"
"s_add_u32 m0, s75, 139264\n\t"
"s_add_u32 s77, s73, 0x8000\n\t"
"buffer_load_dwordx4 v242, s[40:43], s73 offen offset:0 lds\n\t"
"buffer_load_dwordx4 v243, s[40:43], s73 offen offset:1024 lds\n\t"
"buffer_load_dwordx4 v244, s[40:43], s73 offen offset:2048 lds\n\t"
"buffer_load_dwordx4 v245, s[40:43], s73 offen offset:3072 lds\n\t"
"s_add_u32 m0, s75, 143360\n\t"
"s_add_u32 s73, s73, 0x80\n\t"
"buffer_load_dwordx4 v242, s[40:43], s77 offen offset:0 lds\n\t"
"buffer_load_dwordx4 v243, s[40:43], s77 offen offset:1024 lds\n\t"
"s_cmp_eq_u32 s73, 0x400\n\t"
"s_cselect_b32 s73, 0x80, s73\n\t"
"s_waitcnt vmcnt(14)\n\t"
"s_barrier\n\t"
"ds_read_b128 a[32:35], v250 offset:0\n\t"
"ds_read_b64_tr_b16 a[0:1], v251 offset:32768\n\t"
"ds_read_b64_tr_b16 a[2:3], v251 offset:33024\n\t"
"ds_read_b64_tr_b16 a[4:5], v251 offset:32776\n\t"
"ds_read_b64_tr_b16 a[6:7], v251 offset:33032\n\t"
"ds_read_b64_tr_b16 a[8:9], v251 offset:33280\n\t"
"ds_read_b64_tr_b16 a[10:11], v251 offset:33536\n\t"
"ds_read_b64_tr_b16 a[12:13], v251 offset:33288\n\t"
"ds_read_b64_tr_b16 a[14:15], v251 offset:33544\n\t"
"ds_read_b64_tr_b16 a[16:17], v251 offset:33792\n\t"
"ds_read_b64_tr_b16 a[18:19], v251 offset:34048\n\t"
"ds_read_b64_tr_b16 a[20:21], v251 offset:33800\n\t"
"ds_read_b64_tr_b16 a[22:23], v251 offset:34056\n\t"
"ds_read_b64_tr_b16 a[24:25], v251 offset:34304\n\t"
"ds_read_b64_tr_b16 a[26:27], v251 offset:34560\n\t"
"ds_read_b64_tr_b16 a[28:29], v251 offset:34312\n\t"
"ds_read_b64_tr_b16 a[30:31], v251 offset:34568\n\t"
"ds_read_b128 a[36:39], v250 offset:2048\n\t"
"ds_read_b128 a[40:43], v250 offset:4096\n\t"
"ds_read_b128 a[44:47], v250 offset:6144\n\t"
"ds_read_b128 a[48:51], v250 offset:8192\n\t"
"ds_read_b128 a[52:55], v250 offset:10240\n\t"
".p2align 3\n\t"
"s_waitcnt vmcnt(10) lgkmcnt(0)\n\t"
"s_barrier\n\t"
"v_mfma_f32_16x16x32_bf16 v[0:3], a[0:3], a[32:35], 0\n\t"
"s_add_u32 m0, s76, 65536\n\t"
"ds_read_b128 a[88:91], v250 offset:512\n\t"
"v_mfma_f32_16x16x32_bf16 v[4:7], a[4:7], a[32:35], 0\n\t"
"ds_read_b64_tr_b16 a[56:57], v251 offset:0\n\t"
"v_mfma_f32_16x16x32_bf16 v[48:51], a[8:11], a[32:35], 0\n\t"
"ds_read_b64_tr_b16 a[58:59], v251 offset:256\n\t"
"v_mfma_f32_16x16x32_bf16 v[52:55], a[12:15], a[32:35], 0\n\t"
"v_mfma_f32_16x16x32_bf16 v[96:99], a[16:19], a[32:35], 0\n\t"
"buffer_load_dwordx4 v246, s[44:47], s74 offen offset:0 lds\n\t"
"v_mfma_f32_16x16x32_bf16 v[100:103], a[20:23], a[32:35], 0\n\t"
"ds_read_b64_tr_b16 a[60:61], v251 offset:8\n\t"
"v_mfma_f32_16x16x32_bf16 v[144:147], a[24:27], a[32:35], 0\n\t"
"ds_read_b64_tr_b16 a[62:63], v251 offset:264\n\t"
"v_mfma_f32_16x16x32_bf16 v[148:151], a[28:31], a[32:35], 0\n\t"
"ds_read_b64_tr_b16 a[64:65], v251 offset:512\n\t"
"v_mfma_f32_16x16x32_bf16 v[8:11], a[0:3], a[36:39], 0\n\t"
"ds_read_b64_tr_b16 a[66:67], v251 offset:768\n\t"
"v_mfma_f32_16x16x32_bf16 v[12:15], a[4:7], a[36:39], 0\n\t"
"buffer_load_dwordx4 v247, s[44:47], s74 offen offset:1024 lds\n\t"
"v_mfma_f32_16x16x32_bf16 v[56:59], a[8:11], a[36:39], 0\n\t"
"ds_read_b64_tr_b16 a[68:69], v251 offset:520\n\t"
"v_mfma_f32_16x16x32_bf16 v[60:63], a[12:15], a[36:39], 0\n\t"
"ds_read_b64_tr_b16 a[70:71], v251 offset:776\n\t"
"v_mfma_f32_16x16x32_bf16 v[104:107], a[16:19], a[36:39], 0\n\t"
"ds_read_b64_tr_b16 a[72:73], v251 offset:1024\n\t"
"v_mfma_f32_16x16x32_bf16 v[108:111], a[20:23], a[36:39], 0\n\t"
"buffer_load_dwordx4 v248, s[44:47], s74 offen offset:2048 lds\n\t"
"v_mfma_f32_16x16x32_bf16 v[152:155], a[24:27], a[36:39], 0\n\t"
"ds_read_b64_tr_b16 a[74:75], v251 offset:1280\n\t"
"v_mfma_f32_16x16x32_bf16 v[156:159], a[28:31], a[36:39], 0\n\t"
"ds_read_b64_tr_b16 a[76:77], v251 offset:1032\n\t"
"v_mfma_f32_16x16x32_bf16 v[16:19], a[0:3], a[40:43], 0\n\t"
"ds_read_b64_tr_b16 a[78:79], v251 offset:1288\n\t"
"v_mfma_f32_16x16x32_bf16 v[20:23], a[4:7], a[40:43], 0\n\t"
"v_mfma_f32_16x16x32_bf16 v[64:67], a[8:11], a[40:43], 0\n\t"
"buffer_load_dwordx4 v249, s[44:47], s74 offen offset:3072 lds\n\t"
"v_mfma_f32_16x16x32_bf16 v[68:71], a[12:15], a[40:43], 0\n\t"
"ds_read_b64_tr_b16 a[80:81], v251 offset:1536\n\t"
"v_mfma_f32_16x16x32_bf16 v[112:115], a[16:19], a[40:43], 0\n\t"
"ds_read_b64_tr_b16 a[82:83], v251 offset:1792\n\t"
"v_mfma_f32_16x16x32_bf16 v[116:119], a[20:23], a[40:43], 0\n\t"
"ds_read_b64_tr_b16 a[84:85], v251 offset:1544\n\t"
"v_mfma_f32_16x16x32_bf16 v[160:163], a[24:27], a[40:43], 0\n\t"
"s_add_u32 s74, s74, 262144\n\t"
"ds_read_b64_tr_b16 a[86:87], v251 offset:1800\n\t"
"v_mfma_f32_16x16x32_bf16 v[164:167], a[28:31], a[40:43], 0\n\t"
"ds_read_b128 a[92:95], v250 offset:2560\n\t"
"v_mfma_f32_16x16x32_bf16 v[24:27], a[0:3], a[44:47], 0\n\t"
"v_mfma_f32_16x16x32_bf16 v[28:31], a[4:7], a[44:47], 0\n\t"
"ds_read_b128 a[96:99], v250 offset:4608\n\t"
"v_mfma_f32_16x16x32_bf16 v[72:75], a[8:11], a[44:47], 0\n\t"
"ds_read_b128 a[100:103], v250 offset:6656\n\t"
"v_mfma_f32_16x16x32_bf16 v[76:79], a[12:15], a[44:47], 0\n\t"
"s_sub_u32 s69, s74, 3670016\n\t"
"v_mfma_f32_16x16x32_bf16 v[120:123], a[16:19], a[44:47], 0\n\t"
"ds_read_b128 a[104:107], v250 offset:8704\n\t"
"v_mfma_f32_16x16x32_bf16 v[124:127], a[20:23], a[44:47], 0\n\t"
"ds_read_b128 a[108:111], v250 offset:10752\n\t"
"v_mfma_f32_16x16x32_bf16 v[168:171], a[24:27], a[44:47], 0\n\t"
"v_mfma_f32_16x16x32_bf16 v[172:175], a[28:31], a[44:47], 0\n\t"
"s_cmp_ge_u32 s74, 0x400000\n\t"
"v_mfma_f32_16x16x32_bf16 v[32:35], a[0:3], a[48:51], 0\n\t"
"v_mfma_f32_16x16x32_bf16 v[36:39], a[4:7], a[48:51], 0\n\t"
"v_mfma_f32_16x16x32_bf16 v[80:83], a[8:11], a[48:51], 0\n\t"
"v_mfma_f32_16x16x32_bf16 v[84:87], a[12:15], a[48:51], 0\n\t"
"v_mfma_f32_16x16x32_bf16 v[128:131], a[16:19], a[48:51], 0\n\t"
"s_cselect_b32 s74, s69, s74\n\t"
"v_mfma_f32_16x16x32_bf16 v[132:135], a[20:23], a[48:51], 0\n\t"
"v_mfma_f32_16x16x32_bf16 v[176:179], a[24:27], a[48:51], 0\n\t"
"v_mfma_f32_16x16x32_bf16 v[180:183], a[28:31], a[48:51], 0\n\t"
"v_mfma_f32_16x16x32_bf16 v[40:43], a[0:3], a[52:55], 0\n\t"
"v_mfma_f32_16x16x32_bf16 v[44:47], a[4:7], a[52:55], 0\n\t"
"v_mfma_f32_16x16x32_bf16 v[88:91], a[8:11], a[52:55], 0\n\t"
"v_mfma_f32_16x16x32_bf16 v[92:95], a[12:15], a[52:55], 0\n\t"
"v_mfma_f32_16x16x32_bf16 v[136:139], a[16:19], a[52:55], 0\n\t"
"v_mfma_f32_16x16x32_bf16 v[140:143], a[20:23], a[52:55], 0\n\t"
"v_mfma_f32_16x16x32_bf16 v[184:187], a[24:27], a[52:55], 0\n\t"
"v_mfma_f32_16x16x32_bf16 v[188:191], a[28:31], a[52:55], 0\n\t"
"s_waitcnt vmcnt(4) lgkmcnt(0)\n\t"
"s_barrier\n\t"
"v_mfma_f32_16x16x32_bf16 v[0:3], a[56:59], a[88:91], v[0:3]\n\t"
"s_add_u32 m0, s76, 32768\n\t"
"ds_read_b128 a[32:35], v250 offset:24576\n\t"
"v_mfma_f32_16x16x32_bf16 v[4:7], a[60:63], a[88:91], v[4:7]\n\t"
"ds_read_b64_tr_b16 a[0:1], v251 offset:16384\n\t"
"v_mfma_f32_16x16x32_bf16 v[48:51], a[64:67], a[88:91], v[48:51]\n\t"
"buffer_load_dwordx4 v246, s[44:47], s74 offen offset:0 lds\n\t"
"v_mfma_f32_16x16x32_bf16 v[52:55], a[68:71], a[88:91], v[52:55]\n\t"
"ds_read_b64_tr_b16 a[2:3], v251 offset:16640\n\t"
"v_mfma_f32_16x16x32_bf16 v[96:99], a[72:75], a[88:91], v[96:99]\n\t"
"buffer_load_dwordx4 v247, s[44:47], s74 offen offset:1024 lds\n\t"
"v_mfma_f32_16x16x32_bf16 v[100:103], a[76:79], a[88:91], v[100:103]\n\t"
"ds_read_b64_tr_b16 a[4:5], v251 offset:16392\n\t"
"v_mfma_f32_16x16x32_bf16 v[144:147], a[80:83], a[88:91], v[144:147]\n\t"
"buffer_load_dwordx4 v248, s[44:47], s74 offen offset:2048 lds\n\t"
"v_mfma_f32_16x16x32_bf16 v[148:151], a[84:87], a[88:91], v[148:151]\n\t"
"ds_read_b64_tr_b16 a[6:7], v251 offset:16648\n\t"
"v_mfma_f32_16x16x32_bf16 v[8:11], a[56:59], a[92:95], v[8:11]\n\t"
"ds_read_b64_tr_b16 a[8:9], v251 offset:16896\n\t"
"v_mfma_f32_16x16x32_bf16 v[12:15], a[60:63], a[92:95], v[12:15]\n\t"
"buffer_load_dwordx4 v249, s[44:47], s74 offen offset:3072 lds\n\t"
"v_mfma_f32_16x16x32_bf16 v[56:59], a[64:67], a[92:95], v[56:59]\n\t"
"ds_read_b64_tr_b16 a[10:11], v251 offset:17152\n\t"
"v_mfma_f32_16x16x32_bf16 v[60:63], a[68:71], a[92:95], v[60:63]\n\t"
"s_add_u32 s74, s74, 262144\n\t"
"ds_read_b64_tr_b16 a[12:13], v251 offset:16904\n\t"
"v_mfma_f32_16x16x32_bf16 v[104:107], a[72:75], a[92:95], v[104:107]\n\t"
"ds_read_b64_tr_b16 a[14:15], v251 offset:17160\n\t"
"v_mfma_f32_16x16x32_bf16 v[108:111], a[76:79], a[92:95], v[108:111]\n\t"
"s_add_u32 m0, s75, 114688\n\t"
"ds_read_b64_tr_b16 a[16:17], v251 offset:17408\n\t"
"v_mfma_f32_16x16x32_bf16 v[152:155], a[80:83], a[92:95], v[152:155]\n\t"
"ds_read_b64_tr_b16 a[18:19], v251 offset:17664\n\t"
"v_mfma_f32_16x16x32_bf16 v[156:159], a[84:87], a[92:95], v[156:159]\n\t"
"s_add_u32 s77, s73, 0x8000\n\t"
"ds_read_b64_tr_b16 a[20:21], v251 offset:17416\n\t"
"v_mfma_f32_16x16x32_bf16 v[16:19], a[56:59], a[96:99], v[16:19]\n\t"
"ds_read_b64_tr_b16 a[22:23], v251 offset:17672\n\t"
"v_mfma_f32_16x16x32_bf16 v[20:23], a[60:63], a[96:99], v[20:23]\n\t"
"v_mfma_f32_16x16x32_bf16 v[64:67], a[64:67], a[96:99], v[64:67]\n\t"
"buffer_load_dwordx4 v242, s[40:43], s73 offen offset:0 lds\n\t"
"v_mfma_f32_16x16x32_bf16 v[68:71], a[68:71], a[96:99], v[68:71]\n\t"
"ds_read_b64_tr_b16 a[24:25], v251 offset:17920\n\t"
"v_mfma_f32_16x16x32_bf16 v[112:115], a[72:75], a[96:99], v[112:115]\n\t"
"buffer_load_dwordx4 v243, s[40:43], s73 offen offset:1024 lds\n\t"
"v_mfma_f32_16x16x32_bf16 v[116:119], a[76:79], a[96:99], v[116:119]\n\t"
"ds_read_b64_tr_b16 a[26:27], v251 offset:18176\n\t"
"v_mfma_f32_16x16x32_bf16 v[160:163], a[80:83], a[96:99], v[160:163]\n\t"
"buffer_load_dwordx4 v244, s[40:43], s73 offen offset:2048 lds\n\t"
"v_mfma_f32_16x16x32_bf16 v[164:167], a[84:87], a[96:99], v[164:167]\n\t"
"ds_read_b64_tr_b16 a[28:29], v251 offset:17928\n\t"
"v_mfma_f32_16x16x32_bf16 v[24:27], a[56:59], a[100:103], v[24:27]\n\t"
"ds_read_b64_tr_b16 a[30:31], v251 offset:18184\n\t"
"v_mfma_f32_16x16x32_bf16 v[28:31], a[60:63], a[100:103], v[28:31]\n\t"
"buffer_load_dwordx4 v245, s[40:43], s73 offen offset:3072 lds\n\t"
"v_mfma_f32_16x16x32_bf16 v[72:75], a[64:67], a[100:103], v[72:75]\n\t"
"ds_read_b128 a[36:39], v250 offset:26624\n\t"
"v_mfma_f32_16x16x32_bf16 v[76:79], a[68:71], a[100:103], v[76:79]\n\t"
"s_add_u32 m0, s75, 118784\n\t"
"ds_read_b128 a[40:43], v250 offset:28672\n\t"
"v_mfma_f32_16x16x32_bf16 v[120:123], a[72:75], a[100:103], v[120:123]\n\t"
"ds_read_b128 a[44:47], v250 offset:30720\n\t"
"v_mfma_f32_16x16x32_bf16 v[124:127], a[76:79], a[100:103], v[124:127]\n\t"
"s_add_u32 s73, s73, 0x80\n\t"
"ds_read_b128 a[48:51], v250 offset:32768\n\t"
"v_mfma_f32_16x16x32_bf16 v[168:171], a[80:83], a[100:103], v[168:171]\n\t"
"ds_read_b128 a[52:55], v250 offset:34816\n\t"
"v_mfma_f32_16x16x32_bf16 v[172:175], a[84:87], a[100:103], v[172:175]\n\t"
"buffer_load_dwordx4 v242, s[40:43], s77 offen offset:0 lds\n\t"
"v_mfma_f32_16x16x32_bf16 v[32:35], a[56:59], a[104:107], v[32:35]\n\t"
"v_mfma_f32_16x16x32_bf16 v[36:39], a[60:63], a[104:107], v[36:39]\n\t"
"v_mfma_f32_16x16x32_bf16 v[80:83], a[64:67], a[104:107], v[80:83]\n\t"
"buffer_load_dwordx4 v243, s[40:43], s77 offen offset:1024 lds\n\t"
"v_mfma_f32_16x16x32_bf16 v[84:87], a[68:71], a[104:107], v[84:87]\n\t"
"v_mfma_f32_16x16x32_bf16 v[128:131], a[72:75], a[104:107], v[128:131]\n\t"
"s_cmp_eq_u32 s73, 0x400\n\t"
"v_mfma_f32_16x16x32_bf16 v[132:135], a[76:79], a[104:107], v[132:135]\n\t"
"v_mfma_f32_16x16x32_bf16 v[176:179], a[80:83], a[104:107], v[176:179]\n\t"
"s_cselect_b32 s73, 0x80, s73\n\t"
"v_mfma_f32_16x16x32_bf16 v[180:183], a[84:87], a[104:107], v[180:183]\n\t"
"v_mfma_f32_16x16x32_bf16 v[40:43], a[56:59], a[108:111], v[40:43]\n\t"
"v_mfma_f32_16x16x32_bf16 v[44:47], a[60:63], a[108:111], v[44:47]\n\t"
"v_mfma_f32_16x16x32_bf16 v[88:91], a[64:67], a[108:111], v[88:91]\n\t"
"v_mfma_f32_16x16x32_bf16 v[92:95], a[68:71], a[108:111], v[92:95]\n\t"
"v_mfma_f32_16x16x32_bf16 v[136:139], a[72:75], a[108:111], v[136:139]\n\t"
"v_mfma_f32_16x16x32_bf16 v[140:143], a[76:79], a[108:111], v[140:143]\n\t"
"v_mfma_f32_16x16x32_bf16 v[184:187], a[80:83], a[108:111], v[184:187]\n\t"
"v_mfma_f32_16x16x32_bf16 v[188:191], a[84:87], a[108:111], v[188:191]\n\t"
"s_waitcnt vmcnt(10) lgkmcnt(0)\n\t"
"s_barrier\n\t"
"v_mfma_f32_16x16x32_bf16 v[0:3], a[0:3], a[32:35], v[0:3]\n\t"
"s_add_u32 m0, s76, 49152\n\t"
"ds_read_b128 a[88:91], v250 offset:25088\n\t"
"v_mfma_f32_16x16x32_bf16 v[4:7], a[4:7], a[32:35], v[4:7]\n\t"
"ds_read_b64_tr_b16 a[56:57], v251 offset:32768\n\t"
"v_mfma_f32_16x16x32_bf16 v[48:51], a[8:11], a[32:35], v[48:51]\n\t"
"ds_read_b64_tr_b16 a[58:59], v251 offset:33024\n\t"
"v_mfma_f32_16x16x32_bf16 v[52:55], a[12:15], a[32:35], v[52:55]\n\t"
"v_mfma_f32_16x16x32_bf16 v[96:99], a[16:19], a[32:35], v[96:99]\n\t"
"buffer_load_dwordx4 v246, s[44:47], s74 offen offset:0 lds\n\t"
"v_mfma_f32_16x16x32_bf16 v[100:103], a[20:23], a[32:35], v[100:103]\n\t"
"ds_read_b64_tr_b16 a[60:61], v251 offset:32776\n\t"
"v_mfma_f32_16x16x32_bf16 v[144:147], a[24:27], a[32:35], v[144:147]\n\t"
"ds_read_b64_tr_b16 a[62:63], v251 offset:33032\n\t"
"v_mfma_f32_16x16x32_bf16 v[148:151], a[28:31], a[32:35], v[148:151]\n\t"
"ds_read_b64_tr_b16 a[64:65], v251 offset:33280\n\t"
"v_mfma_f32_16x16x32_bf16 v[8:11], a[0:3], a[36:39], v[8:11]\n\t"
"ds_read_b64_tr_b16 a[66:67], v251 offset:33536\n\t"
"v_mfma_f32_16x16x32_bf16 v[12:15], a[4:7], a[36:39], v[12:15]\n\t"
"buffer_load_dwordx4 v247, s[44:47], s74 offen offset:1024 lds\n\t"
"v_mfma_f32_16x16x32_bf16 v[56:59], a[8:11], a[36:39], v[56:59]\n\t"
"ds_read_b64_tr_b16 a[68:69], v251 offset:33288\n\t"
"v_mfma_f32_16x16x32_bf16 v[60:63], a[12:15], a[36:39], v[60:63]\n\t"
"ds_read_b64_tr_b16 a[70:71], v251 offset:33544\n\t"
"v_mfma_f32_16x16x32_bf16 v[104:107], a[16:19], a[36:39], v[104:107]\n\t"
"ds_read_b64_tr_b16 a[72:73], v251 offset:33792\n\t"
"v_mfma_f32_16x16x32_bf16 v[108:111], a[20:23], a[36:39], v[108:111]\n\t"
"buffer_load_dwordx4 v248, s[44:47], s74 offen offset:2048 lds\n\t"
"v_mfma_f32_16x16x32_bf16 v[152:155], a[24:27], a[36:39], v[152:155]\n\t"
"ds_read_b64_tr_b16 a[74:75], v251 offset:34048\n\t"
"v_mfma_f32_16x16x32_bf16 v[156:159], a[28:31], a[36:39], v[156:159]\n\t"
"ds_read_b64_tr_b16 a[76:77], v251 offset:33800\n\t"
"v_mfma_f32_16x16x32_bf16 v[16:19], a[0:3], a[40:43], v[16:19]\n\t"
"ds_read_b64_tr_b16 a[78:79], v251 offset:34056\n\t"
"v_mfma_f32_16x16x32_bf16 v[20:23], a[4:7], a[40:43], v[20:23]\n\t"
"v_mfma_f32_16x16x32_bf16 v[64:67], a[8:11], a[40:43], v[64:67]\n\t"
"buffer_load_dwordx4 v249, s[44:47], s74 offen offset:3072 lds\n\t"
"v_mfma_f32_16x16x32_bf16 v[68:71], a[12:15], a[40:43], v[68:71]\n\t"
"ds_read_b64_tr_b16 a[80:81], v251 offset:34304\n\t"
"v_mfma_f32_16x16x32_bf16 v[112:115], a[16:19], a[40:43], v[112:115]\n\t"
"ds_read_b64_tr_b16 a[82:83], v251 offset:34560\n\t"
"v_mfma_f32_16x16x32_bf16 v[116:119], a[20:23], a[40:43], v[116:119]\n\t"
"ds_read_b64_tr_b16 a[84:85], v251 offset:34312\n\t"
"v_mfma_f32_16x16x32_bf16 v[160:163], a[24:27], a[40:43], v[160:163]\n\t"
"s_add_u32 s74, s74, 262144\n\t"
"ds_read_b64_tr_b16 a[86:87], v251 offset:34568\n\t"
"v_mfma_f32_16x16x32_bf16 v[164:167], a[28:31], a[40:43], v[164:167]\n\t"
"ds_read_b128 a[92:95], v250 offset:27136\n\t"
"v_mfma_f32_16x16x32_bf16 v[24:27], a[0:3], a[44:47], v[24:27]\n\t"
"v_mfma_f32_16x16x32_bf16 v[28:31], a[4:7], a[44:47], v[28:31]\n\t"
"ds_read_b128 a[96:99], v250 offset:29184\n\t"
"v_mfma_f32_16x16x32_bf16 v[72:75], a[8:11], a[44:47], v[72:75]\n\t"
"ds_read_b128 a[100:103], v250 offset:31232\n\t"
"v_mfma_f32_16x16x32_bf16 v[76:79], a[12:15], a[44:47], v[76:79]\n\t"
"s_sub_u32 s69, s74, 3670016\n\t"
"v_mfma_f32_16x16x32_bf16 v[120:123], a[16:19], a[44:47], v[120:123]\n\t"
"ds_read_b128 a[104:107], v250 offset:33280\n\t"
"v_mfma_f32_16x16x32_bf16 v[124:127], a[20:23], a[44:47], v[124:127]\n\t"
"ds_read_b128 a[108:111], v250 offset:35328\n\t"
"v_mfma_f32_16x16x32_bf16 v[168:171], a[24:27], a[44:47], v[168:171]\n\t"
"v_mfma_f32_16x16x32_bf16 v[172:175], a[28:31], a[44:47], v[172:175]\n\t"
"s_cmp_ge_u32 s74, 0x400000\n\t"
"v_mfma_f32_16x16x32_bf16 v[32:35], a[0:3], a[48:51], v[32:35]\n\t"
"v_mfma_f32_16x16x32_bf16 v[36:39], a[4:7], a[48:51], v[36:39]\n\t"
"v_mfma_f32_16x16x32_bf16 v[80:83], a[8:11], a[48:51], v[80:83]\n\t"
"v_mfma_f32_16x16x32_bf16 v[84:87], a[12:15], a[48:51], v[84:87]\n\t"
"v_mfma_f32_16x16x32_bf16 v[128:131], a[16:19], a[48:51], v[128:131]\n\t"
"s_cselect_b32 s74, s69, s74\n\t"
"v_mfma_f32_16x16x32_bf16 v[132:135], a[20:23], a[48:51], v[132:135]\n\t"
"v_mfma_f32_16x16x32_bf16 v[176:179], a[24:27], a[48:51], v[176:179]\n\t"
"v_mfma_f32_16x16x32_bf16 v[180:183], a[28:31], a[48:51], v[180:183]\n\t"
"v_mfma_f32_16x16x32_bf16 v[40:43], a[0:3], a[52:55], v[40:43]\n\t"
"v_mfma_f32_16x16x32_bf16 v[44:47], a[4:7], a[52:55], v[44:47]\n\t"
"v_mfma_f32_16x16x32_bf16 v[88:91], a[8:11], a[52:55], v[88:91]\n\t"
"v_mfma_f32_16x16x32_bf16 v[92:95], a[12:15], a[52:55], v[92:95]\n\t"
"v_mfma_f32_16x16x32_bf16 v[136:139], a[16:19], a[52:55], v[136:139]\n\t"
"v_mfma_f32_16x16x32_bf16 v[140:143], a[20:23], a[52:55], v[140:143]\n\t"
"v_mfma_f32_16x16x32_bf16 v[184:187], a[24:27], a[52:55], v[184:187]\n\t"
"v_mfma_f32_16x16x32_bf16 v[188:191], a[28:31], a[52:55], v[188:191]\n\t"
"s_waitcnt vmcnt(4) lgkmcnt(0)\n\t"
"s_barrier\n\t"
"v_mfma_f32_16x16x32_bf16 v[0:3], a[56:59], a[88:91], v[0:3]\n\t"
"s_add_u32 m0, s76, 65536\n\t"
"ds_read_b128 a[32:35], v250 offset:0\n\t"
"v_mfma_f32_16x16x32_bf16 v[4:7], a[60:63], a[88:91], v[4:7]\n\t"
"ds_read_b64_tr_b16 a[0:1], v251 offset:0\n\t"
"v_mfma_f32_16x16x32_bf16 v[48:51], a[64:67], a[88:91], v[48:51]\n\t"
"buffer_load_dwordx4 v246, s[44:47], s74 offen offset:0 lds\n\t"
"v_mfma_f32_16x16x32_bf16 v[52:55], a[68:71], a[88:91], v[52:55]\n\t"
"ds_read_b64_tr_b16 a[2:3], v251 offset:256\n\t"
"v_mfma_f32_16x16x32_bf16 v[96:99], a[72:75], a[88:91], v[96:99]\n\t"
"buffer_load_dwordx4 v247, s[44:47], s74 offen offset:1024 lds\n\t"
"v_mfma_f32_16x16x32_bf16 v[100:103], a[76:79], a[88:91], v[100:103]\n\t"
"ds_read_b64_tr_b16 a[4:5], v251 offset:8\n\t"
"v_mfma_f32_16x16x32_bf16 v[144:147], a[80:83], a[88:91], v[144:147]\n\t"
"buffer_load_dwordx4 v248, s[44:47], s74 offen offset:2048 lds\n\t"
"v_mfma_f32_16x16x32_bf16 v[148:151], a[84:87], a[88:91], v[148:151]\n\t"
"ds_read_b64_tr_b16 a[6:7], v251 offset:264\n\t"
"v_mfma_f32_16x16x32_bf16 v[8:11], a[56:59], a[92:95], v[8:11]\n\t"
"ds_read_b64_tr_b16 a[8:9], v251 offset:512\n\t"
"v_mfma_f32_16x16x32_bf16 v[12:15], a[60:63], a[92:95], v[12:15]\n\t"
"buffer_load_dwordx4 v249, s[44:47], s74 offen offset:3072 lds\n\t"
"v_mfma_f32_16x16x32_bf16 v[56:59], a[64:67], a[92:95], v[56:59]\n\t"
"ds_read_b64_tr_b16 a[10:11], v251 offset:768\n\t"
"v_mfma_f32_16x16x32_bf16 v[60:63], a[68:71], a[92:95], v[60:63]\n\t"
"s_add_u32 s74, s74, 262144\n\t"
"ds_read_b64_tr_b16 a[12:13], v251 offset:520\n\t"
"v_mfma_f32_16x16x32_bf16 v[104:107], a[72:75], a[92:95], v[104:107]\n\t"
"ds_read_b64_tr_b16 a[14:15], v251 offset:776\n\t"
"v_mfma_f32_16x16x32_bf16 v[108:111], a[76:79], a[92:95], v[108:111]\n\t"
"s_add_u32 m0, s75, 139264\n\t"
"ds_read_b64_tr_b16 a[16:17], v251 offset:1024\n\t"
"v_mfma_f32_16x16x32_bf16 v[152:155], a[80:83], a[92:95], v[152:155]\n\t"
"ds_read_b64_tr_b16 a[18:19], v251 offset:1280\n\t"
"v_mfma_f32_16x16x32_bf16 v[156:159], a[84:87], a[92:95], v[156:159]\n\t"
"s_add_u32 s77, s73, 0x8000\n\t"
"ds_read_b64_tr_b16 a[20:21], v251 offset:1032\n\t"
"v_mfma_f32_16x16x32_bf16 v[16:19], a[56:59], a[96:99], v[16:19]\n\t"
"ds_read_b64_tr_b16 a[22:23], v251 offset:1288\n\t"
"v_mfma_f32_16x16x32_bf16 v[20:23], a[60:63], a[96:99], v[20:23]\n\t"
"v_mfma_f32_16x16x32_bf16 v[64:67], a[64:67], a[96:99], v[64:67]\n\t"
"buffer_load_dwordx4 v242, s[40:43], s73 offen offset:0 lds\n\t"
"v_mfma_f32_16x16x32_bf16 v[68:71], a[68:71], a[96:99], v[68:71]\n\t"
"ds_read_b64_tr_b16 a[24:25], v251 offset:1536\n\t"
"v_mfma_f32_16x16x32_bf16 v[112:115], a[72:75], a[96:99], v[112:115]\n\t"
"buffer_load_dwordx4 v243, s[40:43], s73 offen offset:1024 lds\n\t"
"v_mfma_f32_16x16x32_bf16 v[116:119], a[76:79], a[96:99], v[116:119]\n\t"
"ds_read_b64_tr_b16 a[26:27], v251 offset:1792\n\t"
"v_mfma_f32_16x16x32_bf16 v[160:163], a[80:83], a[96:99], v[160:163]\n\t"
"buffer_load_dwordx4 v244, s[40:43], s73 offen offset:2048 lds\n\t"
"v_mfma_f32_16x16x32_bf16 v[164:167], a[84:87], a[96:99], v[164:167]\n\t"
"ds_read_b64_tr_b16 a[28:29], v251 offset:1544\n\t"
"v_mfma_f32_16x16x32_bf16 v[24:27], a[56:59], a[100:103], v[24:27]\n\t"
"ds_read_b64_tr_b16 a[30:31], v251 offset:1800\n\t"
"v_mfma_f32_16x16x32_bf16 v[28:31], a[60:63], a[100:103], v[28:31]\n\t"
"buffer_load_dwordx4 v245, s[40:43], s73 offen offset:3072 lds\n\t"
"v_mfma_f32_16x16x32_bf16 v[72:75], a[64:67], a[100:103], v[72:75]\n\t"
"ds_read_b128 a[36:39], v250 offset:2048\n\t"
"v_mfma_f32_16x16x32_bf16 v[76:79], a[68:71], a[100:103], v[76:79]\n\t"
"s_add_u32 m0, s75, 143360\n\t"
"ds_read_b128 a[40:43], v250 offset:4096\n\t"
"v_mfma_f32_16x16x32_bf16 v[120:123], a[72:75], a[100:103], v[120:123]\n\t"
"ds_read_b128 a[44:47], v250 offset:6144\n\t"
"v_mfma_f32_16x16x32_bf16 v[124:127], a[76:79], a[100:103], v[124:127]\n\t"
"s_add_u32 s73, s73, 0x80\n\t"
"ds_read_b128 a[48:51], v250 offset:8192\n\t"
"v_mfma_f32_16x16x32_bf16 v[168:171], a[80:83], a[100:103], v[168:171]\n\t"
"ds_read_b128 a[52:55], v250 offset:10240\n\t"
"v_mfma_f32_16x16x32_bf16 v[172:175], a[84:87], a[100:103], v[172:175]\n\t"
"buffer_load_dwordx4 v242, s[40:43], s77 offen offset:0 lds\n\t"
"v_mfma_f32_16x16x32_bf16 v[32:35], a[56:59], a[104:107], v[32:35]\n\t"
"v_mfma_f32_16x16x32_bf16 v[36:39], a[60:63], a[104:107], v[36:39]\n\t"
"v_mfma_f32_16x16x32_bf16 v[80:83], a[64:67], a[104:107], v[80:83]\n\t"
"buffer_load_dwordx4 v243, s[40:43], s77 offen offset:1024 lds\n\t"
"v_mfma_f32_16x16x32_bf16 v[84:87], a[68:71], a[104:107], v[84:87]\n\t"
"v_mfma_f32_16x16x32_bf16 v[128:131], a[72:75], a[104:107], v[128:131]\n\t"
"s_cmp_eq_u32 s73, 0x400\n\t"
"v_mfma_f32_16x16x32_bf16 v[132:135], a[76:79], a[104:107], v[132:135]\n\t"
"v_mfma_f32_16x16x32_bf16 v[176:179], a[80:83], a[104:107], v[176:179]\n\t"
"s_cselect_b32 s73, 0x80, s73\n\t"
"v_mfma_f32_16x16x32_bf16 v[180:183], a[84:87], a[104:107], v[180:183]\n\t"
"v_mfma_f32_16x16x32_bf16 v[40:43], a[56:59], a[108:111], v[40:43]\n\t"
"v_mfma_f32_16x16x32_bf16 v[44:47], a[60:63], a[108:111], v[44:47]\n\t"
"v_mfma_f32_16x16x32_bf16 v[88:91], a[64:67], a[108:111], v[88:91]\n\t"
"v_mfma_f32_16x16x32_bf16 v[92:95], a[68:71], a[108:111], v[92:95]\n\t"
"v_mfma_f32_16x16x32_bf16 v[136:139], a[72:75], a[108:111], v[136:139]\n\t"
"v_mfma_f32_16x16x32_bf16 v[140:143], a[76:79], a[108:111], v[140:143]\n\t"
"v_mfma_f32_16x16x32_bf16 v[184:187], a[80:83], a[108:111], v[184:187]\n\t"
"v_mfma_f32_16x16x32_bf16 v[188:191], a[84:87], a[108:111], v[188:191]\n\t"
"s_waitcnt vmcnt(10) lgkmcnt(0)\n\t"
"s_barrier\n\t"
"v_mfma_f32_16x16x32_bf16 v[0:3], a[0:3], a[32:35], v[0:3]\n\t"
"s_add_u32 m0, s76, 32768\n\t"
"ds_read_b128 a[88:91], v250 offset:512\n\t"
"v_mfma_f32_16x16x32_bf16 v[4:7], a[4:7], a[32:35], v[4:7]\n\t"
"ds_read_b64_tr_b16 a[56:57], v251 offset:16384\n\t"
"v_mfma_f32_16x16x32_bf16 v[48:51], a[8:11], a[32:35], v[48:51]\n\t"
"ds_read_b64_tr_b16 a[58:59], v251 offset:16640\n\t"
"v_mfma_f32_16x16x32_bf16 v[52:55], a[12:15], a[32:35], v[52:55]\n\t"
"v_mfma_f32_16x16x32_bf16 v[96:99], a[16:19], a[32:35], v[96:99]\n\t"
"buffer_load_dwordx4 v246, s[44:47], s74 offen offset:0 lds\n\t"
"v_mfma_f32_16x16x32_bf16 v[100:103], a[20:23], a[32:35], v[100:103]\n\t"
"ds_read_b64_tr_b16 a[60:61], v251 offset:16392\n\t"
"v_mfma_f32_16x16x32_bf16 v[144:147], a[24:27], a[32:35], v[144:147]\n\t"
"ds_read_b64_tr_b16 a[62:63], v251 offset:16648\n\t"
"v_mfma_f32_16x16x32_bf16 v[148:151], a[28:31], a[32:35], v[148:151]\n\t"
"ds_read_b64_tr_b16 a[64:65], v251 offset:16896\n\t"
"v_mfma_f32_16x16x32_bf16 v[8:11], a[0:3], a[36:39], v[8:11]\n\t"
"ds_read_b64_tr_b16 a[66:67], v251 offset:17152\n\t"
"v_mfma_f32_16x16x32_bf16 v[12:15], a[4:7], a[36:39], v[12:15]\n\t"
"buffer_load_dwordx4 v247, s[44:47], s74 offen offset:1024 lds\n\t"
"v_mfma_f32_16x16x32_bf16 v[56:59], a[8:11], a[36:39], v[56:59]\n\t"
"ds_read_b64_tr_b16 a[68:69], v251 offset:16904\n\t"
"v_mfma_f32_16x16x32_bf16 v[60:63], a[12:15], a[36:39], v[60:63]\n\t"
"ds_read_b64_tr_b16 a[70:71], v251 offset:17160\n\t"
"v_mfma_f32_16x16x32_bf16 v[104:107], a[16:19], a[36:39], v[104:107]\n\t"
"ds_read_b64_tr_b16 a[72:73], v251 offset:17408\n\t"
"v_mfma_f32_16x16x32_bf16 v[108:111], a[20:23], a[36:39], v[108:111]\n\t"
"buffer_load_dwordx4 v248, s[44:47], s74 offen offset:2048 lds\n\t"
"v_mfma_f32_16x16x32_bf16 v[152:155], a[24:27], a[36:39], v[152:155]\n\t"
"ds_read_b64_tr_b16 a[74:75], v251 offset:17664\n\t"
"v_mfma_f32_16x16x32_bf16 v[156:159], a[28:31], a[36:39], v[156:159]\n\t"
"ds_read_b64_tr_b16 a[76:77], v251 offset:17416\n\t"
"v_mfma_f32_16x16x32_bf16 v[16:19], a[0:3], a[40:43], v[16:19]\n\t"
"ds_read_b64_tr_b16 a[78:79], v251 offset:17672\n\t"
"v_mfma_f32_16x16x32_bf16 v[20:23], a[4:7], a[40:43], v[20:23]\n\t"
"v_mfma_f32_16x16x32_bf16 v[64:67], a[8:11], a[40:43], v[64:67]\n\t"
"buffer_load_dwordx4 v249, s[44:47], s74 offen offset:3072 lds\n\t"
"v_mfma_f32_16x16x32_bf16 v[68:71], a[12:15], a[40:43], v[68:71]\n\t"
"ds_read_b64_tr_b16 a[80:81], v251 offset:17920\n\t"
"v_mfma_f32_16x16x32_bf16 v[112:115], a[16:19], a[40:43], v[112:115]\n\t"
"ds_read_b64_tr_b16 a[82:83], v251 offset:18176\n\t"
"v_mfma_f32_16x16x32_bf16 v[116:119], a[20:23], a[40:43], v[116:119]\n\t"
"ds_read_b64_tr_b16 a[84:85], v251 offset:17928\n\t"
"v_mfma_f32_16x16x32_bf16 v[160:163], a[24:27], a[40:43], v[160:163]\n\t"
"s_add_u32 s74, s74, 262144\n\t"
"ds_read_b64_tr_b16 a[86:87], v251 offset:18184\n\t"
"v_mfma_f32_16x16x32_bf16 v[164:167], a[28:31], a[40:43], v[164:167]\n\t"
"ds_read_b128 a[92:95], v250 offset:2560\n\t"
"v_mfma_f32_16x16x32_bf16 v[24:27], a[0:3], a[44:47], v[24:27]\n\t"
"v_mfma_f32_16x16x32_bf16 v[28:31], a[4:7], a[44:47], v[28:31]\n\t"
"ds_read_b128 a[96:99], v250 offset:4608\n\t"
"v_mfma_f32_16x16x32_bf16 v[72:75], a[8:11], a[44:47], v[72:75]\n\t"
"ds_read_b128 a[100:103], v250 offset:6656\n\t"
"v_mfma_f32_16x16x32_bf16 v[76:79], a[12:15], a[44:47], v[76:79]\n\t"
"s_sub_u32 s69, s74, 3670016\n\t"
"v_mfma_f32_16x16x32_bf16 v[120:123], a[16:19], a[44:47], v[120:123]\n\t"
"ds_read_b128 a[104:107], v250 offset:8704\n\t"
"v_mfma_f32_16x16x32_bf16 v[124:127], a[20:23], a[44:47], v[124:127]\n\t"
"ds_read_b128 a[108:111], v250 offset:10752\n\t"
"v_mfma_f32_16x16x32_bf16 v[168:171], a[24:27], a[44:47], v[168:171]\n\t"
"v_mfma_f32_16x16x32_bf16 v[172:175], a[28:31], a[44:47], v[172:175]\n\t"
"s_cmp_ge_u32 s74, 0x400000\n\t"
"v_mfma_f32_16x16x32_bf16 v[32:35], a[0:3], a[48:51], v[32:35]\n\t"
"v_mfma_f32_16x16x32_bf16 v[36:39], a[4:7], a[48:51], v[36:39]\n\t"
"v_mfma_f32_16x16x32_bf16 v[80:83], a[8:11], a[48:51], v[80:83]\n\t"
"v_mfma_f32_16x16x32_bf16 v[84:87], a[12:15], a[48:51], v[84:87]\n\t"
"v_mfma_f32_16x16x32_bf16 v[128:131], a[16:19], a[48:51], v[128:131]\n\t"
"s_cselect_b32 s74, s69, s74\n\t"
"v_mfma_f32_16x16x32_bf16 v[132:135], a[20:23], a[48:51], v[132:135]\n\t"
"v_mfma_f32_16x16x32_bf16 v[176:179], a[24:27], a[48:51], v[176:179]\n\t"
"v_mfma_f32_16x16x32_bf16 v[180:183], a[28:31], a[48:51], v[180:183]\n\t"
"v_mfma_f32_16x16x32_bf16 v[40:43], a[0:3], a[52:55], v[40:43]\n\t"
"v_mfma_f32_16x16x32_bf16 v[44:47], a[4:7], a[52:55], v[44:47]\n\t"
"v_mfma_f32_16x16x32_bf16 v[88:91], a[8:11], a[52:55], v[88:91]\n\t"
"v_mfma_f32_16x16x32_bf16 v[92:95], a[12:15], a[52:55], v[92:95]\n\t"
"v_mfma_f32_16x16x32_bf16 v[136:139], a[16:19], a[52:55], v[136:139]\n\t"
"v_mfma_f32_16x16x32_bf16 v[140:143], a[20:23], a[52:55], v[140:143]\n\t"
"v_mfma_f32_16x16x32_bf16 v[184:187], a[24:27], a[52:55], v[184:187]\n\t"
"v_mfma_f32_16x16x32_bf16 v[188:191], a[28:31], a[52:55], v[188:191]\n\t"
"s_waitcnt vmcnt(4) lgkmcnt(0)\n\t"
"s_barrier\n\t"
"v_mfma_f32_16x16x32_bf16 v[0:3], a[56:59], a[88:91], v[0:3]\n\t"
"s_add_u32 m0, s76, 49152\n\t"
"ds_read_b128 a[32:35], v250 offset:24576\n\t"
"v_mfma_f32_16x16x32_bf16 v[4:7], a[60:63], a[88:91], v[4:7]\n\t"
"buffer_load_dwordx4 v246, s[44:47], s74 offen offset:0 lds\n\t"
"v_mfma_f32_16x16x32_bf16 v[48:51], a[64:67], a[88:91], v[48:51]\n\t"
"ds_read_b64_tr_b16 a[0:1], v251 offset:32768\n\t"
"v_mfma_f32_16x16x32_bf16 v[52:55], a[68:71], a[88:91], v[52:55]\n\t"
"buffer_load_dwordx4 v247, s[44:47], s74 offen offset:1024 lds\n\t"
"v_mfma_f32_16x16x32_bf16 v[96:99], a[72:75], a[88:91], v[96:99]\n\t"
"ds_read_b64_tr_b16 a[2:3], v251 offset:33024\n\t"
"v_mfma_f32_16x16x32_bf16 v[100:103], a[76:79], a[88:91], v[100:103]\n\t"
"buffer_load_dwordx4 v248, s[44:47], s74 offen offset:2048 lds\n\t"
"v_mfma_f32_16x16x32_bf16 v[144:147], a[80:83], a[88:91], v[144:147]\n\t"
"ds_read_b64_tr_b16 a[4:5], v251 offset:32776\n\t"
"v_mfma_f32_16x16x32_bf16 v[148:151], a[84:87], a[88:91], v[148:151]\n\t"
"buffer_load_dwordx4 v249, s[44:47], s74 offen offset:3072 lds\n\t"
"v_mfma_f32_16x16x32_bf16 v[8:11], a[56:59], a[92:95], v[8:11]\n\t"
"ds_read_b64_tr_b16 a[6:7], v251 offset:33032\n\t"
"v_mfma_f32_16x16x32_bf16 v[12:15], a[60:63], a[92:95], v[12:15]\n\t"
"s_add_u32 s74, s74, 262144\n\t"
"ds_read_b64_tr_b16 a[8:9], v251 offset:33280\n\t"
"v_mfma_f32_16x16x32_bf16 v[56:59], a[64:67], a[92:95], v[56:59]\n\t"
"ds_read_b64_tr_b16 a[10:11], v251 offset:33536\n\t"
"v_mfma_f32_16x16x32_bf16 v[60:63], a[68:71], a[92:95], v[60:63]\n\t"
"s_add_u32 m0, s75, 114688\n\t"
"ds_read_b64_tr_b16 a[12:13], v251 offset:33288\n\t"
"v_mfma_f32_16x16x32_bf16 v[104:107], a[72:75], a[92:95], v[104:107]\n\t"
"ds_read_b64_tr_b16 a[14:15], v251 offset:33544\n\t"
"v_mfma_f32_16x16x32_bf16 v[108:111], a[76:79], a[92:95], v[108:111]\n\t"
"s_add_u32 s77, s73, 0x8000\n\t"
"ds_read_b64_tr_b16 a[16:17], v251 offset:33792\n\t"
"v_mfma_f32_16x16x32_bf16 v[152:155], a[80:83], a[92:95], v[152:155]\n\t"
"ds_read_b64_tr_b16 a[18:19], v251 offset:34048\n\t"
"v_mfma_f32_16x16x32_bf16 v[156:159], a[84:87], a[92:95], v[156:159]\n\t"
"buffer_load_dwordx4 v242, s[40:43], s73 offen offset:0 lds\n\t"
"v_mfma_f32_16x16x32_bf16 v[16:19], a[56:59], a[96:99], v[16:19]\n\t"
"ds_read_b64_tr_b16 a[20:21], v251 offset:33800\n\t"
"v_mfma_f32_16x16x32_bf16 v[20:23], a[60:63], a[96:99], v[20:23]\n\t"
"buffer_load_dwordx4 v243, s[40:43], s73 offen offset:1024 lds\n\t"
"v_mfma_f32_16x16x32_bf16 v[64:67], a[64:67], a[96:99], v[64:67]\n\t"
"ds_read_b64_tr_b16 a[22:23], v251 offset:34056\n\t"
"v_mfma_f32_16x16x32_bf16 v[68:71], a[68:71], a[96:99], v[68:71]\n\t"
"buffer_load_dwordx4 v244, s[40:43], s73 offen offset:2048 lds\n\t"
"v_mfma_f32_16x16x32_bf16 v[112:115], a[72:75], a[96:99], v[112:115]\n\t"
"ds_read_b64_tr_b16 a[24:25], v251 offset:34304\n\t"
"v_mfma_f32_16x16x32_bf16 v[116:119], a[76:79], a[96:99], v[116:119]\n\t"
"buffer_load_dwordx4 v245, s[40:43], s73 offen offset:3072 lds\n\t"
"v_mfma_f32_16x16x32_bf16 v[160:163], a[80:83], a[96:99], v[160:163]\n\t"
"ds_read_b64_tr_b16 a[26:27], v251 offset:34560\n\t"
"v_mfma_f32_16x16x32_bf16 v[164:167], a[84:87], a[96:99], v[164:167]\n\t"
"s_add_u32 m0, s75, 118784\n\t"
"ds_read_b64_tr_b16 a[28:29], v251 offset:34312\n\t"
"v_mfma_f32_16x16x32_bf16 v[24:27], a[56:59], a[100:103], v[24:27]\n\t"
"ds_read_b64_tr_b16 a[30:31], v251 offset:34568\n\t"
"v_mfma_f32_16x16x32_bf16 v[28:31], a[60:63], a[100:103], v[28:31]\n\t"
"s_add_u32 s73, s73, 0x80\n\t"
"ds_read_b128 a[36:39], v250 offset:26624\n\t"
"v_mfma_f32_16x16x32_bf16 v[72:75], a[64:67], a[100:103], v[72:75]\n\t"
"ds_read_b128 a[40:43], v250 offset:28672\n\t"
"v_mfma_f32_16x16x32_bf16 v[76:79], a[68:71], a[100:103], v[76:79]\n\t"
"buffer_load_dwordx4 v242, s[40:43], s77 offen offset:0 lds\n\t"
"v_mfma_f32_16x16x32_bf16 v[120:123], a[72:75], a[100:103], v[120:123]\n\t"
"ds_read_b128 a[44:47], v250 offset:30720\n\t"
"v_mfma_f32_16x16x32_bf16 v[124:127], a[76:79], a[100:103], v[124:127]\n\t"
"buffer_load_dwordx4 v243, s[40:43], s77 offen offset:1024 lds\n\t"
"v_mfma_f32_16x16x32_bf16 v[168:171], a[80:83], a[100:103], v[168:171]\n\t"
"ds_read_b128 a[48:51], v250 offset:32768\n\t"
"v_mfma_f32_16x16x32_bf16 v[172:175], a[84:87], a[100:103], v[172:175]\n\t"
"s_cmp_eq_u32 s73, 0x400\n\t"
"ds_read_b128 a[52:55], v250 offset:34816\n\t"
"v_mfma_f32_16x16x32_bf16 v[32:35], a[56:59], a[104:107], v[32:35]\n\t"
"v_mfma_f32_16x16x32_bf16 v[36:39], a[60:63], a[104:107], v[36:39]\n\t"
"s_cselect_b32 s73, 0x80, s73\n\t"
"v_mfma_f32_16x16x32_bf16 v[80:83], a[64:67], a[104:107], v[80:83]\n\t"
"v_mfma_f32_16x16x32_bf16 v[84:87], a[68:71], a[104:107], v[84:87]\n\t"
"s_mov_b32 m0, s56\n\t"
"v_mfma_f32_16x16x32_bf16 v[128:131], a[72:75], a[104:107], v[128:131]\n\t"
"v_mfma_f32_16x16x32_bf16 v[132:135], a[76:79], a[104:107], v[132:135]\n\t"
"buffer_load_dwordx4 v223, s[36:39], s54 offen offset:0 nt lds\n\t"
"v_mfma_f32_16x16x32_bf16 v[176:179], a[80:83], a[104:107], v[176:179]\n\t"
"v_mfma_f32_16x16x32_bf16 v[180:183], a[84:87], a[104:107], v[180:183]\n\t"
"buffer_load_dwordx4 v224, s[36:39], s54 offen offset:1024 nt lds\n\t"
"v_mfma_f32_16x16x32_bf16 v[40:43], a[56:59], a[108:111], v[40:43]\n\t"
"v_mfma_f32_16x16x32_bf16 v[44:47], a[60:63], a[108:111], v[44:47]\n\t"
"v_mfma_f32_16x16x32_bf16 v[88:91], a[64:67], a[108:111], v[88:91]\n\t"
"v_mfma_f32_16x16x32_bf16 v[92:95], a[68:71], a[108:111], v[92:95]\n\t"
"v_mfma_f32_16x16x32_bf16 v[136:139], a[72:75], a[108:111], v[136:139]\n\t"
"v_mfma_f32_16x16x32_bf16 v[140:143], a[76:79], a[108:111], v[140:143]\n\t"
"v_mfma_f32_16x16x32_bf16 v[184:187], a[80:83], a[108:111], v[184:187]\n\t"
"v_mfma_f32_16x16x32_bf16 v[188:191], a[84:87], a[108:111], v[188:191]\n\t"
"s_waitcnt vmcnt(12) lgkmcnt(0)\n\t"
"s_barrier\n\t"
"v_mfma_f32_16x16x32_bf16 v[0:3], a[0:3], a[32:35], v[0:3]\n\t"
"s_add_u32 m0, s76, 65536\n\t"
"ds_read_b128 a[88:91], v250 offset:25088\n\t"
"v_mfma_f32_16x16x32_bf16 v[4:7], a[4:7], a[32:35], v[4:7]\n\t"
"ds_read_b64_tr_b16 a[56:57], v251 offset:0\n\t"
"v_mfma_f32_16x16x32_bf16 v[48:51], a[8:11], a[32:35], v[48:51]\n\t"
"ds_read_b64_tr_b16 a[58:59], v251 offset:256\n\t"
"v_mfma_f32_16x16x32_bf16 v[52:55], a[12:15], a[32:35], v[52:55]\n\t"
"buffer_load_dwordx4 v246, s[44:47], s74 offen offset:0 lds\n\t"
"v_mfma_f32_16x16x32_bf16 v[96:99], a[16:19], a[32:35], v[96:99]\n\t"
"ds_read_b64_tr_b16 a[60:61], v251 offset:8\n\t"
"v_mfma_f32_16x16x32_bf16 v[100:103], a[20:23], a[32:35], v[100:103]\n\t"
"ds_read_b64_tr_b16 a[62:63], v251 offset:264\n\t"
"v_mfma_f32_16x16x32_bf16 v[144:147], a[24:27], a[32:35], v[144:147]\n\t"
"buffer_load_dwordx4 v247, s[44:47], s74 offen offset:1024 lds\n\t"
"v_mfma_f32_16x16x32_bf16 v[148:151], a[28:31], a[32:35], v[148:151]\n\t"
"ds_read_b64_tr_b16 a[64:65], v251 offset:512\n\t"
"v_mfma_f32_16x16x32_bf16 v[8:11], a[0:3], a[36:39], v[8:11]\n\t"
"ds_read_b64_tr_b16 a[66:67], v251 offset:768\n\t"
"v_mfma_f32_16x16x32_bf16 v[12:15], a[4:7], a[36:39], v[12:15]\n\t"
"ds_read_b64_tr_b16 a[68:69], v251 offset:520\n\t"
"v_mfma_f32_16x16x32_bf16 v[56:59], a[8:11], a[36:39], v[56:59]\n\t"
"buffer_load_dwordx4 v248, s[44:47], s74 offen offset:2048 lds\n\t"
"v_mfma_f32_16x16x32_bf16 v[60:63], a[12:15], a[36:39], v[60:63]\n\t"
"ds_read_b64_tr_b16 a[70:71], v251 offset:776\n\t"
"v_mfma_f32_16x16x32_bf16 v[104:107], a[16:19], a[36:39], v[104:107]\n\t"
"ds_read_b64_tr_b16 a[72:73], v251 offset:1024\n\t"
"v_mfma_f32_16x16x32_bf16 v[108:111], a[20:23], a[36:39], v[108:111]\n\t"
"buffer_load_dwordx4 v249, s[44:47], s74 offen offset:3072 lds\n\t"
"v_mfma_f32_16x16x32_bf16 v[152:155], a[24:27], a[36:39], v[152:155]\n\t"
"ds_read_b64_tr_b16 a[74:75], v251 offset:1280\n\t"
"v_mfma_f32_16x16x32_bf16 v[156:159], a[28:31], a[36:39], v[156:159]\n\t"
"ds_read_b64_tr_b16 a[76:77], v251 offset:1032\n\t"
"v_mfma_f32_16x16x32_bf16 v[16:19], a[0:3], a[40:43], v[16:19]\n\t"
"ds_read_b64_tr_b16 a[78:79], v251 offset:1288\n\t"
"v_mfma_f32_16x16x32_bf16 v[20:23], a[4:7], a[40:43], v[20:23]\n\t"
"s_add_u32 s74, s74, 262144\n\t"
"v_mfma_f32_16x16x32_bf16 v[64:67], a[8:11], a[40:43], v[64:67]\n\t"
"ds_read_b64_tr_b16 a[80:81], v251 offset:1536\n\t"
"v_mfma_f32_16x16x32_bf16 v[68:71], a[12:15], a[40:43], v[68:71]\n\t"
"ds_read_b64_tr_b16 a[82:83], v251 offset:1792\n\t"
"v_mfma_f32_16x16x32_bf16 v[112:115], a[16:19], a[40:43], v[112:115]\n\t"
"s_sub_u32 s69, s74, 3670016\n\t"
"v_mfma_f32_16x16x32_bf16 v[116:119], a[20:23], a[40:43], v[116:119]\n\t"
"ds_read_b64_tr_b16 a[84:85], v251 offset:1544\n\t"
"v_mfma_f32_16x16x32_bf16 v[160:163], a[24:27], a[40:43], v[160:163]\n\t"
"ds_read_b64_tr_b16 a[86:87], v251 offset:1800\n\t"
"v_mfma_f32_16x16x32_bf16 v[164:167], a[28:31], a[40:43], v[164:167]\n\t"
"s_cmp_ge_u32 s74, 0x400000\n\t"
"ds_read_b128 a[92:95], v250 offset:27136\n\t"
"v_mfma_f32_16x16x32_bf16 v[24:27], a[0:3], a[44:47], v[24:27]\n\t"
"v_mfma_f32_16x16x32_bf16 v[28:31], a[4:7], a[44:47], v[28:31]\n\t"
"ds_read_b128 a[96:99], v250 offset:29184\n\t"
"v_mfma_f32_16x16x32_bf16 v[72:75], a[8:11], a[44:47], v[72:75]\n\t"
"ds_read_b128 a[100:103], v250 offset:31232\n\t"
"v_mfma_f32_16x16x32_bf16 v[76:79], a[12:15], a[44:47], v[76:79]\n\t"
"s_cselect_b32 s74, s69, s74\n\t"
"v_mfma_f32_16x16x32_bf16 v[120:123], a[16:19], a[44:47], v[120:123]\n\t"
"ds_read_b128 a[104:107], v250 offset:33280\n\t"
"v_mfma_f32_16x16x32_bf16 v[124:127], a[20:23], a[44:47], v[124:127]\n\t"
"ds_read_b128 a[108:111], v250 offset:35328\n\t"
"v_mfma_f32_16x16x32_bf16 v[168:171], a[24:27], a[44:47], v[168:171]\n\t"
"s_mov_b32 m0, s56\n\t"
"v_mfma_f32_16x16x32_bf16 v[172:175], a[28:31], a[44:47], v[172:175]\n\t"
"v_mfma_f32_16x16x32_bf16 v[32:35], a[0:3], a[48:51], v[32:35]\n\t"
"v_mfma_f32_16x16x32_bf16 v[36:39], a[4:7], a[48:51], v[36:39]\n\t"
"v_mfma_f32_16x16x32_bf16 v[80:83], a[8:11], a[48:51], v[80:83]\n\t"
"buffer_load_dwordx4 v225, s[36:39], s54 offen offset:2048 nt lds\n\t"
"v_mfma_f32_16x16x32_bf16 v[84:87], a[12:15], a[48:51], v[84:87]\n\t"
"v_mfma_f32_16x16x32_bf16 v[128:131], a[16:19], a[48:51], v[128:131]\n\t"
"v_mfma_f32_16x16x32_bf16 v[132:135], a[20:23], a[48:51], v[132:135]\n\t"
"buffer_load_dwordx4 v226, s[36:39], s54 offen offset:3072 nt lds\n\t"
"v_mfma_f32_16x16x32_bf16 v[176:179], a[24:27], a[48:51], v[176:179]\n\t"
"v_mfma_f32_16x16x32_bf16 v[180:183], a[28:31], a[48:51], v[180:183]\n\t"
"v_mfma_f32_16x16x32_bf16 v[40:43], a[0:3], a[52:55], v[40:43]\n\t"
"v_mfma_f32_16x16x32_bf16 v[44:47], a[4:7], a[52:55], v[44:47]\n\t"
"v_mfma_f32_16x16x32_bf16 v[88:91], a[8:11], a[52:55], v[88:91]\n\t"
"v_mfma_f32_16x16x32_bf16 v[92:95], a[12:15], a[52:55], v[92:95]\n\t"
"v_mfma_f32_16x16x32_bf16 v[136:139], a[16:19], a[52:55], v[136:139]\n\t"
"v_mfma_f32_16x16x32_bf16 v[140:143], a[20:23], a[52:55], v[140:143]\n\t"
"v_mfma_f32_16x16x32_bf16 v[184:187], a[24:27], a[52:55], v[184:187]\n\t"
"v_mfma_f32_16x16x32_bf16 v[188:191], a[28:31], a[52:55], v[188:191]\n\t"
"s_waitcnt vmcnt(8) lgkmcnt(0)\n\t"
"s_barrier\n\t"
"v_mfma_f32_16x16x32_bf16 v[0:3], a[56:59], a[88:91], v[0:3]\n\t"
"s_add_u32 m0, s76, 32768\n\t"
"ds_read_b128 a[32:35], v250 offset:0\n\t"
"v_mfma_f32_16x16x32_bf16 v[4:7], a[60:63], a[88:91], v[4:7]\n\t"
"buffer_load_dwordx4 v246, s[44:47], s74 offen offset:0 lds\n\t"
"v_mfma_f32_16x16x32_bf16 v[48:51], a[64:67], a[88:91], v[48:51]\n\t"
"ds_read_b64_tr_b16 a[0:1], v251 offset:16384\n\t"
"v_mfma_f32_16x16x32_bf16 v[52:55], a[68:71], a[88:91], v[52:55]\n\t"
"buffer_load_dwordx4 v247, s[44:47], s74 offen offset:1024 lds\n\t"
"v_mfma_f32_16x16x32_bf16 v[96:99], a[72:75], a[88:91], v[96:99]\n\t"
"ds_read_b64_tr_b16 a[2:3], v251 offset:16640\n\t"
"v_mfma_f32_16x16x32_bf16 v[100:103], a[76:79], a[88:91], v[100:103]\n\t"
"buffer_load_dwordx4 v248, s[44:47], s74 offen offset:2048 lds\n\t"
"v_mfma_f32_16x16x32_bf16 v[144:147], a[80:83], a[88:91], v[144:147]\n\t"
"ds_read_b64_tr_b16 a[4:5], v251 offset:16392\n\t"
"v_mfma_f32_16x16x32_bf16 v[148:151], a[84:87], a[88:91], v[148:151]\n\t"
"buffer_load_dwordx4 v249, s[44:47], s74 offen offset:3072 lds\n\t"
"v_mfma_f32_16x16x32_bf16 v[8:11], a[56:59], a[92:95], v[8:11]\n\t"
"ds_read_b64_tr_b16 a[6:7], v251 offset:16648\n\t"
"v_mfma_f32_16x16x32_bf16 v[12:15], a[60:63], a[92:95], v[12:15]\n\t"
"s_add_u32 s74, s74, 262144\n\t"
"ds_read_b64_tr_b16 a[8:9], v251 offset:16896\n\t"
"v_mfma_f32_16x16x32_bf16 v[56:59], a[64:67], a[92:95], v[56:59]\n\t"
"ds_read_b64_tr_b16 a[10:11], v251 offset:17152\n\t"
"v_mfma_f32_16x16x32_bf16 v[60:63], a[68:71], a[92:95], v[60:63]\n\t"
"s_add_u32 m0, s75, 139264\n\t"
"ds_read_b64_tr_b16 a[12:13], v251 offset:16904\n\t"
"v_mfma_f32_16x16x32_bf16 v[104:107], a[72:75], a[92:95], v[104:107]\n\t"
"ds_read_b64_tr_b16 a[14:15], v251 offset:17160\n\t"
"v_mfma_f32_16x16x32_bf16 v[108:111], a[76:79], a[92:95], v[108:111]\n\t"
"s_add_u32 s77, s73, 0x8000\n\t"
"ds_read_b64_tr_b16 a[16:17], v251 offset:17408\n\t"
"v_mfma_f32_16x16x32_bf16 v[152:155], a[80:83], a[92:95], v[152:155]\n\t"
"ds_read_b64_tr_b16 a[18:19], v251 offset:17664\n\t"
"v_mfma_f32_16x16x32_bf16 v[156:159], a[84:87], a[92:95], v[156:159]\n\t"
"buffer_load_dwordx4 v242, s[40:43], s73 offen offset:0 lds\n\t"
"v_mfma_f32_16x16x32_bf16 v[16:19], a[56:59], a[96:99], v[16:19]\n\t"
"ds_read_b64_tr_b16 a[20:21], v251 offset:17416\n\t"
"v_mfma_f32_16x16x32_bf16 v[20:23], a[60:63], a[96:99], v[20:23]\n\t"
"buffer_load_dwordx4 v243, s[40:43], s73 offen offset:1024 lds\n\t"
"v_mfma_f32_16x16x32_bf16 v[64:67], a[64:67], a[96:99], v[64:67]\n\t"
"ds_read_b64_tr_b16 a[22:23], v251 offset:17672\n\t"
"v_mfma_f32_16x16x32_bf16 v[68:71], a[68:71], a[96:99], v[68:71]\n\t"
"buffer_load_dwordx4 v244, s[40:43], s73 offen offset:2048 lds\n\t"
"v_mfma_f32_16x16x32_bf16 v[112:115], a[72:75], a[96:99], v[112:115]\n\t"
"ds_read_b64_tr_b16 a[24:25], v251 offset:17920\n\t"
"v_mfma_f32_16x16x32_bf16 v[116:119], a[76:79], a[96:99], v[116:119]\n\t"
"buffer_load_dwordx4 v245, s[40:43], s73 offen offset:3072 lds\n\t"
"v_mfma_f32_16x16x32_bf16 v[160:163], a[80:83], a[96:99], v[160:163]\n\t"
"ds_read_b64_tr_b16 a[26:27], v251 offset:18176\n\t"
"v_mfma_f32_16x16x32_bf16 v[164:167], a[84:87], a[96:99], v[164:167]\n\t"
"s_add_u32 m0, s75, 143360\n\t"
"ds_read_b64_tr_b16 a[28:29], v251 offset:17928\n\t"
"v_mfma_f32_16x16x32_bf16 v[24:27], a[56:59], a[100:103], v[24:27]\n\t"
"ds_read_b64_tr_b16 a[30:31], v251 offset:18184\n\t"
"v_mfma_f32_16x16x32_bf16 v[28:31], a[60:63], a[100:103], v[28:31]\n\t"
"s_add_u32 s73, s73, 0x80\n\t"
"ds_read_b128 a[36:39], v250 offset:2048\n\t"
"v_mfma_f32_16x16x32_bf16 v[72:75], a[64:67], a[100:103], v[72:75]\n\t"
"ds_read_b128 a[40:43], v250 offset:4096\n\t"
"v_mfma_f32_16x16x32_bf16 v[76:79], a[68:71], a[100:103], v[76:79]\n\t"
"buffer_load_dwordx4 v242, s[40:43], s77 offen offset:0 lds\n\t"
"v_mfma_f32_16x16x32_bf16 v[120:123], a[72:75], a[100:103], v[120:123]\n\t"
"ds_read_b128 a[44:47], v250 offset:6144\n\t"
"v_mfma_f32_16x16x32_bf16 v[124:127], a[76:79], a[100:103], v[124:127]\n\t"
"buffer_load_dwordx4 v243, s[40:43], s77 offen offset:1024 lds\n\t"
"v_mfma_f32_16x16x32_bf16 v[168:171], a[80:83], a[100:103], v[168:171]\n\t"
"ds_read_b128 a[48:51], v250 offset:8192\n\t"
"v_mfma_f32_16x16x32_bf16 v[172:175], a[84:87], a[100:103], v[172:175]\n\t"
"s_cmp_eq_u32 s73, 0x400\n\t"
"ds_read_b128 a[52:55], v250 offset:10240\n\t"
"v_mfma_f32_16x16x32_bf16 v[32:35], a[56:59], a[104:107], v[32:35]\n\t"
"v_mfma_f32_16x16x32_bf16 v[36:39], a[60:63], a[104:107], v[36:39]\n\t"
"s_cselect_b32 s73, 0x80, s73\n\t"
"v_mfma_f32_16x16x32_bf16 v[80:83], a[64:67], a[104:107], v[80:83]\n\t"
"v_mfma_f32_16x16x32_bf16 v[84:87], a[68:71], a[104:107], v[84:87]\n\t"
"s_add_u32 m0, s56, 0x1000\n\t"
"v_mfma_f32_16x16x32_bf16 v[128:131], a[72:75], a[104:107], v[128:131]\n\t"
"v_mfma_f32_16x16x32_bf16 v[132:135], a[76:79], a[104:107], v[132:135]\n\t"
"buffer_load_dwordx4 v223, s[36:39], s55 offen offset:0 nt lds\n\t"
"v_mfma_f32_16x16x32_bf16 v[176:179], a[80:83], a[104:107], v[176:179]\n\t"
"v_mfma_f32_16x16x32_bf16 v[180:183], a[84:87], a[104:107], v[180:183]\n\t"
"buffer_load_dwordx4 v224, s[36:39], s55 offen offset:1024 nt lds\n\t"
"v_mfma_f32_16x16x32_bf16 v[40:43], a[56:59], a[108:111], v[40:43]\n\t"
"v_mfma_f32_16x16x32_bf16 v[44:47], a[60:63], a[108:111], v[44:47]\n\t"
"v_mfma_f32_16x16x32_bf16 v[88:91], a[64:67], a[108:111], v[88:91]\n\t"
"v_mfma_f32_16x16x32_bf16 v[92:95], a[68:71], a[108:111], v[92:95]\n\t"
"v_mfma_f32_16x16x32_bf16 v[136:139], a[72:75], a[108:111], v[136:139]\n\t"
"v_mfma_f32_16x16x32_bf16 v[140:143], a[76:79], a[108:111], v[140:143]\n\t"
"v_mfma_f32_16x16x32_bf16 v[184:187], a[80:83], a[108:111], v[184:187]\n\t"
"v_mfma_f32_16x16x32_bf16 v[188:191], a[84:87], a[108:111], v[188:191]\n\t"
"s_waitcnt vmcnt(14) lgkmcnt(0)\n\t"
"s_barrier\n\t"
"v_mfma_f32_16x16x32_bf16 v[0:3], a[0:3], a[32:35], v[0:3]\n\t"
"s_add_u32 m0, s76, 49152\n\t"
"ds_read_b128 a[88:91], v250 offset:512\n\t"
"v_mfma_f32_16x16x32_bf16 v[4:7], a[4:7], a[32:35], v[4:7]\n\t"
"ds_read_b64_tr_b16 a[56:57], v251 offset:32768\n\t"
"v_mfma_f32_16x16x32_bf16 v[48:51], a[8:11], a[32:35], v[48:51]\n\t"
"buffer_load_dwordx4 v246, s[44:47], s74 offen offset:0 lds\n\t"
"v_mfma_f32_16x16x32_bf16 v[52:55], a[12:15], a[32:35], v[52:55]\n\t"
"ds_read_b64_tr_b16 a[58:59], v251 offset:33024\n\t"
"v_mfma_f32_16x16x32_bf16 v[96:99], a[16:19], a[32:35], v[96:99]\n\t"
"ds_read_b64_tr_b16 a[60:61], v251 offset:32776\n\t"
"v_mfma_f32_16x16x32_bf16 v[100:103], a[20:23], a[32:35], v[100:103]\n\t"
"buffer_load_dwordx4 v247, s[44:47], s74 offen offset:1024 lds\n\t"
"v_mfma_f32_16x16x32_bf16 v[144:147], a[24:27], a[32:35], v[144:147]\n\t"
"ds_read_b64_tr_b16 a[62:63], v251 offset:33032\n\t"
"v_mfma_f32_16x16x32_bf16 v[148:151], a[28:31], a[32:35], v[148:151]\n\t"
"ds_read_b64_tr_b16 a[64:65], v251 offset:33280\n\t"
"v_mfma_f32_16x16x32_bf16 v[8:11], a[0:3], a[36:39], v[8:11]\n\t"
"buffer_load_dwordx4 v248, s[44:47], s74 offen offset:2048 lds\n\t"
"v_mfma_f32_16x16x32_bf16 v[12:15], a[4:7], a[36:39], v[12:15]\n\t"
"ds_read_b64_tr_b16 a[66:67], v251 offset:33536\n\t"
"v_mfma_f32_16x16x32_bf16 v[56:59], a[8:11], a[36:39], v[56:59]\n\t"
"buffer_load_dwordx4 v249, s[44:47], s74 offen offset:3072 lds\n\t"
"v_mfma_f32_16x16x32_bf16 v[60:63], a[12:15], a[36:39], v[60:63]\n\t"
"ds_read_b64_tr_b16 a[68:69], v251 offset:33288\n\t"
"v_mfma_f32_16x16x32_bf16 v[104:107], a[16:19], a[36:39], v[104:107]\n\t"
"ds_read_b64_tr_b16 a[70:71], v251 offset:33544\n\t"
"v_mfma_f32_16x16x32_bf16 v[108:111], a[20:23], a[36:39], v[108:111]\n\t"
"s_add_u32 s74, s74, 262144\n\t"
"ds_read_b64_tr_b16 a[72:73], v251 offset:33792\n\t"
"v_mfma_f32_16x16x32_bf16 v[152:155], a[24:27], a[36:39], v[152:155]\n\t"
"ds_read_b64_tr_b16 a[74:75], v251 offset:34048\n\t"
"v_mfma_f32_16x16x32_bf16 v[156:159], a[28:31], a[36:39], v[156:159]\n\t"
"ds_read_b64_tr_b16 a[76:77], v251 offset:33800\n\t"
"v_mfma_f32_16x16x32_bf16 v[16:19], a[0:3], a[40:43], v[16:19]\n\t"
"s_sub_u32 s69, s74, 3670016\n\t"
"ds_read_b64_tr_b16 a[78:79], v251 offset:34056\n\t"
"v_mfma_f32_16x16x32_bf16 v[20:23], a[4:7], a[40:43], v[20:23]\n\t"
"v_mfma_f32_16x16x32_bf16 v[64:67], a[8:11], a[40:43], v[64:67]\n\t"
"ds_read_b64_tr_b16 a[80:81], v251 offset:34304\n\t"
"v_mfma_f32_16x16x32_bf16 v[68:71], a[12:15], a[40:43], v[68:71]\n\t"
"s_cmp_ge_u32 s74, 0x400000\n\t"
"ds_read_b64_tr_b16 a[82:83], v251 offset:34560\n\t"
"v_mfma_f32_16x16x32_bf16 v[112:115], a[16:19], a[40:43], v[112:115]\n\t"
"v_mfma_f32_16x16x32_bf16 v[116:119], a[20:23], a[40:43], v[116:119]\n\t"
"s_cselect_b32 s74, s69, s74\n\t"
"ds_read_b64_tr_b16 a[84:85], v251 offset:34312\n\t"
"v_mfma_f32_16x16x32_bf16 v[160:163], a[24:27], a[40:43], v[160:163]\n\t"
"ds_read_b64_tr_b16 a[86:87], v251 offset:34568\n\t"
"v_mfma_f32_16x16x32_bf16 v[164:167], a[28:31], a[40:43], v[164:167]\n\t"
"ds_read_b128 a[92:95], v250 offset:2560\n\t"
"v_mfma_f32_16x16x32_bf16 v[24:27], a[0:3], a[44:47], v[24:27]\n\t"
"s_add_u32 m0, s56, 0x1000\n\t"
"v_mfma_f32_16x16x32_bf16 v[28:31], a[4:7], a[44:47], v[28:31]\n\t"
"ds_read_b128 a[96:99], v250 offset:4608\n\t"
"v_mfma_f32_16x16x32_bf16 v[72:75], a[8:11], a[44:47], v[72:75]\n\t"
"ds_read_b128 a[100:103], v250 offset:6656\n\t"
"v_mfma_f32_16x16x32_bf16 v[76:79], a[12:15], a[44:47], v[76:79]\n\t"
"buffer_load_dwordx4 v225, s[36:39], s55 offen offset:2048 nt lds\n\t"
"v_mfma_f32_16x16x32_bf16 v[120:123], a[16:19], a[44:47], v[120:123]\n\t"
"ds_read_b128 a[104:107], v250 offset:8704\n\t"
"v_mfma_f32_16x16x32_bf16 v[124:127], a[20:23], a[44:47], v[124:127]\n\t"
"ds_read_b128 a[108:111], v250 offset:10752\n\t"
"v_mfma_f32_16x16x32_bf16 v[168:171], a[24:27], a[44:47], v[168:171]\n\t"
"buffer_load_dwordx4 v226, s[36:39], s55 offen offset:3072 nt lds\n\t"
"v_mfma_f32_16x16x32_bf16 v[172:175], a[28:31], a[44:47], v[172:175]\n\t"
"v_mfma_f32_16x16x32_bf16 v[32:35], a[0:3], a[48:51], v[32:35]\n\t"
"s_add_u32 s54, s54, 0x2000\n\t"
"v_mfma_f32_16x16x32_bf16 v[36:39], a[4:7], a[48:51], v[36:39]\n\t"
"v_mfma_f32_16x16x32_bf16 v[80:83], a[8:11], a[48:51], v[80:83]\n\t"
"v_mfma_f32_16x16x32_bf16 v[84:87], a[12:15], a[48:51], v[84:87]\n\t"
"s_add_u32 s55, s55, 0x2000\n\t"
"v_mfma_f32_16x16x32_bf16 v[128:131], a[16:19], a[48:51], v[128:131]\n\t"
"v_mfma_f32_16x16x32_bf16 v[132:135], a[20:23], a[48:51], v[132:135]\n\t"
"v_mfma_f32_16x16x32_bf16 v[176:179], a[24:27], a[48:51], v[176:179]\n\t"
"s_add_u32 s56, s56, 0x4000\n\t"
"v_mfma_f32_16x16x32_bf16 v[180:183], a[28:31], a[48:51], v[180:183]\n\t"
"v_mfma_f32_16x16x32_bf16 v[40:43], a[0:3], a[52:55], v[40:43]\n\t"
"v_mfma_f32_16x16x32_bf16 v[44:47], a[4:7], a[52:55], v[44:47]\n\t"
"v_mfma_f32_16x16x32_bf16 v[88:91], a[8:11], a[52:55], v[88:91]\n\t"
"v_mfma_f32_16x16x32_bf16 v[92:95], a[12:15], a[52:55], v[92:95]\n\t"
"v_mfma_f32_16x16x32_bf16 v[136:139], a[16:19], a[52:55], v[136:139]\n\t"
"v_mfma_f32_16x16x32_bf16 v[140:143], a[20:23], a[52:55], v[140:143]\n\t"
"v_mfma_f32_16x16x32_bf16 v[184:187], a[24:27], a[52:55], v[184:187]\n\t"
"v_mfma_f32_16x16x32_bf16 v[188:191], a[28:31], a[52:55], v[188:191]\n\t"
"s_waitcnt vmcnt(8) lgkmcnt(0)\n\t"
"s_barrier\n\t"
"v_mfma_f32_16x16x32_bf16 v[0:3], a[56:59], a[88:91], v[0:3]\n\t"
"s_add_u32 m0, s76, 65536\n\t"
"ds_read_b128 a[32:35], v250 offset:24576\n\t"
"v_mfma_f32_16x16x32_bf16 v[4:7], a[60:63], a[88:91], v[4:7]\n\t"
"buffer_load_dwordx4 v246, s[44:47], s74 offen offset:0 lds\n\t"
"v_mfma_f32_16x16x32_bf16 v[48:51], a[64:67], a[88:91], v[48:51]\n\t"
"ds_read_b64_tr_b16 a[0:1], v251 offset:0\n\t"
"v_mfma_f32_16x16x32_bf16 v[52:55], a[68:71], a[88:91], v[52:55]\n\t"
"buffer_load_dwordx4 v247, s[44:47], s74 offen offset:1024 lds\n\t"
"v_mfma_f32_16x16x32_bf16 v[96:99], a[72:75], a[88:91], v[96:99]\n\t"
"ds_read_b64_tr_b16 a[2:3], v251 offset:256\n\t"
"v_mfma_f32_16x16x32_bf16 v[100:103], a[76:79], a[88:91], v[100:103]\n\t"
"buffer_load_dwordx4 v248, s[44:47], s74 offen offset:2048 lds\n\t"
"v_mfma_f32_16x16x32_bf16 v[144:147], a[80:83], a[88:91], v[144:147]\n\t"
"ds_read_b64_tr_b16 a[4:5], v251 offset:8\n\t"
"v_mfma_f32_16x16x32_bf16 v[148:151], a[84:87], a[88:91], v[148:151]\n\t"
"buffer_load_dwordx4 v249, s[44:47], s74 offen offset:3072 lds\n\t"
"v_mfma_f32_16x16x32_bf16 v[8:11], a[56:59], a[92:95], v[8:11]\n\t"
"ds_read_b64_tr_b16 a[6:7], v251 offset:264\n\t"
"v_mfma_f32_16x16x32_bf16 v[12:15], a[60:63], a[92:95], v[12:15]\n\t"
"s_add_u32 s74, s74, 262144\n\t"
"ds_read_b64_tr_b16 a[8:9], v251 offset:512\n\t"
"v_mfma_f32_16x16x32_bf16 v[56:59], a[64:67], a[92:95], v[56:59]\n\t"
"ds_read_b64_tr_b16 a[10:11], v251 offset:768\n\t"
"v_mfma_f32_16x16x32_bf16 v[60:63], a[68:71], a[92:95], v[60:63]\n\t"
"s_add_u32 m0, s75, 114688\n\t"
"ds_read_b64_tr_b16 a[12:13], v251 offset:520\n\t"
"v_mfma_f32_16x16x32_bf16 v[104:107], a[72:75], a[92:95], v[104:107]\n\t"
"ds_read_b64_tr_b16 a[14:15], v251 offset:776\n\t"
"v_mfma_f32_16x16x32_bf16 v[108:111], a[76:79], a[92:95], v[108:111]\n\t"
"s_add_u32 s77, s73, 0x8000\n\t"
"ds_read_b64_tr_b16 a[16:17], v251 offset:1024\n\t"
"v_mfma_f32_16x16x32_bf16 v[152:155], a[80:83], a[92:95], v[152:155]\n\t"
"ds_read_b64_tr_b16 a[18:19], v251 offset:1280\n\t"
"v_mfma_f32_16x16x32_bf16 v[156:159], a[84:87], a[92:95], v[156:159]\n\t"
"buffer_load_dwordx4 v242, s[40:43], s73 offen offset:0 lds\n\t"
"v_mfma_f32_16x16x32_bf16 v[16:19], a[56:59], a[96:99], v[16:19]\n\t"
"ds_read_b64_tr_b16 a[20:21], v251 offset:1032\n\t"
"v_mfma_f32_16x16x32_bf16 v[20:23], a[60:63], a[96:99], v[20:23]\n\t"
"buffer_load_dwordx4 v243, s[40:43], s73 offen offset:1024 lds\n\t"
"v_mfma_f32_16x16x32_bf16 v[64:67], a[64:67], a[96:99], v[64:67]\n\t"
"ds_read_b64_tr_b16 a[22:23], v251 offset:1288\n\t"
"v_mfma_f32_16x16x32_bf16 v[68:71], a[68:71], a[96:99], v[68:71]\n\t"
"buffer_load_dwordx4 v244, s[40:43], s73 offen offset:2048 lds\n\t"
"v_mfma_f32_16x16x32_bf16 v[112:115], a[72:75], a[96:99], v[112:115]\n\t"
"ds_read_b64_tr_b16 a[24:25], v251 offset:1536\n\t"
"v_mfma_f32_16x16x32_bf16 v[116:119], a[76:79], a[96:99], v[116:119]\n\t"
"buffer_load_dwordx4 v245, s[40:43], s73 offen offset:3072 lds\n\t"
"v_mfma_f32_16x16x32_bf16 v[160:163], a[80:83], a[96:99], v[160:163]\n\t"
"ds_read_b64_tr_b16 a[26:27], v251 offset:1792\n\t"
"v_mfma_f32_16x16x32_bf16 v[164:167], a[84:87], a[96:99], v[164:167]\n\t"
"s_add_u32 m0, s75, 118784\n\t"
"ds_read_b64_tr_b16 a[28:29], v251 offset:1544\n\t"
"v_mfma_f32_16x16x32_bf16 v[24:27], a[56:59], a[100:103], v[24:27]\n\t"
"ds_read_b64_tr_b16 a[30:31], v251 offset:1800\n\t"
"v_mfma_f32_16x16x32_bf16 v[28:31], a[60:63], a[100:103], v[28:31]\n\t"
"s_add_u32 s73, s73, 0x80\n\t"
"ds_read_b128 a[36:39], v250 offset:26624\n\t"
"v_mfma_f32_16x16x32_bf16 v[72:75], a[64:67], a[100:103], v[72:75]\n\t"
"ds_read_b128 a[40:43], v250 offset:28672\n\t"
"v_mfma_f32_16x16x32_bf16 v[76:79], a[68:71], a[100:103], v[76:79]\n\t"
"buffer_load_dwordx4 v242, s[40:43], s77 offen offset:0 lds\n\t"
"v_mfma_f32_16x16x32_bf16 v[120:123], a[72:75], a[100:103], v[120:123]\n\t"
"ds_read_b128 a[44:47], v250 offset:30720\n\t"
"v_mfma_f32_16x16x32_bf16 v[124:127], a[76:79], a[100:103], v[124:127]\n\t"
"buffer_load_dwordx4 v243, s[40:43], s77 offen offset:1024 lds\n\t"
"v_mfma_f32_16x16x32_bf16 v[168:171], a[80:83], a[100:103], v[168:171]\n\t"
"ds_read_b128 a[48:51], v250 offset:32768\n\t"
"v_mfma_f32_16x16x32_bf16 v[172:175], a[84:87], a[100:103], v[172:175]\n\t"
"s_cmp_eq_u32 s73, 0x400\n\t"
"ds_read_b128 a[52:55], v250 offset:34816\n\t"
"v_mfma_f32_16x16x32_bf16 v[32:35], a[56:59], a[104:107], v[32:35]\n\t"
"v_mfma_f32_16x16x32_bf16 v[36:39], a[60:63], a[104:107], v[36:39]\n\t"
"s_cselect_b32 s73, 0x80, s73\n\t"
"v_mfma_f32_16x16x32_bf16 v[80:83], a[64:67], a[104:107], v[80:83]\n\t"
"v_mfma_f32_16x16x32_bf16 v[84:87], a[68:71], a[104:107], v[84:87]\n\t"
"s_mov_b32 m0, s56\n\t"
"v_mfma_f32_16x16x32_bf16 v[128:131], a[72:75], a[104:107], v[128:131]\n\t"
"v_mfma_f32_16x16x32_bf16 v[132:135], a[76:79], a[104:107], v[132:135]\n\t"
"buffer_load_dwordx4 v223, s[36:39], s54 offen offset:0 nt lds\n\t"
"v_mfma_f32_16x16x32_bf16 v[176:179], a[80:83], a[104:107], v[176:179]\n\t"
"v_mfma_f32_16x16x32_bf16 v[180:183], a[84:87], a[104:107], v[180:183]\n\t"
"buffer_load_dwordx4 v224, s[36:39], s54 offen offset:1024 nt lds\n\t"
"v_mfma_f32_16x16x32_bf16 v[40:43], a[56:59], a[108:111], v[40:43]\n\t"
"v_mfma_f32_16x16x32_bf16 v[44:47], a[60:63], a[108:111], v[44:47]\n\t"
"v_mfma_f32_16x16x32_bf16 v[88:91], a[64:67], a[108:111], v[88:91]\n\t"
"v_mfma_f32_16x16x32_bf16 v[92:95], a[68:71], a[108:111], v[92:95]\n\t"
"v_mfma_f32_16x16x32_bf16 v[136:139], a[72:75], a[108:111], v[136:139]\n\t"
"v_mfma_f32_16x16x32_bf16 v[140:143], a[76:79], a[108:111], v[140:143]\n\t"
"v_mfma_f32_16x16x32_bf16 v[184:187], a[80:83], a[108:111], v[184:187]\n\t"
"v_mfma_f32_16x16x32_bf16 v[188:191], a[84:87], a[108:111], v[188:191]\n\t"
"s_waitcnt vmcnt(14) lgkmcnt(0)\n\t"
"s_barrier\n\t"
"v_mfma_f32_16x16x32_bf16 v[0:3], a[0:3], a[32:35], v[0:3]\n\t"
"s_add_u32 m0, s76, 32768\n\t"
"ds_read_b128 a[88:91], v250 offset:25088\n\t"
"v_mfma_f32_16x16x32_bf16 v[4:7], a[4:7], a[32:35], v[4:7]\n\t"
"ds_read_b64_tr_b16 a[56:57], v251 offset:16384\n\t"
"v_mfma_f32_16x16x32_bf16 v[48:51], a[8:11], a[32:35], v[48:51]\n\t"
"ds_read_b64_tr_b16 a[58:59], v251 offset:16640\n\t"
"v_mfma_f32_16x16x32_bf16 v[52:55], a[12:15], a[32:35], v[52:55]\n\t"
"buffer_load_dwordx4 v246, s[44:47], s74 offen offset:0 lds\n\t"
"v_mfma_f32_16x16x32_bf16 v[96:99], a[16:19], a[32:35], v[96:99]\n\t"
"ds_read_b64_tr_b16 a[60:61], v251 offset:16392\n\t"
"v_mfma_f32_16x16x32_bf16 v[100:103], a[20:23], a[32:35], v[100:103]\n\t"
"ds_read_b64_tr_b16 a[62:63], v251 offset:16648\n\t"
"v_mfma_f32_16x16x32_bf16 v[144:147], a[24:27], a[32:35], v[144:147]\n\t"
"buffer_load_dwordx4 v247, s[44:47], s74 offen offset:1024 lds\n\t"
"v_mfma_f32_16x16x32_bf16 v[148:151], a[28:31], a[32:35], v[148:151]\n\t"
"ds_read_b64_tr_b16 a[64:65], v251 offset:16896\n\t"
"v_mfma_f32_16x16x32_bf16 v[8:11], a[0:3], a[36:39], v[8:11]\n\t"
"ds_read_b64_tr_b16 a[66:67], v251 offset:17152\n\t"
"v_mfma_f32_16x16x32_bf16 v[12:15], a[4:7], a[36:39], v[12:15]\n\t"
"ds_read_b64_tr_b16 a[68:69], v251 offset:16904\n\t"
"v_mfma_f32_16x16x32_bf16 v[56:59], a[8:11], a[36:39], v[56:59]\n\t"
"buffer_load_dwordx4 v248, s[44:47], s74 offen offset:2048 lds\n\t"
"v_mfma_f32_16x16x32_bf16 v[60:63], a[12:15], a[36:39], v[60:63]\n\t"
"ds_read_b64_tr_b16 a[70:71], v251 offset:17160\n\t"
"v_mfma_f32_16x16x32_bf16 v[104:107], a[16:19], a[36:39], v[104:107]\n\t"
"ds_read_b64_tr_b16 a[72:73], v251 offset:17408\n\t"
"v_mfma_f32_16x16x32_bf16 v[108:111], a[20:23], a[36:39], v[108:111]\n\t"
"buffer_load_dwordx4 v249, s[44:47], s74 offen offset:3072 lds\n\t"
"v_mfma_f32_16x16x32_bf16 v[152:155], a[24:27], a[36:39], v[152:155]\n\t"
"ds_read_b64_tr_b16 a[74:75], v251 offset:17664\n\t"
"v_mfma_f32_16x16x32_bf16 v[156:159], a[28:31], a[36:39], v[156:159]\n\t"
"ds_read_b64_tr_b16 a[76:77], v251 offset:17416\n\t"
"v_mfma_f32_16x16x32_bf16 v[16:19], a[0:3], a[40:43], v[16:19]\n\t"
"ds_read_b64_tr_b16 a[78:79], v251 offset:17672\n\t"
"v_mfma_f32_16x16x32_bf16 v[20:23], a[4:7], a[40:43], v[20:23]\n\t"
"s_add_u32 s74, s74, 262144\n\t"
"v_mfma_f32_16x16x32_bf16 v[64:67], a[8:11], a[40:43], v[64:67]\n\t"
"ds_read_b64_tr_b16 a[80:81], v251 offset:17920\n\t"
"v_mfma_f32_16x16x32_bf16 v[68:71], a[12:15], a[40:43], v[68:71]\n\t"
"ds_read_b64_tr_b16 a[82:83], v251 offset:18176\n\t"
"v_mfma_f32_16x16x32_bf16 v[112:115], a[16:19], a[40:43], v[112:115]\n\t"
"s_sub_u32 s69, s74, 3670016\n\t"
"v_mfma_f32_16x16x32_bf16 v[116:119], a[20:23], a[40:43], v[116:119]\n\t"
"ds_read_b64_tr_b16 a[84:85], v251 offset:17928\n\t"
"v_mfma_f32_16x16x32_bf16 v[160:163], a[24:27], a[40:43], v[160:163]\n\t"
"ds_read_b64_tr_b16 a[86:87], v251 offset:18184\n\t"
"v_mfma_f32_16x16x32_bf16 v[164:167], a[28:31], a[40:43], v[164:167]\n\t"
"s_cmp_ge_u32 s74, 0x400000\n\t"
"ds_read_b128 a[92:95], v250 offset:27136\n\t"
"v_mfma_f32_16x16x32_bf16 v[24:27], a[0:3], a[44:47], v[24:27]\n\t"
"v_mfma_f32_16x16x32_bf16 v[28:31], a[4:7], a[44:47], v[28:31]\n\t"
"ds_read_b128 a[96:99], v250 offset:29184\n\t"
"v_mfma_f32_16x16x32_bf16 v[72:75], a[8:11], a[44:47], v[72:75]\n\t"
"ds_read_b128 a[100:103], v250 offset:31232\n\t"
"v_mfma_f32_16x16x32_bf16 v[76:79], a[12:15], a[44:47], v[76:79]\n\t"
"s_cselect_b32 s74, s69, s74\n\t"
"v_mfma_f32_16x16x32_bf16 v[120:123], a[16:19], a[44:47], v[120:123]\n\t"
"ds_read_b128 a[104:107], v250 offset:33280\n\t"
"v_mfma_f32_16x16x32_bf16 v[124:127], a[20:23], a[44:47], v[124:127]\n\t"
"ds_read_b128 a[108:111], v250 offset:35328\n\t"
"v_mfma_f32_16x16x32_bf16 v[168:171], a[24:27], a[44:47], v[168:171]\n\t"
"s_mov_b32 m0, s56\n\t"
"v_mfma_f32_16x16x32_bf16 v[172:175], a[28:31], a[44:47], v[172:175]\n\t"
"v_mfma_f32_16x16x32_bf16 v[32:35], a[0:3], a[48:51], v[32:35]\n\t"
"v_mfma_f32_16x16x32_bf16 v[36:39], a[4:7], a[48:51], v[36:39]\n\t"
"v_mfma_f32_16x16x32_bf16 v[80:83], a[8:11], a[48:51], v[80:83]\n\t"
"buffer_load_dwordx4 v225, s[36:39], s54 offen offset:2048 nt lds\n\t"
"v_mfma_f32_16x16x32_bf16 v[84:87], a[12:15], a[48:51], v[84:87]\n\t"
"v_mfma_f32_16x16x32_bf16 v[128:131], a[16:19], a[48:51], v[128:131]\n\t"
"v_mfma_f32_16x16x32_bf16 v[132:135], a[20:23], a[48:51], v[132:135]\n\t"
"buffer_load_dwordx4 v226, s[36:39], s54 offen offset:3072 nt lds\n\t"
"v_mfma_f32_16x16x32_bf16 v[176:179], a[24:27], a[48:51], v[176:179]\n\t"
"v_mfma_f32_16x16x32_bf16 v[180:183], a[28:31], a[48:51], v[180:183]\n\t"
"v_mfma_f32_16x16x32_bf16 v[40:43], a[0:3], a[52:55], v[40:43]\n\t"
"v_mfma_f32_16x16x32_bf16 v[44:47], a[4:7], a[52:55], v[44:47]\n\t"
"v_mfma_f32_16x16x32_bf16 v[88:91], a[8:11], a[52:55], v[88:91]\n\t"
"v_mfma_f32_16x16x32_bf16 v[92:95], a[12:15], a[52:55], v[92:95]\n\t"
"v_mfma_f32_16x16x32_bf16 v[136:139], a[16:19], a[52:55], v[136:139]\n\t"
"v_mfma_f32_16x16x32_bf16 v[140:143], a[20:23], a[52:55], v[140:143]\n\t"
"v_mfma_f32_16x16x32_bf16 v[184:187], a[24:27], a[52:55], v[184:187]\n\t"
"v_mfma_f32_16x16x32_bf16 v[188:191], a[28:31], a[52:55], v[188:191]\n\t"
"s_waitcnt vmcnt(8) lgkmcnt(0)\n\t"
"s_barrier\n\t"
"v_mfma_f32_16x16x32_bf16 v[0:3], a[56:59], a[88:91], v[0:3]\n\t"
"s_add_u32 m0, s76, 49152\n\t"
"ds_read_b128 a[32:35], v250 offset:0\n\t"
"v_mfma_f32_16x16x32_bf16 v[4:7], a[60:63], a[88:91], v[4:7]\n\t"
"buffer_load_dwordx4 v246, s[44:47], s74 offen offset:0 lds\n\t"
"v_mfma_f32_16x16x32_bf16 v[48:51], a[64:67], a[88:91], v[48:51]\n\t"
"ds_read_b64_tr_b16 a[0:1], v251 offset:32768\n\t"
"v_mfma_f32_16x16x32_bf16 v[52:55], a[68:71], a[88:91], v[52:55]\n\t"
"buffer_load_dwordx4 v247, s[44:47], s74 offen offset:1024 lds\n\t"
"v_mfma_f32_16x16x32_bf16 v[96:99], a[72:75], a[88:91], v[96:99]\n\t"
"ds_read_b64_tr_b16 a[2:3], v251 offset:33024\n\t"
"v_mfma_f32_16x16x32_bf16 v[100:103], a[76:79], a[88:91], v[100:103]\n\t"
"buffer_load_dwordx4 v248, s[44:47], s74 offen offset:2048 lds\n\t"
"v_mfma_f32_16x16x32_bf16 v[144:147], a[80:83], a[88:91], v[144:147]\n\t"
"ds_read_b64_tr_b16 a[4:5], v251 offset:32776\n\t"
"v_mfma_f32_16x16x32_bf16 v[148:151], a[84:87], a[88:91], v[148:151]\n\t"
"buffer_load_dwordx4 v249, s[44:47], s74 offen offset:3072 lds\n\t"
"v_mfma_f32_16x16x32_bf16 v[8:11], a[56:59], a[92:95], v[8:11]\n\t"
"ds_read_b64_tr_b16 a[6:7], v251 offset:33032\n\t"
"v_mfma_f32_16x16x32_bf16 v[12:15], a[60:63], a[92:95], v[12:15]\n\t"
"s_add_u32 s74, s74, 262144\n\t"
"ds_read_b64_tr_b16 a[8:9], v251 offset:33280\n\t"
"v_mfma_f32_16x16x32_bf16 v[56:59], a[64:67], a[92:95], v[56:59]\n\t"
"ds_read_b64_tr_b16 a[10:11], v251 offset:33536\n\t"
"v_mfma_f32_16x16x32_bf16 v[60:63], a[68:71], a[92:95], v[60:63]\n\t"
"s_add_u32 m0, s75, 139264\n\t"
"ds_read_b64_tr_b16 a[12:13], v251 offset:33288\n\t"
"v_mfma_f32_16x16x32_bf16 v[104:107], a[72:75], a[92:95], v[104:107]\n\t"
"ds_read_b64_tr_b16 a[14:15], v251 offset:33544\n\t"
"v_mfma_f32_16x16x32_bf16 v[108:111], a[76:79], a[92:95], v[108:111]\n\t"
"s_add_u32 s77, s73, 0x8000\n\t"
"ds_read_b64_tr_b16 a[16:17], v251 offset:33792\n\t"
"v_mfma_f32_16x16x32_bf16 v[152:155], a[80:83], a[92:95], v[152:155]\n\t"
"ds_read_b64_tr_b16 a[18:19], v251 offset:34048\n\t"
"v_mfma_f32_16x16x32_bf16 v[156:159], a[84:87], a[92:95], v[156:159]\n\t"
"buffer_load_dwordx4 v242, s[40:43], s73 offen offset:0 lds\n\t"
"v_mfma_f32_16x16x32_bf16 v[16:19], a[56:59], a[96:99], v[16:19]\n\t"
"ds_read_b64_tr_b16 a[20:21], v251 offset:33800\n\t"
"v_mfma_f32_16x16x32_bf16 v[20:23], a[60:63], a[96:99], v[20:23]\n\t"
"buffer_load_dwordx4 v243, s[40:43], s73 offen offset:1024 lds\n\t"
"v_mfma_f32_16x16x32_bf16 v[64:67], a[64:67], a[96:99], v[64:67]\n\t"
"ds_read_b64_tr_b16 a[22:23], v251 offset:34056\n\t"
"v_mfma_f32_16x16x32_bf16 v[68:71], a[68:71], a[96:99], v[68:71]\n\t"
"buffer_load_dwordx4 v244, s[40:43], s73 offen offset:2048 lds\n\t"
"v_mfma_f32_16x16x32_bf16 v[112:115], a[72:75], a[96:99], v[112:115]\n\t"
"ds_read_b64_tr_b16 a[24:25], v251 offset:34304\n\t"
"v_mfma_f32_16x16x32_bf16 v[116:119], a[76:79], a[96:99], v[116:119]\n\t"
"buffer_load_dwordx4 v245, s[40:43], s73 offen offset:3072 lds\n\t"
"v_mfma_f32_16x16x32_bf16 v[160:163], a[80:83], a[96:99], v[160:163]\n\t"
"ds_read_b64_tr_b16 a[26:27], v251 offset:34560\n\t"
"v_mfma_f32_16x16x32_bf16 v[164:167], a[84:87], a[96:99], v[164:167]\n\t"
"s_add_u32 m0, s75, 143360\n\t"
"ds_read_b64_tr_b16 a[28:29], v251 offset:34312\n\t"
"v_mfma_f32_16x16x32_bf16 v[24:27], a[56:59], a[100:103], v[24:27]\n\t"
"ds_read_b64_tr_b16 a[30:31], v251 offset:34568\n\t"
"v_mfma_f32_16x16x32_bf16 v[28:31], a[60:63], a[100:103], v[28:31]\n\t"
"s_add_u32 s73, s73, 0x80\n\t"
"ds_read_b128 a[36:39], v250 offset:2048\n\t"
"v_mfma_f32_16x16x32_bf16 v[72:75], a[64:67], a[100:103], v[72:75]\n\t"
"ds_read_b128 a[40:43], v250 offset:4096\n\t"
"v_mfma_f32_16x16x32_bf16 v[76:79], a[68:71], a[100:103], v[76:79]\n\t"
"buffer_load_dwordx4 v242, s[40:43], s77 offen offset:0 lds\n\t"
"v_mfma_f32_16x16x32_bf16 v[120:123], a[72:75], a[100:103], v[120:123]\n\t"
"ds_read_b128 a[44:47], v250 offset:6144\n\t"
"v_mfma_f32_16x16x32_bf16 v[124:127], a[76:79], a[100:103], v[124:127]\n\t"
"buffer_load_dwordx4 v243, s[40:43], s77 offen offset:1024 lds\n\t"
"v_mfma_f32_16x16x32_bf16 v[168:171], a[80:83], a[100:103], v[168:171]\n\t"
"ds_read_b128 a[48:51], v250 offset:8192\n\t"
"v_mfma_f32_16x16x32_bf16 v[172:175], a[84:87], a[100:103], v[172:175]\n\t"
"s_cmp_eq_u32 s73, 0x400\n\t"
"ds_read_b128 a[52:55], v250 offset:10240\n\t"
"v_mfma_f32_16x16x32_bf16 v[32:35], a[56:59], a[104:107], v[32:35]\n\t"
"v_mfma_f32_16x16x32_bf16 v[36:39], a[60:63], a[104:107], v[36:39]\n\t"
"s_cselect_b32 s73, 0x80, s73\n\t"
"v_mfma_f32_16x16x32_bf16 v[80:83], a[64:67], a[104:107], v[80:83]\n\t"
"v_mfma_f32_16x16x32_bf16 v[84:87], a[68:71], a[104:107], v[84:87]\n\t"
"s_add_u32 m0, s56, 0x1000\n\t"
"v_mfma_f32_16x16x32_bf16 v[128:131], a[72:75], a[104:107], v[128:131]\n\t"
"v_mfma_f32_16x16x32_bf16 v[132:135], a[76:79], a[104:107], v[132:135]\n\t"
"buffer_load_dwordx4 v223, s[36:39], s55 offen offset:0 nt lds\n\t"
"v_mfma_f32_16x16x32_bf16 v[176:179], a[80:83], a[104:107], v[176:179]\n\t"
"v_mfma_f32_16x16x32_bf16 v[180:183], a[84:87], a[104:107], v[180:183]\n\t"
"buffer_load_dwordx4 v224, s[36:39], s55 offen offset:1024 nt lds\n\t"
"v_mfma_f32_16x16x32_bf16 v[40:43], a[56:59], a[108:111], v[40:43]\n\t"
"v_mfma_f32_16x16x32_bf16 v[44:47], a[60:63], a[108:111], v[44:47]\n\t"
"v_mfma_f32_16x16x32_bf16 v[88:91], a[64:67], a[108:111], v[88:91]\n\t"
"v_mfma_f32_16x16x32_bf16 v[92:95], a[68:71], a[108:111], v[92:95]\n\t"
"v_mfma_f32_16x16x32_bf16 v[136:139], a[72:75], a[108:111], v[136:139]\n\t"
"v_mfma_f32_16x16x32_bf16 v[140:143], a[76:79], a[108:111], v[140:143]\n\t"
"v_mfma_f32_16x16x32_bf16 v[184:187], a[80:83], a[108:111], v[184:187]\n\t"
"v_mfma_f32_16x16x32_bf16 v[188:191], a[84:87], a[108:111], v[188:191]\n\t"
"s_waitcnt vmcnt(14) lgkmcnt(0)\n\t"
"s_barrier\n\t"
"v_mfma_f32_16x16x32_bf16 v[0:3], a[0:3], a[32:35], v[0:3]\n\t"
"s_add_u32 m0, s76, 65536\n\t"
"ds_read_b128 a[88:91], v250 offset:512\n\t"
"v_mfma_f32_16x16x32_bf16 v[4:7], a[4:7], a[32:35], v[4:7]\n\t"
"ds_read_b64_tr_b16 a[56:57], v251 offset:0\n\t"
"v_mfma_f32_16x16x32_bf16 v[48:51], a[8:11], a[32:35], v[48:51]\n\t"
"buffer_load_dwordx4 v246, s[44:47], s74 offen offset:0 lds\n\t"
"v_mfma_f32_16x16x32_bf16 v[52:55], a[12:15], a[32:35], v[52:55]\n\t"
"ds_read_b64_tr_b16 a[58:59], v251 offset:256\n\t"
"v_mfma_f32_16x16x32_bf16 v[96:99], a[16:19], a[32:35], v[96:99]\n\t"
"ds_read_b64_tr_b16 a[60:61], v251 offset:8\n\t"
"v_mfma_f32_16x16x32_bf16 v[100:103], a[20:23], a[32:35], v[100:103]\n\t"
"buffer_load_dwordx4 v247, s[44:47], s74 offen offset:1024 lds\n\t"
"v_mfma_f32_16x16x32_bf16 v[144:147], a[24:27], a[32:35], v[144:147]\n\t"
"ds_read_b64_tr_b16 a[62:63], v251 offset:264\n\t"
"v_mfma_f32_16x16x32_bf16 v[148:151], a[28:31], a[32:35], v[148:151]\n\t"
"ds_read_b64_tr_b16 a[64:65], v251 offset:512\n\t"
"v_mfma_f32_16x16x32_bf16 v[8:11], a[0:3], a[36:39], v[8:11]\n\t"
"buffer_load_dwordx4 v248, s[44:47], s74 offen offset:2048 lds\n\t"
"v_mfma_f32_16x16x32_bf16 v[12:15], a[4:7], a[36:39], v[12:15]\n\t"
"ds_read_b64_tr_b16 a[66:67], v251 offset:768\n\t"
"v_mfma_f32_16x16x32_bf16 v[56:59], a[8:11], a[36:39], v[56:59]\n\t"
"buffer_load_dwordx4 v249, s[44:47], s74 offen offset:3072 lds\n\t"
"v_mfma_f32_16x16x32_bf16 v[60:63], a[12:15], a[36:39], v[60:63]\n\t"
"ds_read_b64_tr_b16 a[68:69], v251 offset:520\n\t"
"v_mfma_f32_16x16x32_bf16 v[104:107], a[16:19], a[36:39], v[104:107]\n\t"
"ds_read_b64_tr_b16 a[70:71], v251 offset:776\n\t"
"v_mfma_f32_16x16x32_bf16 v[108:111], a[20:23], a[36:39], v[108:111]\n\t"
"s_add_u32 s74, s74, 262144\n\t"
"ds_read_b64_tr_b16 a[72:73], v251 offset:1024\n\t"
"v_mfma_f32_16x16x32_bf16 v[152:155], a[24:27], a[36:39], v[152:155]\n\t"
"ds_read_b64_tr_b16 a[74:75], v251 offset:1280\n\t"
"v_mfma_f32_16x16x32_bf16 v[156:159], a[28:31], a[36:39], v[156:159]\n\t"
"ds_read_b64_tr_b16 a[76:77], v251 offset:1032\n\t"
"v_mfma_f32_16x16x32_bf16 v[16:19], a[0:3], a[40:43], v[16:19]\n\t"
"s_sub_u32 s69, s74, 3670016\n\t"
"ds_read_b64_tr_b16 a[78:79], v251 offset:1288\n\t"
"v_mfma_f32_16x16x32_bf16 v[20:23], a[4:7], a[40:43], v[20:23]\n\t"
"v_mfma_f32_16x16x32_bf16 v[64:67], a[8:11], a[40:43], v[64:67]\n\t"
"ds_read_b64_tr_b16 a[80:81], v251 offset:1536\n\t"
"v_mfma_f32_16x16x32_bf16 v[68:71], a[12:15], a[40:43], v[68:71]\n\t"
"s_cmp_ge_u32 s74, 0x400000\n\t"
"ds_read_b64_tr_b16 a[82:83], v251 offset:1792\n\t"
"v_mfma_f32_16x16x32_bf16 v[112:115], a[16:19], a[40:43], v[112:115]\n\t"
"v_mfma_f32_16x16x32_bf16 v[116:119], a[20:23], a[40:43], v[116:119]\n\t"
"s_cselect_b32 s74, s69, s74\n\t"
"ds_read_b64_tr_b16 a[84:85], v251 offset:1544\n\t"
"v_mfma_f32_16x16x32_bf16 v[160:163], a[24:27], a[40:43], v[160:163]\n\t"
"ds_read_b64_tr_b16 a[86:87], v251 offset:1800\n\t"
"v_mfma_f32_16x16x32_bf16 v[164:167], a[28:31], a[40:43], v[164:167]\n\t"
"ds_read_b128 a[92:95], v250 offset:2560\n\t"
"v_mfma_f32_16x16x32_bf16 v[24:27], a[0:3], a[44:47], v[24:27]\n\t"
"s_add_u32 m0, s56, 0x1000\n\t"
"v_mfma_f32_16x16x32_bf16 v[28:31], a[4:7], a[44:47], v[28:31]\n\t"
"ds_read_b128 a[96:99], v250 offset:4608\n\t"
"v_mfma_f32_16x16x32_bf16 v[72:75], a[8:11], a[44:47], v[72:75]\n\t"
"ds_read_b128 a[100:103], v250 offset:6656\n\t"
"v_mfma_f32_16x16x32_bf16 v[76:79], a[12:15], a[44:47], v[76:79]\n\t"
"buffer_load_dwordx4 v225, s[36:39], s55 offen offset:2048 nt lds\n\t"
"v_mfma_f32_16x16x32_bf16 v[120:123], a[16:19], a[44:47], v[120:123]\n\t"
"ds_read_b128 a[104:107], v250 offset:8704\n\t"
"v_mfma_f32_16x16x32_bf16 v[124:127], a[20:23], a[44:47], v[124:127]\n\t"
"ds_read_b128 a[108:111], v250 offset:10752\n\t"
"v_mfma_f32_16x16x32_bf16 v[168:171], a[24:27], a[44:47], v[168:171]\n\t"
"buffer_load_dwordx4 v226, s[36:39], s55 offen offset:3072 nt lds\n\t"
"v_mfma_f32_16x16x32_bf16 v[172:175], a[28:31], a[44:47], v[172:175]\n\t"
"v_mfma_f32_16x16x32_bf16 v[32:35], a[0:3], a[48:51], v[32:35]\n\t"
"s_add_u32 s54, s54, 0x2000\n\t"
"v_mfma_f32_16x16x32_bf16 v[36:39], a[4:7], a[48:51], v[36:39]\n\t"
"v_mfma_f32_16x16x32_bf16 v[80:83], a[8:11], a[48:51], v[80:83]\n\t"
"v_mfma_f32_16x16x32_bf16 v[84:87], a[12:15], a[48:51], v[84:87]\n\t"
"s_add_u32 s55, s55, 0x2000\n\t"
"v_mfma_f32_16x16x32_bf16 v[128:131], a[16:19], a[48:51], v[128:131]\n\t"
"v_mfma_f32_16x16x32_bf16 v[132:135], a[20:23], a[48:51], v[132:135]\n\t"
"v_mfma_f32_16x16x32_bf16 v[176:179], a[24:27], a[48:51], v[176:179]\n\t"
"s_add_u32 s56, s56, 0x4000\n\t"
"v_mfma_f32_16x16x32_bf16 v[180:183], a[28:31], a[48:51], v[180:183]\n\t"
"v_mfma_f32_16x16x32_bf16 v[40:43], a[0:3], a[52:55], v[40:43]\n\t"
"v_mfma_f32_16x16x32_bf16 v[44:47], a[4:7], a[52:55], v[44:47]\n\t"
"v_mfma_f32_16x16x32_bf16 v[88:91], a[8:11], a[52:55], v[88:91]\n\t"
"v_mfma_f32_16x16x32_bf16 v[92:95], a[12:15], a[52:55], v[92:95]\n\t"
"v_mfma_f32_16x16x32_bf16 v[136:139], a[16:19], a[52:55], v[136:139]\n\t"
"v_mfma_f32_16x16x32_bf16 v[140:143], a[20:23], a[52:55], v[140:143]\n\t"
"v_mfma_f32_16x16x32_bf16 v[184:187], a[24:27], a[52:55], v[184:187]\n\t"
"v_mfma_f32_16x16x32_bf16 v[188:191], a[28:31], a[52:55], v[188:191]\n\t"
"s_waitcnt vmcnt(8) lgkmcnt(0)\n\t"
"s_barrier\n\t"
"v_mfma_f32_16x16x32_bf16 v[0:3], a[56:59], a[88:91], v[0:3]\n\t"
"s_mov_b32 m0, s56\n\t"
"ds_read_b128 a[32:35], v250 offset:24576\n\t"
"v_mfma_f32_16x16x32_bf16 v[4:7], a[60:63], a[88:91], v[4:7]\n\t"
"ds_read_b64_tr_b16 a[0:1], v251 offset:16384\n\t"
"v_mfma_f32_16x16x32_bf16 v[48:51], a[64:67], a[88:91], v[48:51]\n\t"
"buffer_load_dwordx4 v223, s[36:39], s54 offen offset:0 nt lds\n\t"
"v_mfma_f32_16x16x32_bf16 v[52:55], a[68:71], a[88:91], v[52:55]\n\t"
"ds_read_b64_tr_b16 a[2:3], v251 offset:16640\n\t"
"v_mfma_f32_16x16x32_bf16 v[96:99], a[72:75], a[88:91], v[96:99]\n\t"
"ds_read_b64_tr_b16 a[4:5], v251 offset:16392\n\t"
"v_mfma_f32_16x16x32_bf16 v[100:103], a[76:79], a[88:91], v[100:103]\n\t"
"buffer_load_dwordx4 v224, s[36:39], s54 offen offset:1024 nt lds\n\t"
"v_mfma_f32_16x16x32_bf16 v[144:147], a[80:83], a[88:91], v[144:147]\n\t"
"ds_read_b64_tr_b16 a[6:7], v251 offset:16648\n\t"
"v_mfma_f32_16x16x32_bf16 v[148:151], a[84:87], a[88:91], v[148:151]\n\t"
"ds_read_b64_tr_b16 a[8:9], v251 offset:16896\n\t"
"v_mfma_f32_16x16x32_bf16 v[8:11], a[56:59], a[92:95], v[8:11]\n\t"
"buffer_load_dwordx4 v225, s[36:39], s54 offen offset:2048 nt lds\n\t"
"v_mfma_f32_16x16x32_bf16 v[12:15], a[60:63], a[92:95], v[12:15]\n\t"
"ds_read_b64_tr_b16 a[10:11], v251 offset:17152\n\t"
"v_mfma_f32_16x16x32_bf16 v[56:59], a[64:67], a[92:95], v[56:59]\n\t"
"buffer_load_dwordx4 v226, s[36:39], s54 offen offset:3072 nt lds\n\t"
"v_mfma_f32_16x16x32_bf16 v[60:63], a[68:71], a[92:95], v[60:63]\n\t"
"ds_read_b64_tr_b16 a[12:13], v251 offset:16904\n\t"
"v_mfma_f32_16x16x32_bf16 v[104:107], a[72:75], a[92:95], v[104:107]\n\t"
"ds_read_b64_tr_b16 a[14:15], v251 offset:17160\n\t"
"v_mfma_f32_16x16x32_bf16 v[108:111], a[76:79], a[92:95], v[108:111]\n\t"
"s_add_u32 m0, s56, 0x1000\n\t"
"ds_read_b64_tr_b16 a[16:17], v251 offset:17408\n\t"
"v_mfma_f32_16x16x32_bf16 v[152:155], a[80:83], a[92:95], v[152:155]\n\t"
"ds_read_b64_tr_b16 a[18:19], v251 offset:17664\n\t"
"v_mfma_f32_16x16x32_bf16 v[156:159], a[84:87], a[92:95], v[156:159]\n\t"
"ds_read_b64_tr_b16 a[20:21], v251 offset:17416\n\t"
"v_mfma_f32_16x16x32_bf16 v[16:19], a[56:59], a[96:99], v[16:19]\n\t"
"s_add_u32 s54, s54, 0x2000\n\t"
"ds_read_b64_tr_b16 a[22:23], v251 offset:17672\n\t"
"v_mfma_f32_16x16x32_bf16 v[20:23], a[60:63], a[96:99], v[20:23]\n\t"
"v_mfma_f32_16x16x32_bf16 v[64:67], a[64:67], a[96:99], v[64:67]\n\t"
"ds_read_b64_tr_b16 a[24:25], v251 offset:17920\n\t"
"v_mfma_f32_16x16x32_bf16 v[68:71], a[68:71], a[96:99], v[68:71]\n\t"
"buffer_load_dwordx4 v223, s[36:39], s55 offen offset:0 nt lds\n\t"
"v_mfma_f32_16x16x32_bf16 v[112:115], a[72:75], a[96:99], v[112:115]\n\t"
"ds_read_b64_tr_b16 a[26:27], v251 offset:18176\n\t"
"v_mfma_f32_16x16x32_bf16 v[116:119], a[76:79], a[96:99], v[116:119]\n\t"
"buffer_load_dwordx4 v224, s[36:39], s55 offen offset:1024 nt lds\n\t"
"v_mfma_f32_16x16x32_bf16 v[160:163], a[80:83], a[96:99], v[160:163]\n\t"
"ds_read_b64_tr_b16 a[28:29], v251 offset:17928\n\t"
"v_mfma_f32_16x16x32_bf16 v[164:167], a[84:87], a[96:99], v[164:167]\n\t"
"ds_read_b64_tr_b16 a[30:31], v251 offset:18184\n\t"
"v_mfma_f32_16x16x32_bf16 v[24:27], a[56:59], a[100:103], v[24:27]\n\t"
"buffer_load_dwordx4 v225, s[36:39], s55 offen offset:2048 nt lds\n\t"
"v_mfma_f32_16x16x32_bf16 v[28:31], a[60:63], a[100:103], v[28:31]\n\t"
"ds_read_b128 a[36:39], v250 offset:26624\n\t"
"v_mfma_f32_16x16x32_bf16 v[72:75], a[64:67], a[100:103], v[72:75]\n\t"
"ds_read_b128 a[40:43], v250 offset:28672\n\t"
"v_mfma_f32_16x16x32_bf16 v[76:79], a[68:71], a[100:103], v[76:79]\n\t"
"buffer_load_dwordx4 v226, s[36:39], s55 offen offset:3072 nt lds\n\t"
"v_mfma_f32_16x16x32_bf16 v[120:123], a[72:75], a[100:103], v[120:123]\n\t"
"ds_read_b128 a[44:47], v250 offset:30720\n\t"
"v_mfma_f32_16x16x32_bf16 v[124:127], a[76:79], a[100:103], v[124:127]\n\t"
"ds_read_b128 a[48:51], v250 offset:32768\n\t"
"v_mfma_f32_16x16x32_bf16 v[168:171], a[80:83], a[100:103], v[168:171]\n\t"
"s_add_u32 s55, s55, 0x2000\n\t"
"ds_read_b128 a[52:55], v250 offset:34816\n\t"
"v_mfma_f32_16x16x32_bf16 v[172:175], a[84:87], a[100:103], v[172:175]\n\t"
"v_mfma_f32_16x16x32_bf16 v[32:35], a[56:59], a[104:107], v[32:35]\n\t"
"s_add_u32 s56, s56, 0x4000\n\t"
"v_mfma_f32_16x16x32_bf16 v[36:39], a[60:63], a[104:107], v[36:39]\n\t"
"v_mfma_f32_16x16x32_bf16 v[80:83], a[64:67], a[104:107], v[80:83]\n\t"
"v_mfma_f32_16x16x32_bf16 v[84:87], a[68:71], a[104:107], v[84:87]\n\t"
"s_cmp_eq_u32 s56, s57\n\t"
"v_mfma_f32_16x16x32_bf16 v[128:131], a[72:75], a[104:107], v[128:131]\n\t"
"v_mfma_f32_16x16x32_bf16 v[132:135], a[76:79], a[104:107], v[132:135]\n\t"
"v_mfma_f32_16x16x32_bf16 v[176:179], a[80:83], a[104:107], v[176:179]\n\t"
"s_cselect_b32 s56, s58, s56\n\t"
"v_mfma_f32_16x16x32_bf16 v[180:183], a[84:87], a[104:107], v[180:183]\n\t"
"v_mfma_f32_16x16x32_bf16 v[40:43], a[56:59], a[108:111], v[40:43]\n\t"
"v_mfma_f32_16x16x32_bf16 v[44:47], a[60:63], a[108:111], v[44:47]\n\t"
"v_mfma_f32_16x16x32_bf16 v[88:91], a[64:67], a[108:111], v[88:91]\n\t"
"v_mfma_f32_16x16x32_bf16 v[92:95], a[68:71], a[108:111], v[92:95]\n\t"
"v_mfma_f32_16x16x32_bf16 v[136:139], a[72:75], a[108:111], v[136:139]\n\t"
"v_mfma_f32_16x16x32_bf16 v[140:143], a[76:79], a[108:111], v[140:143]\n\t"
"v_mfma_f32_16x16x32_bf16 v[184:187], a[80:83], a[108:111], v[184:187]\n\t"
"v_mfma_f32_16x16x32_bf16 v[188:191], a[84:87], a[108:111], v[188:191]\n\t"
"s_waitcnt vmcnt(10) lgkmcnt(0)\n\t"
"s_barrier\n\t"
"v_mfma_f32_16x16x32_bf16 v[0:3], a[0:3], a[32:35], v[0:3]\n\t"
"ds_read_b128 a[88:91], v250 offset:25088\n\t"
"v_mbcnt_lo_u32_b32 v252, -1, 0\n\t"
"v_mbcnt_hi_u32_b32 v252, -1, v252\n\t"
"v_mfma_f32_16x16x32_bf16 v[4:7], a[4:7], a[32:35], v[4:7]\n\t"
"ds_read_b64_tr_b16 a[56:57], v251 offset:32768\n\t"
"v_and_b32_e32 v246, 3, v252\n\t"
"v_lshlrev_b32_e32 v247, 9, v246\n\t"
"v_mfma_f32_16x16x32_bf16 v[48:51], a[8:11], a[32:35], v[48:51]\n\t"
"ds_read_b64_tr_b16 a[58:59], v251 offset:33024\n\t"
"v_lshlrev_b32_e32 v246, 10, v246\n\t"
"v_and_b32_e32 v248, 15, v252\n\t"
"v_mfma_f32_16x16x32_bf16 v[52:55], a[12:15], a[32:35], v[52:55]\n\t"
"v_lshlrev_b32_e32 v248, 2, v248\n\t"
"v_mov_b32_e32 v245, 0\n\t"
"v_mfma_f32_16x16x32_bf16 v[96:99], a[16:19], a[32:35], v[96:99]\n\t"
"ds_read_b64_tr_b16 a[60:61], v251 offset:32776\n\t"
"global_load_dword v253, v246, %13 sc1\n\t"
"global_load_dword v254, v247, %17 sc1\n\t"
"v_mfma_f32_16x16x32_bf16 v[100:103], a[20:23], a[32:35], v[100:103]\n\t"
"ds_read_b64_tr_b16 a[62:63], v251 offset:33032\n\t"
"global_load_dword v241, v248, %16 sc1\n\t"
"s_mov_b64 s[44:45], %10\n\t"
"v_mfma_f32_16x16x32_bf16 v[144:147], a[24:27], a[32:35], v[144:147]\n\t"
"s_and_b32 s45, s45, 0xffff\n\t"
"s_mov_b32 s46, 1047680\n\t"
"v_mfma_f32_16x16x32_bf16 v[148:151], a[28:31], a[32:35], v[148:151]\n\t"
"ds_read_b64_tr_b16 a[64:65], v251 offset:33280\n\t"
"s_mov_b64 s[40:41], %9\n\t"
"s_and_b32 s41, s41, 0xffff\n\t"
"v_mfma_f32_16x16x32_bf16 v[8:11], a[0:3], a[36:39], v[8:11]\n\t"
"ds_read_b64_tr_b16 a[66:67], v251 offset:33536\n\t"
"s_lshl_b32 s72, s52, 12\n\t"
"s_add_u32 s72, s72, s53\n\t"
"v_mfma_f32_16x16x32_bf16 v[12:15], a[4:7], a[36:39], v[12:15]\n\t"
"ds_read_b64_tr_b16 a[68:69], v251 offset:33288\n\t"
"v_bfe_u32 v235, v252, 2, 3\n\t"
"v_lshlrev_b32_e64 v242, 10, v235\n\t"
"v_mfma_f32_16x16x32_bf16 v[56:59], a[8:11], a[36:39], v[56:59]\n\t"
"v_lshrrev_b32_e64 v235, 5, v252\n\t"
"v_lshl_add_u32 v242, v235, 6, v242\n\t"
"v_mfma_f32_16x16x32_bf16 v[60:63], a[12:15], a[36:39], v[60:63]\n\t"
"ds_read_b64_tr_b16 a[70:71], v251 offset:33544\n\t"
"v_and_b32_e64 v236, 3, v252\n\t"
"v_lshl_add_u32 v242, v236, 4, v242\n\t"
"v_mfma_f32_16x16x32_bf16 v[104:107], a[16:19], a[36:39], v[104:107]\n\t"
"ds_read_b64_tr_b16 a[72:73], v251 offset:33792\n\t"
"v_lshlrev_b32_e32 v237, 1, v252\n\t"
"v_and_b32_e32 v237, 32, v237\n\t"
"v_mfma_f32_16x16x32_bf16 v[108:111], a[20:23], a[36:39], v[108:111]\n\t"
"v_xor_b32_e64 v242, v242, v237\n\t"
"v_add_u32_e32 v243, 7168, v242\n\t"
"v_mfma_f32_16x16x32_bf16 v[152:155], a[24:27], a[36:39], v[152:155]\n\t"
"ds_read_b64_tr_b16 a[74:75], v251 offset:34048\n\t"
"v_add_u32_e32 v244, 14336, v242\n\t"
"v_add_u32_e32 v245, 21504, v242\n\t"
"v_mfma_f32_16x16x32_bf16 v[156:159], a[28:31], a[36:39], v[156:159]\n\t"
"ds_read_b64_tr_b16 a[76:77], v251 offset:33800\n\t"
"v_lshlrev_b32_e64 v246, 10, v235\n\t"
"v_bfe_u32 v237, v252, 2, 2\n\t"
"v_mfma_f32_16x16x32_bf16 v[16:19], a[0:3], a[40:43], v[16:19]\n\t"
"ds_read_b64_tr_b16 a[78:79], v251 offset:34056\n\t"
"v_lshl_add_u32 v246, v237, 6, v246\n\t"
"v_bfe_u32 v237, v252, 4, 1\n\t"
"v_mfma_f32_16x16x32_bf16 v[20:23], a[4:7], a[40:43], v[20:23]\n\t"
"v_mul_u32_u24_e32 v237, 288, v237\n\t"
"v_add_u32_e64 v246, v246, v237\n\t"
"v_mfma_f32_16x16x32_bf16 v[64:67], a[8:11], a[40:43], v[64:67]\n\t"
"ds_read_b64_tr_b16 a[80:81], v251 offset:34304\n\t"
"v_lshl_add_u32 v246, v236, 3, v246\n\t"
"v_xor_b32_e64 v247, 32, v246\n\t"
"v_mfma_f32_16x16x32_bf16 v[68:71], a[12:15], a[40:43], v[68:71]\n\t"
"ds_read_b64_tr_b16 a[82:83], v251 offset:34560\n\t"
"v_add_u32_e64 v246, s72, v246\n\t"
"v_add_u32_e32 v248, 65536, v246\n\t"
"v_mfma_f32_16x16x32_bf16 v[112:115], a[16:19], a[40:43], v[112:115]\n\t"
"v_add_u32_e64 v247, s72, v247\n\t"
"v_add_u32_e32 v249, 65536, v247\n\t"
"v_mfma_f32_16x16x32_bf16 v[116:119], a[20:23], a[40:43], v[116:119]\n\t"
"ds_read_b64_tr_b16 a[84:85], v251 offset:34312\n\t"
"v_mfma_f32_16x16x32_bf16 v[160:163], a[24:27], a[40:43], v[160:163]\n\t"
"ds_read_b64_tr_b16 a[86:87], v251 offset:34568\n\t"
"v_mfma_f32_16x16x32_bf16 v[164:167], a[28:31], a[40:43], v[164:167]\n\t"
"ds_read_b128 a[92:95], v250 offset:27136\n\t"
"v_mfma_f32_16x16x32_bf16 v[24:27], a[0:3], a[44:47], v[24:27]\n\t"
"v_mfma_f32_16x16x32_bf16 v[28:31], a[4:7], a[44:47], v[28:31]\n\t"
"ds_read_b128 a[96:99], v250 offset:29184\n\t"
"v_mfma_f32_16x16x32_bf16 v[72:75], a[8:11], a[44:47], v[72:75]\n\t"
"ds_read_b128 a[100:103], v250 offset:31232\n\t"
"v_mfma_f32_16x16x32_bf16 v[76:79], a[12:15], a[44:47], v[76:79]\n\t"
"v_mfma_f32_16x16x32_bf16 v[120:123], a[16:19], a[44:47], v[120:123]\n\t"
"ds_read_b128 a[104:107], v250 offset:33280\n\t"
"v_mfma_f32_16x16x32_bf16 v[124:127], a[20:23], a[44:47], v[124:127]\n\t"
"ds_read_b128 a[108:111], v250 offset:35328\n\t"
"v_mfma_f32_16x16x32_bf16 v[168:171], a[24:27], a[44:47], v[168:171]\n\t"
"v_mfma_f32_16x16x32_bf16 v[172:175], a[28:31], a[44:47], v[172:175]\n\t"
"v_mfma_f32_16x16x32_bf16 v[32:35], a[0:3], a[48:51], v[32:35]\n\t"
"v_mfma_f32_16x16x32_bf16 v[36:39], a[4:7], a[48:51], v[36:39]\n\t"
"v_mfma_f32_16x16x32_bf16 v[80:83], a[8:11], a[48:51], v[80:83]\n\t"
"v_mfma_f32_16x16x32_bf16 v[84:87], a[12:15], a[48:51], v[84:87]\n\t"
"s_cmp_lg_u32 s82, 0\n\t"
"s_cbranch_scc1 P0S%=\n\t"
"s_mov_b32 m0, s56\n\t"
"s_nop 0\n\t"
"buffer_load_dwordx4 v223, s[36:39], s54 offen offset:0 nt lds\n\t"
"buffer_load_dwordx4 v224, s[36:39], s54 offen offset:1024 nt lds\n\t"
"buffer_load_dwordx4 v225, s[36:39], s54 offen offset:2048 nt lds\n\t"
"buffer_load_dwordx4 v226, s[36:39], s54 offen offset:3072 nt lds\n\t"
"s_add_u32 m0, s56, 0x1000\n\t"
"s_add_u32 s54, s54, 0x2000\n\t"
"buffer_load_dwordx4 v223, s[36:39], s55 offen offset:0 nt lds\n\t"
"buffer_load_dwordx4 v224, s[36:39], s55 offen offset:1024 nt lds\n\t"
"buffer_load_dwordx4 v225, s[36:39], s55 offen offset:2048 nt lds\n\t"
"buffer_load_dwordx4 v226, s[36:39], s55 offen offset:3072 nt lds\n\t"
"s_add_u32 s55, s55, 0x2000\n\t"
"s_add_u32 s56, s56, 0x4000\n\t"
"s_cmp_eq_u32 s56, s57\n\t"
"s_cselect_b32 s56, s58, s56\n\t"
"P0S%=:\n\t"
"v_mfma_f32_16x16x32_bf16 v[128:131], a[16:19], a[48:51], v[128:131]\n\t"
"v_mfma_f32_16x16x32_bf16 v[132:135], a[20:23], a[48:51], v[132:135]\n\t"
"v_mfma_f32_16x16x32_bf16 v[176:179], a[24:27], a[48:51], v[176:179]\n\t"
"v_mfma_f32_16x16x32_bf16 v[180:183], a[28:31], a[48:51], v[180:183]\n\t"
"v_mfma_f32_16x16x32_bf16 v[40:43], a[0:3], a[52:55], v[40:43]\n\t"
"v_mfma_f32_16x16x32_bf16 v[44:47], a[4:7], a[52:55], v[44:47]\n\t"
"v_mfma_f32_16x16x32_bf16 v[88:91], a[8:11], a[52:55], v[88:91]\n\t"
"v_mfma_f32_16x16x32_bf16 v[92:95], a[12:15], a[52:55], v[92:95]\n\t"
"v_mfma_f32_16x16x32_bf16 v[136:139], a[16:19], a[52:55], v[136:139]\n\t"
"v_mfma_f32_16x16x32_bf16 v[140:143], a[20:23], a[52:55], v[140:143]\n\t"
"v_mfma_f32_16x16x32_bf16 v[184:187], a[24:27], a[52:55], v[184:187]\n\t"
"v_mfma_f32_16x16x32_bf16 v[188:191], a[28:31], a[52:55], v[188:191]\n\t"
"s_waitcnt lgkmcnt(0)\n\t"
"s_barrier\n\t"
"v_mfma_f32_16x16x32_bf16 v[0:3], a[56:59], a[88:91], v[0:3]\n\t"
"ds_read_b128 a[192:195], v220 offset:0\n\t"
"v_mfma_f32_16x16x32_bf16 v[4:7], a[60:63], a[88:91], v[4:7]\n\t"
"ds_read_b128 a[196:199], v220 offset:512\n\t"
"v_mfma_f32_16x16x32_bf16 v[48:51], a[64:67], a[88:91], v[48:51]\n\t"
"ds_read_b128 a[200:203], v220 offset:1024\n\t"
"v_mfma_f32_16x16x32_bf16 v[52:55], a[68:71], a[88:91], v[52:55]\n\t"
"ds_read_b128 a[204:207], v220 offset:1536\n\t"
"v_mfma_f32_16x16x32_bf16 v[96:99], a[72:75], a[88:91], v[96:99]\n\t"
"v_mfma_f32_16x16x32_bf16 v[100:103], a[76:79], a[88:91], v[100:103]\n\t"
"ds_read_b128 a[208:211], v220 offset:4096\n\t"
"v_mfma_f32_16x16x32_bf16 v[144:147], a[80:83], a[88:91], v[144:147]\n\t"
"ds_read_b128 a[212:215], v220 offset:4608\n\t"
"v_mfma_f32_16x16x32_bf16 v[148:151], a[84:87], a[88:91], v[148:151]\n\t"
"ds_read_b128 a[216:219], v220 offset:5120\n\t"
"v_mfma_f32_16x16x32_bf16 v[8:11], a[56:59], a[92:95], v[8:11]\n\t"
"ds_read_b128 a[220:223], v220 offset:5632\n\t"
"v_mfma_f32_16x16x32_bf16 v[12:15], a[60:63], a[92:95], v[12:15]\n\t"
"v_mfma_f32_16x16x32_bf16 v[56:59], a[64:67], a[92:95], v[56:59]\n\t"
"v_mfma_f32_16x16x32_bf16 v[60:63], a[68:71], a[92:95], v[60:63]\n\t"
"v_mul_f32_e32 v96, s64, v96\n\t"
"v_mul_f32_e32 v97, s64, v97\n\t"
"v_mul_f32_e32 v98, s64, v98\n\t"
"v_mul_f32_e32 v99, s64, v99\n\t"
"v_mfma_f32_16x16x32_bf16 v[104:107], a[72:75], a[92:95], v[104:107]\n\t"
"v_cvt_pk_bf16_f32 v96, v96, v97\n\t"
"v_mul_f32_e32 v100, s64, v100\n\t"
"v_mul_f32_e32 v101, s64, v101\n\t"
"v_mfma_f32_16x16x32_bf16 v[108:111], a[76:79], a[92:95], v[108:111]\n\t"
"v_cvt_pk_bf16_f32 v97, v98, v99\n\t"
"v_mul_f32_e32 v102, s64, v102\n\t"
"v_mul_f32_e32 v103, s64, v103\n\t"
"v_cvt_pk_bf16_f32 v98, v100, v101\n\t"
"v_mfma_f32_16x16x32_bf16 v[152:155], a[80:83], a[92:95], v[152:155]\n\t"
"v_cvt_pk_bf16_f32 v99, v102, v103\n\t"
"v_mul_f32_e32 v144, s64, v144\n\t"
"v_mul_f32_e32 v145, s64, v145\n\t"
"v_mfma_f32_16x16x32_bf16 v[156:159], a[84:87], a[92:95], v[156:159]\n\t"
"v_mul_f32_e32 v146, s64, v146\n\t"
"v_mul_f32_e32 v147, s64, v147\n\t"
"v_cvt_pk_bf16_f32 v144, v144, v145\n\t"
"v_mul_f32_e64 v148, s64, v148\n\t"
"v_mfma_f32_16x16x32_bf16 v[16:19], a[56:59], a[96:99], v[16:19]\n\t"
"v_mul_f32_e64 v149, s64, v149\n\t"
"v_cvt_pk_bf16_f32 v145, v146, v147\n\t"
"v_mul_f32_e64 v150, s64, v150\n\t"
"v_mfma_f32_16x16x32_bf16 v[20:23], a[60:63], a[96:99], v[20:23]\n\t"
"v_mul_f32_e64 v151, s64, v151\n\t"
"v_cvt_pk_bf16_f32 v146, v148, v149\n\t"
"v_cvt_pk_bf16_f32 v147, v150, v151\n\t"
"v_mfma_f32_16x16x32_bf16 v[64:67], a[64:67], a[96:99], v[64:67]\n\t"
"v_mul_f32_e32 v0, s64, v0\n\t"
"v_mul_f32_e32 v1, s64, v1\n\t"
"v_mul_f32_e32 v2, s64, v2\n\t"
"v_mul_f32_e32 v3, s64, v3\n\t"
"v_mfma_f32_16x16x32_bf16 v[68:71], a[68:71], a[96:99], v[68:71]\n\t"
"v_cvt_pk_bf16_f32 v100, v0, v1\n\t"
"v_mul_f32_e32 v4, s64, v4\n\t"
"v_mul_f32_e32 v5, s64, v5\n\t"
"v_mfma_f32_16x16x32_bf16 v[112:115], a[72:75], a[96:99], v[112:115]\n\t"
"v_cvt_pk_bf16_f32 v101, v2, v3\n\t"
"v_mul_f32_e32 v6, s64, v6\n\t"
"v_mul_f32_e32 v7, s64, v7\n\t"
"v_cvt_pk_bf16_f32 v102, v4, v5\n\t"
"v_mfma_f32_16x16x32_bf16 v[116:119], a[76:79], a[96:99], v[116:119]\n\t"
"v_cvt_pk_bf16_f32 v103, v6, v7\n\t"
"v_mul_f32_e32 v48, s64, v48\n\t"
"v_mul_f32_e32 v49, s64, v49\n\t"
"v_mfma_f32_16x16x32_bf16 v[160:163], a[80:83], a[96:99], v[160:163]\n\t"
"v_mul_f32_e32 v50, s64, v50\n\t"
"v_mul_f32_e32 v51, s64, v51\n\t"
"v_cvt_pk_bf16_f32 v148, v48, v49\n\t"
"v_mul_f32_e64 v52, s64, v52\n\t"
"v_mfma_f32_16x16x32_bf16 v[164:167], a[84:87], a[96:99], v[164:167]\n\t"
"v_mul_f32_e64 v53, s64, v53\n\t"
"v_cvt_pk_bf16_f32 v149, v50, v51\n\t"
"v_mul_f32_e64 v54, s64, v54\n\t"
"v_mfma_f32_32x32x16_bf16 a[0:15], v[216:219], v[216:219], 0\n\t"
"v_mul_f32_e64 v55, s64, v55\n\t"
"v_cvt_pk_bf16_f32 v150, v52, v53\n\t"
"v_cvt_pk_bf16_f32 v151, v54, v55\n\t"
"v_mfma_f32_32x32x16_bf16 a[16:31], v[216:219], v[216:219], 0\n\t"
"v_mul_f32_e32 v104, s64, v104\n\t"
"v_mul_f32_e32 v105, s64, v105\n\t"
"v_mul_f32_e32 v106, s64, v106\n\t"
"v_mul_f32_e32 v107, s64, v107\n\t"
"v_mfma_f32_32x32x16_bf16 a[32:47], v[216:219], v[216:219], 0\n\t"
"v_cvt_pk_bf16_f32 v104, v104, v105\n\t"
"v_mul_f32_e32 v108, s64, v108\n\t"
"v_mul_f32_e32 v109, s64, v109\n\t"
"v_mfma_f32_16x16x32_bf16 v[24:27], a[56:59], a[100:103], v[24:27]\n\t"
"v_cvt_pk_bf16_f32 v105, v106, v107\n\t"
"v_mul_f32_e32 v110, s64, v110\n\t"
"v_mul_f32_e32 v111, s64, v111\n\t"
"v_cvt_pk_bf16_f32 v106, v108, v109\n\t"
"v_mfma_f32_16x16x32_bf16 v[28:31], a[60:63], a[100:103], v[28:31]\n\t"
"v_cvt_pk_bf16_f32 v107, v110, v111\n\t"
"v_mul_f32_e32 v152, s64, v152\n\t"
"v_mul_f32_e32 v153, s64, v153\n\t"
"v_mfma_f32_16x16x32_bf16 v[72:75], a[64:67], a[100:103], v[72:75]\n\t"
"v_mul_f32_e32 v154, s64, v154\n\t"
"v_mul_f32_e32 v155, s64, v155\n\t"
"v_cvt_pk_bf16_f32 v152, v152, v153\n\t"
"v_mul_f32_e64 v156, s64, v156\n\t"
"v_mfma_f32_16x16x32_bf16 v[76:79], a[68:71], a[100:103], v[76:79]\n\t"
"v_mul_f32_e64 v157, s64, v157\n\t"
"v_cvt_pk_bf16_f32 v153, v154, v155\n\t"
"v_mul_f32_e64 v158, s64, v158\n\t"
"v_mfma_f32_16x16x32_bf16 v[120:123], a[72:75], a[100:103], v[120:123]\n\t"
"v_mul_f32_e64 v159, s64, v159\n\t"
"v_cvt_pk_bf16_f32 v154, v156, v157\n\t"
"v_cvt_pk_bf16_f32 v155, v158, v159\n\t"
"v_mfma_f32_16x16x32_bf16 v[124:127], a[76:79], a[100:103], v[124:127]\n\t"
"v_mul_f32_e32 v8, s64, v8\n\t"
"v_mul_f32_e32 v9, s64, v9\n\t"
"v_mul_f32_e32 v10, s64, v10\n\t"
"v_mul_f32_e32 v11, s64, v11\n\t"
"v_mfma_f32_16x16x32_bf16 v[168:171], a[80:83], a[100:103], v[168:171]\n\t"
"v_cvt_pk_bf16_f32 v108, v8, v9\n\t"
"v_mul_f32_e32 v12, s64, v12\n\t"
"v_mul_f32_e32 v13, s64, v13\n\t"
"v_mfma_f32_16x16x32_bf16 v[172:175], a[84:87], a[100:103], v[172:175]\n\t"
"v_cvt_pk_bf16_f32 v109, v10, v11\n\t"
"v_mul_f32_e32 v14, s64, v14\n\t"
"v_mul_f32_e32 v15, s64, v15\n\t"
"v_cvt_pk_bf16_f32 v110, v12, v13\n\t"
"v_mfma_f32_16x16x32_bf16 v[32:35], a[56:59], a[104:107], v[32:35]\n\t"
"v_cvt_pk_bf16_f32 v111, v14, v15\n\t"
"v_mul_f32_e32 v56, s64, v56\n\t"
"v_mul_f32_e32 v57, s64, v57\n\t"
"v_mfma_f32_16x16x32_bf16 v[36:39], a[60:63], a[104:107], v[36:39]\n\t"
"v_mul_f32_e32 v58, s64, v58\n\t"
"v_mul_f32_e32 v59, s64, v59\n\t"
"v_cvt_pk_bf16_f32 v156, v56, v57\n\t"
"v_mul_f32_e64 v60, s64, v60\n\t"
"v_mfma_f32_16x16x32_bf16 v[80:83], a[64:67], a[104:107], v[80:83]\n\t"
"v_mul_f32_e64 v61, s64, v61\n\t"
"v_cvt_pk_bf16_f32 v157, v58, v59\n\t"
"v_mul_f32_e64 v62, s64, v62\n\t"
"v_mfma_f32_16x16x32_bf16 v[84:87], a[68:71], a[104:107], v[84:87]\n\t"
"v_mul_f32_e64 v63, s64, v63\n\t"
"v_cvt_pk_bf16_f32 v158, v60, v61\n\t"
"v_cvt_pk_bf16_f32 v159, v62, v63\n\t"
"v_mfma_f32_16x16x32_bf16 v[128:131], a[72:75], a[104:107], v[128:131]\n\t"
"v_mul_f32_e32 v112, s64, v112\n\t"
"v_mul_f32_e32 v113, s64, v113\n\t"
"v_mul_f32_e64 v114, s64, v114\n\t"
"v_mfma_f32_16x16x32_bf16 v[132:135], a[76:79], a[104:107], v[132:135]\n\t"
"s_waitcnt lgkmcnt(0)\n\t"
"v_mul_f32_e32 v115, s64, v115\n\t"
"v_cvt_pk_bf16_f32 v112, v112, v113\n\t"
"v_mul_f32_e64 v116, s64, v116\n\t"
"v_mfma_f32_16x16x32_bf16 v[176:179], a[80:83], a[104:107], v[176:179]\n\t"
"v_mul_f32_e64 v117, s64, v117\n\t"
"v_cvt_pk_bf16_f32 v113, v114, v115\n\t"
"v_mul_f32_e64 v118, s64, v118\n\t"
"v_mfma_f32_16x16x32_bf16 v[180:183], a[84:87], a[104:107], v[180:183]\n\t"
"v_mul_f32_e64 v119, s64, v119\n\t"
"v_cvt_pk_bf16_f32 v114, v116, v117\n\t"
"v_cvt_pk_bf16_f32 v115, v118, v119\n\t"
"v_mfma_f32_16x16x32_bf16 v[40:43], a[56:59], a[108:111], v[40:43]\n\t"
"v_mul_f32_e32 v160, s64, v160\n\t"
"v_mul_f32_e32 v161, s64, v161\n\t"
"v_mul_f32_e64 v162, s64, v162\n\t"
"v_mfma_f32_16x16x32_bf16 v[44:47], a[60:63], a[108:111], v[44:47]\n\t"
"v_mul_f32_e64 v163, s64, v163\n\t"
"v_cvt_pk_bf16_f32 v160, v160, v161\n\t"
"v_mul_f32_e64 v164, s64, v164\n\t"
"v_mfma_f32_16x16x32_bf16 v[88:91], a[64:67], a[108:111], v[88:91]\n\t"
"v_mul_f32_e64 v165, s64, v165\n\t"
"v_cvt_pk_bf16_f32 v161, v162, v163\n\t"
"v_mul_f32_e64 v166, s64, v166\n\t"
"v_mfma_f32_16x16x32_bf16 v[92:95], a[68:71], a[108:111], v[92:95]\n\t"
"v_mul_f32_e64 v167, s64, v167\n\t"
"v_cvt_pk_bf16_f32 v162, v164, v165\n\t"
"v_cvt_pk_bf16_f32 v163, v166, v167\n\t"
"v_mfma_f32_16x16x32_bf16 v[136:139], a[72:75], a[108:111], v[136:139]\n\t"
"v_mul_f32_e32 v16, s64, v16\n\t"
"v_mul_f32_e32 v17, s64, v17\n\t"
"v_mul_f32_e64 v18, s64, v18\n\t"
"v_mfma_f32_16x16x32_bf16 v[140:143], a[76:79], a[108:111], v[140:143]\n\t"
"v_mul_f32_e64 v19, s64, v19\n\t"
"v_cvt_pk_bf16_f32 v116, v16, v17\n\t"
"v_mul_f32_e64 v20, s64, v20\n\t"
"v_mfma_f32_16x16x32_bf16 v[184:187], a[80:83], a[108:111], v[184:187]\n\t"
"v_mul_f32_e64 v21, s64, v21\n\t"
"v_cvt_pk_bf16_f32 v117, v18, v19\n\t"
"v_mul_f32_e64 v22, s64, v22\n\t"
"v_mfma_f32_16x16x32_bf16 v[188:191], a[84:87], a[108:111], v[188:191]\n\t"
"v_mul_f32_e64 v23, s64, v23\n\t"
"v_cvt_pk_bf16_f32 v118, v20, v21\n\t"
"v_cvt_pk_bf16_f32 v119, v22, v23\n\t"
"v_mfma_f32_16x16x32_bf16 v[0:3], a[192:195], v[100:103], v[192:195]\n\t"
"v_mul_f32_e32 v64, s64, v64\n\t"
"v_mul_f32_e32 v65, s64, v65\n\t"
"v_mul_f32_e64 v66, s64, v66\n\t"
"v_mfma_f32_16x16x32_bf16 v[4:7], a[208:211], v[100:103], v[192:195]\n\t"
"v_mul_f32_e64 v67, s64, v67\n\t"
"v_cvt_pk_bf16_f32 v164, v64, v65\n\t"
"v_mul_f32_e64 v68, s64, v68\n\t"
"v_mfma_f32_16x16x32_bf16 v[0:3], a[196:199], v[148:151], v[0:3]\n\t"
"v_mul_f32_e64 v69, s64, v69\n\t"
"v_cvt_pk_bf16_f32 v165, v66, v67\n\t"
"v_mul_f32_e64 v70, s64, v70\n\t"
"v_mfma_f32_16x16x32_bf16 v[4:7], a[212:215], v[148:151], v[4:7]\n\t"
"v_mul_f32_e64 v71, s64, v71\n\t"
"v_cvt_pk_bf16_f32 v166, v68, v69\n\t"
"v_cvt_pk_bf16_f32 v167, v70, v71\n\t"
"v_mfma_f32_16x16x32_bf16 v[0:3], a[200:203], v[96:99], v[0:3]\n\t"
"v_mul_f32_e32 v120, s64, v120\n\t"
"v_mul_f32_e32 v121, s64, v121\n\t"
"v_mul_f32_e32 v122, s64, v122\n\t"
"v_mul_f32_e32 v123, s64, v123\n\t"
"v_mfma_f32_16x16x32_bf16 v[4:7], a[216:219], v[96:99], v[4:7]\n\t"
"v_cvt_pk_bf16_f32 v120, v120, v121\n\t"
"v_mul_f32_e32 v124, s64, v124\n\t"
"v_mul_f32_e32 v125, s64, v125\n\t"
"v_mfma_f32_16x16x32_bf16 v[0:3], a[204:207], v[144:147], v[0:3]\n\t"
"v_cvt_pk_bf16_f32 v121, v122, v123\n\t"
"v_mul_f32_e32 v126, s64, v126\n\t"
"v_mul_f32_e32 v127, s64, v127\n\t"
"v_mfma_f32_16x16x32_bf16 v[4:7], a[220:223], v[144:147], v[4:7]\n\t"
"v_cvt_pk_bf16_f32 v122, v124, v125\n\t"
"v_cvt_pk_bf16_f32 v123, v126, v127\n\t"
"v_mul_f32_e64 v168, s64, v168\n\t"
"v_mfma_f32_16x16x32_bf16 v[8:11], a[192:195], v[108:111], v[196:199]\n\t"
"v_mul_f32_e32 v169, s64, v169\n\t"
"v_mul_f32_e32 v170, s64, v170\n\t"
"v_mul_f32_e64 v171, s64, v171\n\t"
"v_mfma_f32_16x16x32_bf16 v[12:15], a[208:211], v[108:111], v[196:199]\n\t"
"v_cvt_pk_bf16_f32 v168, v168, v169\n\t"
"v_mul_f32_e32 v172, s64, v172\n\t"
"v_mul_f32_e32 v173, s64, v173\n\t"
"v_cvt_pk_bf16_f32 v169, v170, v171\n\t"
"v_mfma_f32_16x16x32_bf16 v[8:11], a[196:199], v[156:159], v[8:11]\n\t"
"v_mul_f32_e32 v174, s64, v174\n\t"
"v_mul_f32_e32 v175, s64, v175\n\t"
"v_cvt_pk_bf16_f32 v170, v172, v173\n\t"
"v_mfma_f32_16x16x32_bf16 v[12:15], a[212:215], v[156:159], v[12:15]\n\t"
"v_cvt_pk_bf16_f32 v171, v174, v175\n\t"
"v_mul_f32_e32 v24, s64, v24\n\t"
"v_mul_f32_e32 v25, s64, v25\n\t"
"v_mfma_f32_16x16x32_bf16 v[8:11], a[200:203], v[104:107], v[8:11]\n\t"
"v_mul_f32_e32 v26, s64, v26\n\t"
"v_mul_f32_e32 v27, s64, v27\n\t"
"v_cvt_pk_bf16_f32 v124, v24, v25\n\t"
"v_mfma_f32_16x16x32_bf16 v[12:15], a[216:219], v[104:107], v[12:15]\n\t"
"v_mul_f32_e32 v28, s64, v28\n\t"
"v_mul_f32_e32 v29, s64, v29\n\t"
"v_cvt_pk_bf16_f32 v125, v26, v27\n\t"
"v_mfma_f32_16x16x32_bf16 v[8:11], a[204:207], v[152:155], v[8:11]\n\t"
"v_mul_f32_e32 v30, s64, v30\n\t"
"v_mul_f32_e32 v31, s64, v31\n\t"
"v_cvt_pk_bf16_f32 v126, v28, v29\n\t"
"v_cvt_pk_bf16_f32 v127, v30, v31\n\t"
"v_mfma_f32_16x16x32_bf16 v[12:15], a[220:223], v[152:155], v[12:15]\n\t"
"v_mul_f32_e32 v72, s64, v72\n\t"
"v_mul_f32_e32 v73, s64, v73\n\t"
"v_mul_f32_e64 v74, s64, v74\n\t"
"v_mfma_f32_32x32x16_bf16 a[48:63], v[216:219], v[216:219], 0\n\t"
"v_mul_f32_e64 v75, s64, v75\n\t"
"v_cvt_pk_bf16_f32 v172, v72, v73\n\t"
"v_mul_f32_e64 v76, s64, v76\n\t"
"v_mfma_f32_32x32x16_bf16 a[64:79], v[216:219], v[216:219], 0\n\t"
"v_mul_f32_e64 v77, s64, v77\n\t"
"v_cvt_pk_bf16_f32 v173, v74, v75\n\t"
"v_mul_f32_e64 v78, s64, v78\n\t"
"v_mfma_f32_16x16x32_bf16 v[16:19], a[192:195], v[116:119], v[200:203]\n\t"
"v_mul_f32_e64 v79, s64, v79\n\t"
"v_cvt_pk_bf16_f32 v174, v76, v77\n\t"
"v_cvt_pk_bf16_f32 v175, v78, v79\n\t"
"v_mfma_f32_16x16x32_bf16 v[20:23], a[208:211], v[116:119], v[200:203]\n\t"
"v_mul_f32_e32 v128, s64, v128\n\t"
"v_mul_f32_e32 v129, s64, v129\n\t"
"v_mul_f32_e32 v130, s64, v130\n\t"
"v_mul_f32_e32 v131, s64, v131\n\t"
"v_mfma_f32_16x16x32_bf16 v[16:19], a[196:199], v[164:167], v[16:19]\n\t"
"v_cvt_pk_bf16_f32 v128, v128, v129\n\t"
"v_mul_f32_e32 v132, s64, v132\n\t"
"v_mul_f32_e32 v133, s64, v133\n\t"
"v_mfma_f32_16x16x32_bf16 v[20:23], a[212:215], v[164:167], v[20:23]\n\t"
"v_cvt_pk_bf16_f32 v129, v130, v131\n\t"
"v_mul_f32_e32 v134, s64, v134\n\t"
"v_mul_f32_e32 v135, s64, v135\n\t"
"v_cvt_pk_bf16_f32 v130, v132, v133\n\t"
"v_mfma_f32_16x16x32_bf16 v[16:19], a[200:203], v[112:115], v[16:19]\n\t"
"v_cvt_pk_bf16_f32 v131, v134, v135\n\t"
"v_mul_f32_e32 v176, s64, v176\n\t"
"v_mul_f32_e32 v177, s64, v177\n\t"
"v_mfma_f32_16x16x32_bf16 v[20:23], a[216:219], v[112:115], v[20:23]\n\t"
"v_mul_f32_e32 v178, s64, v178\n\t"
"v_mul_f32_e32 v179, s64, v179\n\t"
"v_cvt_pk_bf16_f32 v176, v176, v177\n\t"
"v_mul_f32_e64 v180, s64, v180\n\t"
"v_mfma_f32_16x16x32_bf16 v[16:19], a[204:207], v[160:163], v[16:19]\n\t"
"v_mul_f32_e64 v181, s64, v181\n\t"
"v_cvt_pk_bf16_f32 v177, v178, v179\n\t"
"v_mul_f32_e64 v182, s64, v182\n\t"
"v_mfma_f32_16x16x32_bf16 v[20:23], a[220:223], v[160:163], v[20:23]\n\t"
"v_mul_f32_e64 v183, s64, v183\n\t"
"v_cvt_pk_bf16_f32 v178, v180, v181\n\t"
"v_cvt_pk_bf16_f32 v179, v182, v183\n\t"
"v_mfma_f32_32x32x16_bf16 v[48:63], v[216:219], v[216:219], 0\n\t"
"v_mul_f32_e32 v32, s64, v32\n\t"
"v_mul_f32_e32 v33, s64, v33\n\t"
"v_mul_f32_e32 v34, s64, v34\n\t"
"v_mul_f32_e32 v35, s64, v35\n\t"
"v_mfma_f32_32x32x16_bf16 a[80:95], v[216:219], v[216:219], 0\n\t"
"v_cvt_pk_bf16_f32 v132, v32, v33\n\t"
"v_mul_f32_e32 v36, s64, v36\n\t"
"v_mul_f32_e32 v37, s64, v37\n\t"
"v_mfma_f32_32x32x16_bf16 a[96:111], v[216:219], v[216:219], 0\n\t"
"v_cvt_pk_bf16_f32 v133, v34, v35\n\t"
"v_mul_f32_e32 v38, s64, v38\n\t"
"v_mul_f32_e32 v39, s64, v39\n\t"
"v_cvt_pk_bf16_f32 v134, v36, v37\n\t"
"v_mfma_f32_16x16x32_bf16 v[24:27], a[192:195], v[124:127], v[204:207]\n\t"
"v_cvt_pk_bf16_f32 v135, v38, v39\n\t"
"v_mul_f32_e32 v80, s64, v80\n\t"
"v_mul_f32_e32 v81, s64, v81\n\t"
"v_mfma_f32_16x16x32_bf16 v[28:31], a[208:211], v[124:127], v[204:207]\n\t"
"v_mul_f32_e32 v82, s64, v82\n\t"
"v_mul_f32_e32 v83, s64, v83\n\t"
"v_cvt_pk_bf16_f32 v180, v80, v81\n\t"
"v_mul_f32_e64 v84, s64, v84\n\t"
"v_mfma_f32_16x16x32_bf16 v[24:27], a[196:199], v[172:175], v[24:27]\n\t"
"v_mul_f32_e64 v85, s64, v85\n\t"
"v_cvt_pk_bf16_f32 v181, v82, v83\n\t"
"v_mul_f32_e64 v86, s64, v86\n\t"
"v_mfma_f32_16x16x32_bf16 v[28:31], a[212:215], v[172:175], v[28:31]\n\t"
"v_mul_f32_e64 v87, s64, v87\n\t"
"v_cvt_pk_bf16_f32 v182, v84, v85\n\t"
"v_cvt_pk_bf16_f32 v183, v86, v87\n\t"
"v_mfma_f32_16x16x32_bf16 v[24:27], a[200:203], v[120:123], v[24:27]\n\t"
"v_mul_f32_e32 v136, s64, v136\n\t"
"v_mul_f32_e32 v137, s64, v137\n\t"
"v_mul_f32_e32 v138, s64, v138\n\t"
"v_mul_f32_e32 v139, s64, v139\n\t"
"v_mfma_f32_16x16x32_bf16 v[28:31], a[216:219], v[120:123], v[28:31]\n\t"
"v_cvt_pk_bf16_f32 v136, v136, v137\n\t"
"v_mul_f32_e32 v140, s64, v140\n\t"
"v_mul_f32_e32 v141, s64, v141\n\t"
"v_cvt_pk_bf16_f32 v137, v138, v139\n\t"
"v_mfma_f32_16x16x32_bf16 v[24:27], a[204:207], v[168:171], v[24:27]\n\t"
"v_mul_f32_e32 v142, s64, v142\n\t"
"v_mul_f32_e32 v143, s64, v143\n\t"
"v_cvt_pk_bf16_f32 v138, v140, v141\n\t"
"v_cvt_pk_bf16_f32 v139, v142, v143\n\t"
"v_mfma_f32_16x16x32_bf16 v[28:31], a[220:223], v[168:171], v[28:31]\n\t"
"v_mul_f32_e32 v184, s64, v184\n\t"
"v_mul_f32_e32 v185, s64, v185\n\t"
"v_mul_f32_e64 v186, s64, v186\n\t"
"v_mfma_f32_32x32x16_bf16 v[64:79], v[216:219], v[216:219], 0\n\t"
"v_mul_f32_e64 v187, s64, v187\n\t"
"v_cvt_pk_bf16_f32 v184, v184, v185\n\t"
"v_mul_f32_e32 v188, s64, v188\n\t"
"v_mul_f32_e32 v189, s64, v189\n\t"
"v_mfma_f32_16x16x32_bf16 v[32:35], a[192:195], v[132:135], v[208:211]\n\t"
"v_cvt_pk_bf16_f32 v185, v186, v187\n\t"
"v_mul_f32_e32 v190, s64, v190\n\t"
"v_mul_f32_e32 v191, s64, v191\n\t"
"v_cvt_pk_bf16_f32 v186, v188, v189\n\t"
"v_mfma_f32_16x16x32_bf16 v[36:39], a[208:211], v[132:135], v[208:211]\n\t"
"v_cvt_pk_bf16_f32 v187, v190, v191\n\t"
"v_mul_f32_e32 v40, s64, v40\n\t"
"v_mul_f32_e32 v41, s64, v41\n\t"
"v_mfma_f32_16x16x32_bf16 v[32:35], a[196:199], v[180:183], v[32:35]\n\t"
"v_mul_f32_e32 v42, s64, v42\n\t"
"v_mul_f32_e32 v43, s64, v43\n\t"
"v_cvt_pk_bf16_f32 v140, v40, v41\n\t"
"v_mul_f32_e64 v44, s64, v44\n\t"
"v_mfma_f32_16x16x32_bf16 v[36:39], a[212:215], v[180:183], v[36:39]\n\t"
"v_mul_f32_e64 v45, s64, v45\n\t"
"v_cvt_pk_bf16_f32 v141, v42, v43\n\t"
"v_mul_f32_e32 v46, s64, v46\n\t"
"v_mul_f32_e32 v47, s64, v47\n\t"
"v_mfma_f32_16x16x32_bf16 v[32:35], a[200:203], v[128:131], v[32:35]\n\t"
"v_cvt_pk_bf16_f32 v142, v44, v45\n\t"
"v_cvt_pk_bf16_f32 v143, v46, v47\n\t"
"v_mul_f32_e64 v88, s64, v88\n\t"
"v_mfma_f32_16x16x32_bf16 v[36:39], a[216:219], v[128:131], v[36:39]\n\t"
"v_mul_f32_e32 v89, s64, v89\n\t"
"v_mul_f32_e32 v90, s64, v90\n\t"
"v_mul_f32_e64 v91, s64, v91\n\t"
"v_cvt_pk_bf16_f32 v188, v88, v89\n\t"
"v_mfma_f32_16x16x32_bf16 v[32:35], a[204:207], v[176:179], v[32:35]\n\t"
"v_mul_f32_e32 v92, s64, v92\n\t"
"v_mul_f32_e32 v93, s64, v93\n\t"
"v_cvt_pk_bf16_f32 v189, v90, v91\n\t"
"v_mul_f32_e64 v94, s64, v94\n\t"
"v_mfma_f32_16x16x32_bf16 v[36:39], a[220:223], v[176:179], v[36:39]\n\t"
"v_mul_f32_e64 v95, s64, v95\n\t"
"v_cvt_pk_bf16_f32 v190, v92, v93\n\t"
"v_cvt_pk_bf16_f32 v191, v94, v95\n\t"
"v_mfma_f32_16x16x32_bf16 v[40:43], a[192:195], v[140:143], v[212:215]\n\t"
"v_mfma_f32_16x16x32_bf16 v[44:47], a[208:211], v[140:143], v[212:215]\n\t"
"v_mfma_f32_16x16x32_bf16 v[40:43], a[196:199], v[188:191], v[40:43]\n\t"
"v_mfma_f32_16x16x32_bf16 v[44:47], a[212:215], v[188:191], v[44:47]\n\t"
"v_mfma_f32_16x16x32_bf16 v[40:43], a[200:203], v[136:139], v[40:43]\n\t"
"v_mfma_f32_16x16x32_bf16 v[44:47], a[216:219], v[136:139], v[44:47]\n\t"
"v_mfma_f32_16x16x32_bf16 v[40:43], a[204:207], v[184:187], v[40:43]\n\t"
"v_mfma_f32_16x16x32_bf16 v[44:47], a[220:223], v[184:187], v[44:47]\n\t"
"v_mfma_f32_32x32x16_bf16 v[80:95], v[216:219], v[216:219], 0\n\t"
"v_max3_f32 v233, v0, v1, v2\n\t"
"v_max3_f32 v234, v3, v4, v5\n\t"
"v_max3_f32 v233, v233, v6, v7\n\t"
"v_max_f32_e32 v233, v233, v234\n\t"
"v_mov_b32_e32 v234, v233\n\t"
"s_nop 1\n\t"
"v_permlane16_swap_b32_e32 v233, v234\n\t"
"s_nop 0\n\t"
"v_max_f32_e32 v233, v233, v234\n\t"
"v_mov_b32_e32 v234, v233\n\t"
"s_nop 1\n\t"
"v_permlane32_swap_b32_e32 v233, v234\n\t"
"s_nop 0\n\t"
"v_max_f32_e32 v233, v233, v234\n\t"
"v_sub_f32_e32 v0, v0, v233\n\t"
"v_sub_f32_e32 v1, v1, v233\n\t"
"v_sub_f32_e32 v2, v2, v233\n\t"
"v_sub_f32_e32 v3, v3, v233\n\t"
"v_sub_f32_e32 v4, v4, v233\n\t"
"v_sub_f32_e32 v5, v5, v233\n\t"
"v_sub_f32_e32 v6, v6, v233\n\t"
"v_sub_f32_e32 v7, v7, v233\n\t"
"v_sub_f32_e32 v192, v192, v233\n\t"
"v_sub_f32_e32 v193, v193, v233\n\t"
"v_sub_f32_e32 v194, v194, v233\n\t"
"v_sub_f32_e64 v195, v195, v233\n\t"
"v_max3_f32 v233, v8, v9, v10\n\t"
"v_max3_f32 v234, v11, v12, v13\n\t"
"v_max3_f32 v233, v233, v14, v15\n\t"
"v_max_f32_e32 v233, v233, v234\n\t"
"v_mov_b32_e32 v234, v233\n\t"
"s_nop 1\n\t"
"v_permlane16_swap_b32_e32 v233, v234\n\t"
"s_nop 0\n\t"
"v_max_f32_e32 v233, v233, v234\n\t"
"v_mov_b32_e32 v234, v233\n\t"
"s_nop 1\n\t"
"v_permlane32_swap_b32_e32 v233, v234\n\t"
"s_nop 0\n\t"
"v_max_f32_e32 v233, v233, v234\n\t"
"v_sub_f32_e32 v8, v8, v233\n\t"
"v_sub_f32_e32 v9, v9, v233\n\t"
"v_sub_f32_e32 v10, v10, v233\n\t"
"v_sub_f32_e32 v11, v11, v233\n\t"
"v_sub_f32_e32 v12, v12, v233\n\t"
"v_sub_f32_e32 v13, v13, v233\n\t"
"v_sub_f32_e32 v14, v14, v233\n\t"
"v_sub_f32_e32 v15, v15, v233\n\t"
"v_sub_f32_e32 v196, v196, v233\n\t"
"v_sub_f32_e32 v197, v197, v233\n\t"
"v_sub_f32_e32 v198, v198, v233\n\t"
"v_sub_f32_e64 v199, v199, v233\n\t"
"v_max3_f32 v233, v16, v17, v18\n\t"
"v_max3_f32 v234, v19, v20, v21\n\t"
"v_max3_f32 v233, v233, v22, v23\n\t"
"v_max_f32_e32 v233, v233, v234\n\t"
"v_mov_b32_e32 v234, v233\n\t"
"s_nop 1\n\t"
"v_permlane16_swap_b32_e32 v233, v234\n\t"
"s_nop 0\n\t"
"v_max_f32_e32 v233, v233, v234\n\t"
"v_mov_b32_e32 v234, v233\n\t"
"s_nop 1\n\t"
"v_permlane32_swap_b32_e32 v233, v234\n\t"
"s_nop 0\n\t"
"v_max_f32_e32 v233, v233, v234\n\t"
"v_sub_f32_e32 v16, v16, v233\n\t"
"v_sub_f32_e32 v17, v17, v233\n\t"
"v_sub_f32_e32 v18, v18, v233\n\t"
"v_sub_f32_e32 v19, v19, v233\n\t"
"v_sub_f32_e32 v20, v20, v233\n\t"
"v_sub_f32_e32 v21, v21, v233\n\t"
"v_sub_f32_e32 v22, v22, v233\n\t"
"v_sub_f32_e32 v23, v23, v233\n\t"
"v_sub_f32_e32 v200, v200, v233\n\t"
"v_sub_f32_e32 v201, v201, v233\n\t"
"v_sub_f32_e32 v202, v202, v233\n\t"
"v_sub_f32_e64 v203, v203, v233\n\t"
"v_max3_f32 v233, v24, v25, v26\n\t"
"v_max3_f32 v234, v27, v28, v29\n\t"
"v_max3_f32 v233, v233, v30, v31\n\t"
"v_max_f32_e32 v233, v233, v234\n\t"
"v_mov_b32_e32 v234, v233\n\t"
"s_nop 1\n\t"
"v_permlane16_swap_b32_e32 v233, v234\n\t"
"s_nop 0\n\t"
"v_max_f32_e32 v233, v233, v234\n\t"
"v_mov_b32_e32 v234, v233\n\t"
"s_nop 1\n\t"
"v_permlane32_swap_b32_e32 v233, v234\n\t"
"s_nop 0\n\t"
"v_max_f32_e32 v233, v233, v234\n\t"
"v_sub_f32_e32 v24, v24, v233\n\t"
"v_sub_f32_e32 v25, v25, v233\n\t"
"v_sub_f32_e32 v26, v26, v233\n\t"
"v_sub_f32_e32 v27, v27, v233\n\t"
"v_sub_f32_e32 v28, v28, v233\n\t"
"v_sub_f32_e32 v29, v29, v233\n\t"
"v_sub_f32_e32 v30, v30, v233\n\t"
"v_sub_f32_e32 v31, v31, v233\n\t"
"v_sub_f32_e32 v204, v204, v233\n\t"
"v_sub_f32_e32 v205, v205, v233\n\t"
"v_sub_f32_e32 v206, v206, v233\n\t"
"v_sub_f32_e64 v207, v207, v233\n\t"
"v_max3_f32 v233, v32, v33, v34\n\t"
"v_max3_f32 v234, v35, v36, v37\n\t"
"v_max3_f32 v233, v233, v38, v39\n\t"
"v_max_f32_e32 v233, v233, v234\n\t"
"v_mov_b32_e32 v234, v233\n\t"
"s_nop 1\n\t"
"v_permlane16_swap_b32_e32 v233, v234\n\t"
"s_nop 0\n\t"
"v_max_f32_e32 v233, v233, v234\n\t"
"v_mov_b32_e32 v234, v233\n\t"
"s_nop 1\n\t"
"v_permlane32_swap_b32_e32 v233, v234\n\t"
"s_nop 0\n\t"
"v_max_f32_e32 v233, v233, v234\n\t"
"v_sub_f32_e32 v32, v32, v233\n\t"
"v_sub_f32_e32 v33, v33, v233\n\t"
"v_sub_f32_e32 v34, v34, v233\n\t"
"v_sub_f32_e32 v35, v35, v233\n\t"
"v_sub_f32_e32 v36, v36, v233\n\t"
"v_sub_f32_e32 v37, v37, v233\n\t"
"v_sub_f32_e32 v38, v38, v233\n\t"
"v_sub_f32_e32 v39, v39, v233\n\t"
"v_sub_f32_e32 v208, v208, v233\n\t"
"v_sub_f32_e32 v209, v209, v233\n\t"
"v_sub_f32_e32 v210, v210, v233\n\t"
"v_sub_f32_e64 v211, v211, v233\n\t"
"v_max3_f32 v233, v40, v41, v42\n\t"
"v_max3_f32 v234, v43, v44, v45\n\t"
"v_max3_f32 v233, v233, v46, v47\n\t"
"v_max_f32_e32 v233, v233, v234\n\t"
"v_mov_b32_e32 v234, v233\n\t"
"s_nop 1\n\t"
"v_permlane16_swap_b32_e32 v233, v234\n\t"
"s_nop 0\n\t"
"v_max_f32_e32 v233, v233, v234\n\t"
"v_mov_b32_e32 v234, v233\n\t"
"s_nop 1\n\t"
"v_permlane32_swap_b32_e32 v233, v234\n\t"
"s_nop 0\n\t"
"v_max_f32_e32 v233, v233, v234\n\t"
"v_sub_f32_e32 v40, v40, v233\n\t"
"v_sub_f32_e32 v41, v41, v233\n\t"
"v_sub_f32_e32 v42, v42, v233\n\t"
"v_sub_f32_e32 v43, v43, v233\n\t"
"v_sub_f32_e32 v44, v44, v233\n\t"
"v_sub_f32_e32 v45, v45, v233\n\t"
"v_sub_f32_e32 v46, v46, v233\n\t"
"v_sub_f32_e32 v47, v47, v233\n\t"
"v_sub_f32_e32 v212, v212, v233\n\t"
"v_sub_f32_e32 v213, v213, v233\n\t"
"v_sub_f32_e32 v214, v214, v233\n\t"
"v_sub_f32_e64 v215, v215, v233\n\t"
"s_mov_b32 s68, 16384\n\t"
"s_mov_b32 s69, 16384\n\t"
"s_mov_b32 s60, 0x47800000\n\t"
"s_mov_b32 m0, s56\n\t"
"s_waitcnt vmcnt(16)\n\t"
"s_barrier\n\t"
"global_load_dword v255, v216, %12 sc1\n\t"
"s_mov_b32 s71, %15\n\t"
"v_lshrrev_b32_e32 v250, 5, v252\n\t"
"v_lshlrev_b32_e32 v250, 10, v250\n\t"
"s_lshr_b32 s73, s71, 20\n\t"
"s_lshl_b32 s73, s73, 20\n\t"
"s_lshl_b32 s71, s71, 2\n\t"
"s_add_u32 s71, s71, s73\n\t"
"s_and_b32 s71, s71, 0x3fffff\n\t"
"s_sub_u32 s73, 0x180, s78\n\t"
"s_and_b32 s73, s73, 0x380\n\t"
"s_add_u32 s71, s71, s73\n\t"
"v_add_u32_e64 v250, s71, v250\n\t"
"s_mov_b32 s83, 0\n\t"
"s_memrealtime s[74:75]\n\t"
"s_waitcnt lgkmcnt(0)\n\t"
"s_mov_b32 s77, 0\n\t"
"s_sub_u32 s74, s74, 40\n\t"
"s_mov_b32 s80, 142\n\t"
"s_memtime s[64:65]\n\t"
"s_waitcnt lgkmcnt(0)\n\t"
"s_add_u32 s65, s74, 40\n\t"
"s_mov_b32 s62, 132\n\t"
"v_mov_b32_e32 v216, 0x3f803f80\n\t"
"v_mov_b32_e32 v217, 0x3f803f80\n\t"
"v_mov_b32_e32 v218, 0x3f803f80\n\t"
"v_mov_b32_e32 v219, 0x3f803f80\n\t"
"s_nop 7\n\t"
".p2align 6\n\t"
"MAIN%=:\n\t"
"v_mfma_f32_16x16x32_bf16 a[0:3], a[224:227], v[48:51], a[0:3]\n\t"
"buffer_load_dwordx4 v223, s[36:39], s54 offen offset:0 nt lds\n\t"
"v_add_u32_e64 v220, s69, v220\n\t"
"v_mfma_f32_16x16x32_bf16 a[4:7], a[228:231], v[48:51], a[4:7]\n\t"
"buffer_load_dwordx4 v224, s[36:39], s54 offen offset:1024 nt lds\n\t"
"v_add_u32_e64 v250, s83, v250\n\t"
"v_mfma_f32_16x16x32_bf16 a[8:11], a[232:235], v[48:51], a[8:11]\n\t"
"v_exp_f32_e64 v0, v0\n\t"
"v_mfma_f32_16x16x32_bf16 a[12:15], a[236:239], v[48:51], a[12:15]\n\t"
"buffer_load_dwordx4 v225, s[36:39], s54 offen offset:2048 nt lds\n\t"
"v_add_f32_e64 v227, v227, v52\n\t"
"v_mfma_f32_16x16x32_bf16 a[16:19], a[240:243], v[48:51], a[16:19]\n\t"
"buffer_load_dwordx4 v226, s[36:39], s54 offen offset:3072 nt lds\n\t"
"v_add_f32_e64 v228, v228, v60\n\t"
"v_mfma_f32_16x16x32_bf16 a[20:23], a[244:247], v[48:51], a[20:23]\n\t"
"v_exp_f32_e64 v1, v1\n\t"
"v_mfma_f32_16x16x32_bf16 a[24:27], a[248:251], v[48:51], a[24:27]\n\t"
"s_add_u32 m0, s56, 0x1000\n\t"
"v_exp_f32_e64 v2, v2\n\t"
"v_mfma_f32_16x16x32_bf16 a[28:31], a[252:255], v[48:51], a[28:31]\n\t"
"v_exp_f32_e64 v3, v3\n\t"
"v_mfma_f32_16x16x32_bf16 a[32:35], a[224:227], v[56:59], a[32:35]\n\t"
"s_add_u32 s54, s54, 0x2000\n\t"
"v_exp_f32_e64 v4, v4\n\t"
"v_mfma_f32_16x16x32_bf16 a[36:39], a[228:231], v[56:59], a[36:39]\n\t"
"buffer_load_dwordx4 v223, s[36:39], s55 offen offset:0 nt lds\n\t"
"v_add_f32_e64 v229, v229, v68\n\t"
"v_mfma_f32_16x16x32_bf16 a[40:43], a[232:235], v[56:59], a[40:43]\n\t"
"v_exp_f32_e64 v5, v5\n\t"
"v_mfma_f32_16x16x32_bf16 a[44:47], a[236:239], v[56:59], a[44:47]\n\t"
"buffer_load_dwordx4 v224, s[36:39], s55 offen offset:1024 nt lds\n\t"
"v_add_f32_e64 v230, v230, v76\n\t"
"v_mfma_f32_16x16x32_bf16 a[48:51], a[240:243], v[56:59], a[48:51]\n\t"
"v_exp_f32_e64 v6, v6\n\t"
"v_mfma_f32_16x16x32_bf16 a[52:55], a[244:247], v[56:59], a[52:55]\n\t"
"buffer_load_dwordx4 v225, s[36:39], s55 offen offset:2048 nt lds\n\t"
"v_add_f32_e64 v231, v231, v84\n\t"
"v_mfma_f32_16x16x32_bf16 a[56:59], a[248:251], v[56:59], a[56:59]\n\t"
"buffer_load_dwordx4 v226, s[36:39], s55 offen offset:3072 nt lds\n\t"
"v_add_f32_e64 v232, v232, v92\n\t"
"v_mfma_f32_16x16x32_bf16 a[60:63], a[252:255], v[56:59], a[60:63]\n\t"
"v_exp_f32_e64 v7, v7\n\t"
"v_mfma_f32_16x16x32_bf16 a[64:67], a[224:227], v[64:67], a[64:67]\n\t"
"s_add_u32 s55, s55, 0x2000\n\t"
"v_exp_f32_e64 v8, v8\n\t"
"v_mfma_f32_16x16x32_bf16 a[68:71], a[228:231], v[64:67], a[68:71]\n\t"
"v_exp_f32_e64 v9, v9\n\t"
"v_mfma_f32_16x16x32_bf16 a[72:75], a[232:235], v[64:67], a[72:75]\n\t"
"s_add_u32 s56, s56, 0x4000\n\t"
"v_exp_f32_e64 v10, v10\n\t"
"v_mfma_f32_16x16x32_bf16 a[76:79], a[236:239], v[64:67], a[76:79]\n\t"
"s_cmp_eq_u32 s56, s57\n\t"
"v_exp_f32_e32 v11, v11\n\t"
"v_mfma_f32_16x16x32_bf16 a[80:83], a[240:243], v[64:67], a[80:83]\n\t"
"v_exp_f32_e64 v12, v12\n\t"
"v_mfma_f32_16x16x32_bf16 a[84:87], a[244:247], v[64:67], a[84:87]\n\t"
"s_cselect_b32 s56, s58, s56\n\t"
"v_exp_f32_e32 v13, v13\n\t"
"v_mfma_f32_16x16x32_bf16 a[88:91], a[248:251], v[64:67], a[88:91]\n\t"
"v_exp_f32_e64 v14, v14\n\t"
"v_mfma_f32_16x16x32_bf16 a[92:95], a[252:255], v[64:67], a[92:95]\n\t"
"global_load_dword v251, v250, s[78:79]\n\t"
"v_mfma_f32_16x16x32_bf16 a[96:99], a[224:227], v[72:75], a[96:99]\n\t"
"ds_read_b128 a[192:195], v220 offset:0\n\t"
"v_mfma_f32_16x16x32_bf16 a[100:103], a[228:231], v[72:75], a[100:103]\n\t"
"ds_read_b128 a[196:199], v220 offset:512\n\t"
"v_mfma_f32_16x16x32_bf16 a[104:107], a[232:235], v[72:75], a[104:107]\n\t"
"v_exp_f32_e64 v15, v15\n\t"
"v_mfma_f32_16x16x32_bf16 a[108:111], a[236:239], v[72:75], a[108:111]\n\t"
"ds_read_b128 a[200:203], v220 offset:1024\n\t"
"v_mfma_f32_16x16x32_bf16 a[112:115], a[240:243], v[72:75], a[112:115]\n\t"
"v_exp_f32_e64 v16, v16\n\t"
"v_mfma_f32_16x16x32_bf16 a[116:119], a[244:247], v[72:75], a[116:119]\n\t"
"ds_read_b128 a[204:207], v220 offset:1536\n\t"
"v_mfma_f32_16x16x32_bf16 a[120:123], a[248:251], v[72:75], a[120:123]\n\t"
"v_exp_f32_e64 v17, v17\n\t"
"v_mfma_f32_16x16x32_bf16 a[124:127], a[252:255], v[72:75], a[124:127]\n\t"
"ds_read_b128 a[208:211], v220 offset:4096\n\t"
"v_mfma_f32_16x16x32_bf16 a[128:131], a[224:227], v[80:83], a[128:131]\n\t"
"v_exp_f32_e64 v18, v18\n\t"
"v_mfma_f32_16x16x32_bf16 a[132:135], a[228:231], v[80:83], a[132:135]\n\t"
"ds_read_b128 a[212:215], v220 offset:4608\n\t"
"v_mfma_f32_16x16x32_bf16 a[136:139], a[232:235], v[80:83], a[136:139]\n\t"
"v_exp_f32_e64 v19, v19\n\t"
"v_mfma_f32_16x16x32_bf16 a[140:143], a[236:239], v[80:83], a[140:143]\n\t"
"ds_read_b128 a[216:219], v220 offset:5120\n\t"
"v_mfma_f32_16x16x32_bf16 a[144:147], a[240:243], v[80:83], a[144:147]\n\t"
"v_exp_f32_e64 v20, v20\n\t"
"v_mfma_f32_16x16x32_bf16 a[148:151], a[244:247], v[80:83], a[148:151]\n\t"
"ds_read_b128 a[220:223], v220 offset:5632\n\t"
"v_mfma_f32_16x16x32_bf16 a[152:155], a[248:251], v[80:83], a[152:155]\n\t"
"v_exp_f32_e64 v21, v21\n\t"
"v_mfma_f32_16x16x32_bf16 a[156:159], a[252:255], v[80:83], a[156:159]\n\t"
"v_exp_f32_e64 v22, v22\n\t"
"v_mfma_f32_16x16x32_bf16 a[160:163], a[224:227], v[88:91], a[160:163]\n\t"
"s_max_i32 s77, s77, -10\n\t"
"v_exp_f32_e32 v23, v23\n\t"
"v_mfma_f32_16x16x32_bf16 a[164:167], a[228:231], v[88:91], a[164:167]\n\t"
"s_add_u32 s76, s74, s77\n\t"
"v_exp_f32_e32 v24, v24\n\t"
"v_mfma_f32_16x16x32_bf16 a[168:171], a[232:235], v[88:91], a[168:171]\n\t"
"s_add_u32 s76, s76, s80\n\t"
"v_exp_f32_e32 v25, v25\n\t"
"v_mfma_f32_16x16x32_bf16 a[172:175], a[236:239], v[88:91], a[172:175]\n\t"
"v_exp_f32_e64 v26, v26\n\t"
"v_mfma_f32_16x16x32_bf16 a[176:179], a[240:243], v[88:91], a[176:179]\n\t"
"v_exp_f32_e64 v27, v27\n\t"
"v_mfma_f32_16x16x32_bf16 a[180:183], a[244:247], v[88:91], a[180:183]\n\t"
"s_waitcnt lgkmcnt(0)\n\t"
"v_exp_f32_e32 v28, v28\n\t"
"v_mfma_f32_16x16x32_bf16 a[184:187], a[248:251], v[88:91], a[184:187]\n\t"
"v_exp_f32_e64 v29, v29\n\t"
"v_mfma_f32_16x16x32_bf16 a[188:191], a[252:255], v[88:91], a[188:191]\n\t"
"s_cmp_eq_u32 s59, 28\n\t"
"v_exp_f32_e32 v30, v30\n\t"
"v_exp_f32_e32 v31, v31\n\t"
"s_cbranch_scc1 EPI%=\n\t"
"v_mfma_f32_16x16x32_bf16 v[48:51], a[192:195], v[100:103], v[192:195]\n\t"
"v_add_u32_e32 v221, s70, v221\n\t"
"v_add_u32_e32 v222, s70, v222\n\t"
"v_mfma_f32_16x16x32_bf16 v[52:55], a[208:211], v[100:103], v[192:195]\n\t"
"ds_read_b64_tr_b16 a[224:225], v221 offset:0\n\t"
"v_cvt_pk_bf16_f32 v0, v0, v1\n\t"
"v_mfma_f32_16x16x32_bf16 v[48:51], a[196:199], v[148:151], v[48:51]\n\t"
"s_mov_b32 s70, s69\n\t"
"v_cvt_pk_bf16_f32 v1, v2, v3\n\t"
"v_cvt_pk_bf16_f32 v2, v4, v5\n\t"
"v_mfma_f32_16x16x32_bf16 v[52:55], a[212:215], v[148:151], v[52:55]\n\t"
"ds_read_b64_tr_b16 a[226:227], v221 offset:4096\n\t"
"v_cvt_pk_bf16_f32 v3, v6, v7\n\t"
"v_mfma_f32_16x16x32_bf16 v[48:51], a[200:203], v[96:99], v[48:51]\n\t"
"s_add_u32 s71, s68, 16384\n\t"
"v_cvt_pk_bf16_f32 v8, v8, v9\n\t"
"v_cvt_pk_bf16_f32 v9, v10, v11\n\t"
"v_mfma_f32_16x16x32_bf16 v[52:55], a[216:219], v[96:99], v[52:55]\n\t"
"ds_read_b64_tr_b16 a[228:229], v222 offset:0\n\t"
"v_cvt_pk_bf16_f32 v10, v12, v13\n\t"
"v_mfma_f32_16x16x32_bf16 v[48:51], a[204:207], v[144:147], v[48:51]\n\t"
"s_and_b32 s71, s71, 65535\n\t"
"v_cvt_pk_bf16_f32 v11, v14, v15\n\t"
"v_mfma_f32_16x16x32_bf16 v[52:55], a[220:223], v[144:147], v[52:55]\n\t"
"ds_read_b64_tr_b16 a[230:231], v222 offset:4096\n\t"
"v_mfma_f32_16x16x32_bf16 v[56:59], a[192:195], v[108:111], v[196:199]\n\t"
"s_sub_u32 s69, s71, s68\n\t"
"v_exp_f32_e64 v32, v32\n\t"
"v_mfma_f32_16x16x32_bf16 v[60:63], a[208:211], v[108:111], v[196:199]\n\t"
"ds_read_b64_tr_b16 a[232:233], v221 offset:512\n\t"
"v_mfma_f32_16x16x32_bf16 v[56:59], a[196:199], v[156:159], v[56:59]\n\t"
"s_mov_b32 s68, s71\n\t"
"v_exp_f32_e32 v33, v33\n\t"
"v_mfma_f32_16x16x32_bf16 v[60:63], a[212:215], v[156:159], v[60:63]\n\t"
"ds_read_b64_tr_b16 a[234:235], v221 offset:4608\n\t"
"v_cvt_pk_bf16_f32 v16, v16, v17\n\t"
"v_mfma_f32_16x16x32_bf16 v[4:7], v[216:219], v[0:3], 0\n\t"
"v_cvt_pk_bf16_f32 v17, v18, v19\n\t"
"v_cvt_pk_bf16_f32 v18, v20, v21\n\t"
"v_mfma_f32_16x16x32_bf16 v[12:15], v[216:219], v[8:11], 0\n\t"
"s_add_u32 s59, s59, 1\n\t"
"ds_read_b64_tr_b16 a[236:237], v222 offset:512\n\t"
"v_cvt_pk_bf16_f32 v19, v22, v23\n\t"
"v_mfma_f32_16x16x32_bf16 v[56:59], a[200:203], v[104:107], v[56:59]\n\t"
"v_cvt_pk_bf16_f32 v24, v24, v25\n\t"
"v_cvt_pk_bf16_f32 v25, v26, v27\n\t"
"v_mfma_f32_16x16x32_bf16 v[60:63], a[216:219], v[104:107], v[60:63]\n\t"
"s_sub_i32 s80, s80, 2\n\t"
"ds_read_b64_tr_b16 a[238:239], v222 offset:4608\n\t"
"v_cvt_pk_bf16_f32 v26, v28, v29\n\t"
"v_mfma_f32_16x16x32_bf16 v[56:59], a[204:207], v[152:155], v[56:59]\n\t"
"v_cvt_pk_bf16_f32 v27, v30, v31\n\t"
"v_mfma_f32_16x16x32_bf16 v[60:63], a[220:223], v[152:155], v[60:63]\n\t"
"s_max_i32 s80, s80, s62\n\t"
"ds_read_b64_tr_b16 a[240:241], v221 offset:1024\n\t"
"v_mfma_f32_16x16x32_bf16 v[64:67], a[192:195], v[116:119], v[200:203]\n\t"
"v_exp_f32_e32 v34, v34\n\t"
"v_mfma_f32_16x16x32_bf16 v[68:71], a[208:211], v[116:119], v[200:203]\n\t"
"s_cmp_ge_u32 s59, 28\n\t"
"ds_read_b64_tr_b16 a[242:243], v221 offset:5120\n\t"
"v_mfma_f32_16x16x32_bf16 v[64:67], a[196:199], v[164:167], v[64:67]\n\t"
"v_exp_f32_e32 v35, v35\n\t"
"v_mfma_f32_16x16x32_bf16 v[68:71], a[212:215], v[164:167], v[68:71]\n\t"
"ds_read_b64_tr_b16 a[244:245], v222 offset:1024\n\t"
"v_mfma_f32_16x16x32_bf16 v[64:67], a[200:203], v[112:115], v[64:67]\n\t"
"s_cselect_b32 s80, 0, s80\n\t"
"v_exp_f32_e32 v36, v36\n\t"
"v_mfma_f32_16x16x32_bf16 v[68:71], a[216:219], v[112:115], v[68:71]\n\t"
"ds_read_b64_tr_b16 a[246:247], v222 offset:5120\n\t"
"v_mfma_f32_16x16x32_bf16 v[20:23], v[216:219], v[16:19], 0\n\t"
"s_sub_u32 s71, s59, 2\n\t"
"v_exp_f32_e32 v37, v37\n\t"
"v_mfma_f32_16x16x32_bf16 v[28:31], v[216:219], v[24:27], 0\n\t"
"ds_read_b64_tr_b16 a[248:249], v221 offset:1536\n\t"
"v_mfma_f32_16x16x32_bf16 v[64:67], a[204:207], v[160:163], v[64:67]\n\t"
"s_cmp_lt_u32 s71, 15\n\t"
"v_exp_f32_e32 v38, v38\n\t"
"v_mfma_f32_16x16x32_bf16 v[68:71], a[220:223], v[160:163], v[68:71]\n\t"
"ds_read_b64_tr_b16 a[250:251], v221 offset:5632\n\t"
"v_mfma_f32_16x16x32_bf16 v[72:75], a[192:195], v[124:127], v[204:207]\n\t"
"s_cselect_b32 s83, 0x800, 0\n\t"
"v_exp_f32_e64 v39, v39\n\t"
"v_mfma_f32_16x16x32_bf16 v[76:79], a[208:211], v[124:127], v[204:207]\n\t"
"ds_read_b64_tr_b16 a[252:253], v222 offset:1536\n\t"
"v_mfma_f32_16x16x32_bf16 v[72:75], a[196:199], v[172:175], v[72:75]\n\t"
"v_exp_f32_e64 v40, v40\n\t"
"v_mfma_f32_16x16x32_bf16 v[76:79], a[212:215], v[172:175], v[76:79]\n\t"
"ds_read_b64_tr_b16 a[254:255], v222 offset:5632\n\t"
"v_mfma_f32_16x16x32_bf16 v[72:75], a[200:203], v[120:123], v[72:75]\n\t"
"v_exp_f32_e64 v41, v41\n\t"
"v_mfma_f32_16x16x32_bf16 v[76:79], a[216:219], v[120:123], v[76:79]\n\t"
"v_exp_f32_e64 v42, v42\n\t"
"v_mfma_f32_16x16x32_bf16 v[72:75], a[204:207], v[168:171], v[72:75]\n\t"
"v_exp_f32_e64 v43, v43\n\t"
"v_mfma_f32_16x16x32_bf16 v[76:79], a[220:223], v[168:171], v[76:79]\n\t"
"s_cmp_eq_u32 s59, 3\n\t"
"v_exp_f32_e32 v44, v44\n\t"
"v_mfma_f32_16x16x32_bf16 v[80:83], a[192:195], v[132:135], v[208:211]\n\t"
"s_cbranch_scc1 LC%=\n\t"
"LCB%=:\n\t"
"v_exp_f32_e32 v45, v45\n\t"
"v_mfma_f32_16x16x32_bf16 v[84:87], a[208:211], v[132:135], v[208:211]\n\t"
"v_exp_f32_e32 v46, v46\n\t"
"v_exp_f32_e32 v47, v47\n\t"
"v_mfma_f32_16x16x32_bf16 v[80:83], a[196:199], v[180:183], v[80:83]\n\t"
"s_memrealtime s[74:75]\n\t"
"v_cvt_pk_bf16_f32 v32, v32, v33\n\t"
"v_cvt_pk_bf16_f32 v33, v34, v35\n\t"
"v_mfma_f32_16x16x32_bf16 v[84:87], a[212:215], v[180:183], v[84:87]\n\t"
"v_cvt_pk_bf16_f32 v34, v36, v37\n\t"
"v_cvt_pk_bf16_f32 v35, v38, v39\n\t"
"v_mfma_f32_16x16x32_bf16 v[80:83], a[200:203], v[128:131], v[80:83]\n\t"
"v_cvt_pk_bf16_f32 v40, v40, v41\n\t"
"v_cvt_pk_bf16_f32 v41, v42, v43\n\t"
"v_mfma_f32_16x16x32_bf16 v[84:87], a[216:219], v[128:131], v[84:87]\n\t"
"v_cvt_pk_bf16_f32 v42, v44, v45\n\t"
"v_cvt_pk_bf16_f32 v43, v46, v47\n\t"
"v_mfma_f32_16x16x32_bf16 v[80:83], a[204:207], v[176:179], v[80:83]\n\t"
"v_mfma_f32_16x16x32_bf16 v[84:87], a[220:223], v[176:179], v[84:87]\n\t"
"v_mfma_f32_16x16x32_bf16 v[36:39], v[216:219], v[32:35], 0\n\t"
"v_mfma_f32_16x16x32_bf16 v[44:47], v[216:219], v[40:43], 0\n\t"
"v_mfma_f32_16x16x32_bf16 v[88:91], a[192:195], v[140:143], v[212:215]\n\t"
"s_waitcnt lgkmcnt(0)\n\t"
"v_mfma_f32_16x16x32_bf16 v[92:95], a[208:211], v[140:143], v[212:215]\n\t"
"s_sub_u32 s77, s76, s74\n\t"
"v_max3_f32 v233, v4, v12, v20\n\t"
"v_mfma_f32_16x16x32_bf16 v[88:91], a[196:199], v[188:191], v[88:91]\n\t"
"s_cmp_gt_i32 s77, 0\n\t"
"v_mfma_f32_16x16x32_bf16 v[92:95], a[212:215], v[188:191], v[92:95]\n\t"
"s_cbranch_scc1 PC1%=\n\t"
"PCB1%=:\n\t"
"v_mfma_f32_16x16x32_bf16 v[88:91], a[200:203], v[136:139], v[88:91]\n\t"
"s_mov_b32 m0, s56\n\t"
"v_max3_f32 v233, v233, v28, v36\n\t"
"v_mfma_f32_16x16x32_bf16 v[92:95], a[216:219], v[136:139], v[92:95]\n\t"
"s_waitcnt vmcnt(19) lgkmcnt(0)\n\t"
"v_max_f32_e64 v233, v233, v44\n\t"
"v_mfma_f32_16x16x32_bf16 v[88:91], a[204:207], v[184:187], v[88:91]\n\t"
"s_barrier\n\t"
"v_cmp_nge_f32_e32 vcc, s60, v233\n\t"
"v_mfma_f32_16x16x32_bf16 v[92:95], a[220:223], v[184:187], v[92:95]\n\t"
"s_cbranch_vccnz CS0_%=\n\t"
"CB0_%=:\n\t"
"v_mfma_f32_16x16x32_bf16 a[0:3], a[224:227], v[0:3], a[0:3]\n\t"
"buffer_load_dwordx4 v223, s[36:39], s54 offen offset:0 nt lds\n\t"
"v_add_u32_e32 v220, s69, v220\n\t"
"v_mfma_f32_16x16x32_bf16 a[4:7], a[228:231], v[0:3], a[4:7]\n\t"
"buffer_load_dwordx4 v224, s[36:39], s54 offen offset:1024 nt lds\n\t"
"v_add_u32_e64 v250, s83, v250\n\t"
"v_mfma_f32_16x16x32_bf16 a[8:11], a[232:235], v[0:3], a[8:11]\n\t"
"v_exp_f32_e64 v48, v48\n\t"
"v_mfma_f32_16x16x32_bf16 a[12:15], a[236:239], v[0:3], a[12:15]\n\t"
"buffer_load_dwordx4 v225, s[36:39], s54 offen offset:2048 nt lds\n\t"
"v_add_f32_e64 v227, v227, v4\n\t"
"v_mfma_f32_16x16x32_bf16 a[16:19], a[240:243], v[0:3], a[16:19]\n\t"
"buffer_load_dwordx4 v226, s[36:39], s54 offen offset:3072 nt lds\n\t"
"v_add_f32_e64 v228, v228, v12\n\t"
"v_mfma_f32_16x16x32_bf16 a[20:23], a[244:247], v[0:3], a[20:23]\n\t"
"v_exp_f32_e64 v49, v49\n\t"
"v_mfma_f32_16x16x32_bf16 a[24:27], a[248:251], v[0:3], a[24:27]\n\t"
"s_add_u32 m0, s56, 0x1000\n\t"
"v_exp_f32_e64 v50, v50\n\t"
"v_mfma_f32_16x16x32_bf16 a[28:31], a[252:255], v[0:3], a[28:31]\n\t"
"v_exp_f32_e64 v51, v51\n\t"
"v_mfma_f32_16x16x32_bf16 a[32:35], a[224:227], v[8:11], a[32:35]\n\t"
"s_add_u32 s54, s54, 0x2000\n\t"
"v_exp_f32_e64 v52, v52\n\t"
"v_mfma_f32_16x16x32_bf16 a[36:39], a[228:231], v[8:11], a[36:39]\n\t"
"buffer_load_dwordx4 v223, s[36:39], s55 offen offset:0 nt lds\n\t"
"v_add_f32_e64 v229, v229, v20\n\t"
"v_mfma_f32_16x16x32_bf16 a[40:43], a[232:235], v[8:11], a[40:43]\n\t"
"v_exp_f32_e64 v53, v53\n\t"
"v_mfma_f32_16x16x32_bf16 a[44:47], a[236:239], v[8:11], a[44:47]\n\t"
"buffer_load_dwordx4 v224, s[36:39], s55 offen offset:1024 nt lds\n\t"
"v_add_f32_e64 v230, v230, v28\n\t"
"v_mfma_f32_16x16x32_bf16 a[48:51], a[240:243], v[8:11], a[48:51]\n\t"
"v_exp_f32_e64 v54, v54\n\t"
"v_mfma_f32_16x16x32_bf16 a[52:55], a[244:247], v[8:11], a[52:55]\n\t"
"buffer_load_dwordx4 v225, s[36:39], s55 offen offset:2048 nt lds\n\t"
"v_add_f32_e64 v231, v231, v36\n\t"
"v_mfma_f32_16x16x32_bf16 a[56:59], a[248:251], v[8:11], a[56:59]\n\t"
"buffer_load_dwordx4 v226, s[36:39], s55 offen offset:3072 nt lds\n\t"
"v_add_f32_e64 v232, v232, v44\n\t"
"v_mfma_f32_16x16x32_bf16 a[60:63], a[252:255], v[8:11], a[60:63]\n\t"
"v_exp_f32_e64 v55, v55\n\t"
"v_mfma_f32_16x16x32_bf16 a[64:67], a[224:227], v[16:19], a[64:67]\n\t"
"s_add_u32 s55, s55, 0x2000\n\t"
"v_exp_f32_e64 v56, v56\n\t"
"v_mfma_f32_16x16x32_bf16 a[68:71], a[228:231], v[16:19], a[68:71]\n\t"
"v_exp_f32_e64 v57, v57\n\t"
"v_mfma_f32_16x16x32_bf16 a[72:75], a[232:235], v[16:19], a[72:75]\n\t"
"s_add_u32 s56, s56, 0x4000\n\t"
"v_exp_f32_e64 v58, v58\n\t"
"v_mfma_f32_16x16x32_bf16 a[76:79], a[236:239], v[16:19], a[76:79]\n\t"
"s_cmp_eq_u32 s56, s57\n\t"
"v_exp_f32_e32 v59, v59\n\t"
"v_mfma_f32_16x16x32_bf16 a[80:83], a[240:243], v[16:19], a[80:83]\n\t"
"v_exp_f32_e64 v60, v60\n\t"
"v_mfma_f32_16x16x32_bf16 a[84:87], a[244:247], v[16:19], a[84:87]\n\t"
"s_cselect_b32 s56, s58, s56\n\t"
"v_exp_f32_e32 v61, v61\n\t"
"v_mfma_f32_16x16x32_bf16 a[88:91], a[248:251], v[16:19], a[88:91]\n\t"
"v_exp_f32_e64 v62, v62\n\t"
"v_mfma_f32_16x16x32_bf16 a[92:95], a[252:255], v[16:19], a[92:95]\n\t"
"global_load_dword v251, v250, s[78:79]\n\t"
"v_mfma_f32_16x16x32_bf16 a[96:99], a[224:227], v[24:27], a[96:99]\n\t"
"ds_read_b128 a[192:195], v220 offset:0\n\t"
"v_mfma_f32_16x16x32_bf16 a[100:103], a[228:231], v[24:27], a[100:103]\n\t"
"ds_read_b128 a[196:199], v220 offset:512\n\t"
"v_mfma_f32_16x16x32_bf16 a[104:107], a[232:235], v[24:27], a[104:107]\n\t"
"v_exp_f32_e64 v63, v63\n\t"
"v_mfma_f32_16x16x32_bf16 a[108:111], a[236:239], v[24:27], a[108:111]\n\t"
"ds_read_b128 a[200:203], v220 offset:1024\n\t"
"v_mfma_f32_16x16x32_bf16 a[112:115], a[240:243], v[24:27], a[112:115]\n\t"
"v_exp_f32_e64 v64, v64\n\t"
"v_mfma_f32_16x16x32_bf16 a[116:119], a[244:247], v[24:27], a[116:119]\n\t"
"ds_read_b128 a[204:207], v220 offset:1536\n\t"
"v_mfma_f32_16x16x32_bf16 a[120:123], a[248:251], v[24:27], a[120:123]\n\t"
"v_exp_f32_e64 v65, v65\n\t"
"v_mfma_f32_16x16x32_bf16 a[124:127], a[252:255], v[24:27], a[124:127]\n\t"
"ds_read_b128 a[208:211], v220 offset:4096\n\t"
"v_mfma_f32_16x16x32_bf16 a[128:131], a[224:227], v[32:35], a[128:131]\n\t"
"v_exp_f32_e64 v66, v66\n\t"
"v_mfma_f32_16x16x32_bf16 a[132:135], a[228:231], v[32:35], a[132:135]\n\t"
"ds_read_b128 a[212:215], v220 offset:4608\n\t"
"v_mfma_f32_16x16x32_bf16 a[136:139], a[232:235], v[32:35], a[136:139]\n\t"
"v_exp_f32_e64 v67, v67\n\t"
"v_mfma_f32_16x16x32_bf16 a[140:143], a[236:239], v[32:35], a[140:143]\n\t"
"ds_read_b128 a[216:219], v220 offset:5120\n\t"
"v_mfma_f32_16x16x32_bf16 a[144:147], a[240:243], v[32:35], a[144:147]\n\t"
"v_exp_f32_e64 v68, v68\n\t"
"v_mfma_f32_16x16x32_bf16 a[148:151], a[244:247], v[32:35], a[148:151]\n\t"
"ds_read_b128 a[220:223], v220 offset:5632\n\t"
"v_mfma_f32_16x16x32_bf16 a[152:155], a[248:251], v[32:35], a[152:155]\n\t"
"v_exp_f32_e64 v69, v69\n\t"
"v_mfma_f32_16x16x32_bf16 a[156:159], a[252:255], v[32:35], a[156:159]\n\t"
"v_exp_f32_e64 v70, v70\n\t"
"v_mfma_f32_16x16x32_bf16 a[160:163], a[224:227], v[40:43], a[160:163]\n\t"
"s_max_i32 s77, s77, -10\n\t"
"v_exp_f32_e32 v71, v71\n\t"
"v_mfma_f32_16x16x32_bf16 a[164:167], a[228:231], v[40:43], a[164:167]\n\t"
"s_add_u32 s76, s74, s77\n\t"
"v_exp_f32_e32 v72, v72\n\t"
"v_mfma_f32_16x16x32_bf16 a[168:171], a[232:235], v[40:43], a[168:171]\n\t"
"s_add_u32 s76, s76, s80\n\t"
"v_exp_f32_e32 v73, v73\n\t"
"v_mfma_f32_16x16x32_bf16 a[172:175], a[236:239], v[40:43], a[172:175]\n\t"
"v_exp_f32_e64 v74, v74\n\t"
"v_mfma_f32_16x16x32_bf16 a[176:179], a[240:243], v[40:43], a[176:179]\n\t"
"v_exp_f32_e64 v75, v75\n\t"
"v_mfma_f32_16x16x32_bf16 a[180:183], a[244:247], v[40:43], a[180:183]\n\t"
"s_waitcnt lgkmcnt(0)\n\t"
"v_exp_f32_e32 v76, v76\n\t"
"v_mfma_f32_16x16x32_bf16 a[184:187], a[248:251], v[40:43], a[184:187]\n\t"
"v_exp_f32_e64 v77, v77\n\t"
"v_mfma_f32_16x16x32_bf16 a[188:191], a[252:255], v[40:43], a[188:191]\n\t"
"v_exp_f32_e32 v78, v78\n\t"
"v_exp_f32_e32 v79, v79\n\t"
"v_mfma_f32_16x16x32_bf16 v[0:3], a[192:195], v[100:103], v[192:195]\n\t"
"v_add_u32_e32 v221, s70, v221\n\t"
"v_add_u32_e32 v222, s70, v222\n\t"
"v_mfma_f32_16x16x32_bf16 v[4:7], a[208:211], v[100:103], v[192:195]\n\t"
"ds_read_b64_tr_b16 a[224:225], v221 offset:0\n\t"
"v_cvt_pk_bf16_f32 v48, v48, v49\n\t"
"v_mfma_f32_16x16x32_bf16 v[0:3], a[196:199], v[148:151], v[0:3]\n\t"
"s_mov_b32 s70, s69\n\t"
"v_cvt_pk_bf16_f32 v49, v50, v51\n\t"
"v_cvt_pk_bf16_f32 v50, v52, v53\n\t"
"v_mfma_f32_16x16x32_bf16 v[4:7], a[212:215], v[148:151], v[4:7]\n\t"
"ds_read_b64_tr_b16 a[226:227], v221 offset:4096\n\t"
"v_cvt_pk_bf16_f32 v51, v54, v55\n\t"
"v_mfma_f32_16x16x32_bf16 v[0:3], a[200:203], v[96:99], v[0:3]\n\t"
"s_add_u32 s71, s68, 16384\n\t"
"v_cvt_pk_bf16_f32 v56, v56, v57\n\t"
"v_cvt_pk_bf16_f32 v57, v58, v59\n\t"
"v_mfma_f32_16x16x32_bf16 v[4:7], a[216:219], v[96:99], v[4:7]\n\t"
"ds_read_b64_tr_b16 a[228:229], v222 offset:0\n\t"
"v_cvt_pk_bf16_f32 v58, v60, v61\n\t"
"v_mfma_f32_16x16x32_bf16 v[0:3], a[204:207], v[144:147], v[0:3]\n\t"
"v_cvt_pk_bf16_f32 v59, v62, v63\n\t"
"v_mfma_f32_16x16x32_bf16 v[4:7], a[220:223], v[144:147], v[4:7]\n\t"
"s_and_b32 s71, s71, 65535\n\t"
"ds_read_b64_tr_b16 a[230:231], v222 offset:4096\n\t"
"v_mfma_f32_16x16x32_bf16 v[8:11], a[192:195], v[108:111], v[196:199]\n\t"
"v_exp_f32_e32 v80, v80\n\t"
"v_mfma_f32_16x16x32_bf16 v[12:15], a[208:211], v[108:111], v[196:199]\n\t"
"s_sub_u32 s69, s71, s68\n\t"
"ds_read_b64_tr_b16 a[232:233], v221 offset:512\n\t"
"v_mfma_f32_16x16x32_bf16 v[8:11], a[196:199], v[156:159], v[8:11]\n\t"
"v_exp_f32_e32 v81, v81\n\t"
"v_mfma_f32_16x16x32_bf16 v[12:15], a[212:215], v[156:159], v[12:15]\n\t"
"ds_read_b64_tr_b16 a[234:235], v221 offset:4608\n\t"
"v_cvt_pk_bf16_f32 v64, v64, v65\n\t"
"v_mfma_f32_16x16x32_bf16 v[52:55], v[216:219], v[48:51], 0\n\t"
"s_mov_b32 s68, s71\n\t"
"v_cvt_pk_bf16_f32 v65, v66, v67\n\t"
"v_cvt_pk_bf16_f32 v66, v68, v69\n\t"
"v_mfma_f32_16x16x32_bf16 v[60:63], v[216:219], v[56:59], 0\n\t"
"ds_read_b64_tr_b16 a[236:237], v222 offset:512\n\t"
"v_cvt_pk_bf16_f32 v67, v70, v71\n\t"
"v_mfma_f32_16x16x32_bf16 v[8:11], a[200:203], v[104:107], v[8:11]\n\t"
"v_cvt_pk_bf16_f32 v72, v72, v73\n\t"
"v_cvt_pk_bf16_f32 v73, v74, v75\n\t"
"v_mfma_f32_16x16x32_bf16 v[12:15], a[216:219], v[104:107], v[12:15]\n\t"
"s_add_u32 s59, s59, 1\n\t"
"ds_read_b64_tr_b16 a[238:239], v222 offset:4608\n\t"
"v_cvt_pk_bf16_f32 v74, v76, v77\n\t"
"v_mfma_f32_16x16x32_bf16 v[8:11], a[204:207], v[152:155], v[8:11]\n\t"
"v_cvt_pk_bf16_f32 v75, v78, v79\n\t"
"v_mfma_f32_16x16x32_bf16 v[12:15], a[220:223], v[152:155], v[12:15]\n\t"
"s_cmp_ge_u32 s59, 28\n\t"
"ds_read_b64_tr_b16 a[240:241], v221 offset:1024\n\t"
"v_mfma_f32_16x16x32_bf16 v[16:19], a[192:195], v[116:119], v[200:203]\n\t"
"v_exp_f32_e32 v82, v82\n\t"
"v_mfma_f32_16x16x32_bf16 v[20:23], a[208:211], v[116:119], v[200:203]\n\t"
"ds_read_b64_tr_b16 a[242:243], v221 offset:5120\n\t"
"v_mfma_f32_16x16x32_bf16 v[16:19], a[196:199], v[164:167], v[16:19]\n\t"
"s_cselect_b32 s80, 0, s80\n\t"
"v_exp_f32_e32 v83, v83\n\t"
"v_mfma_f32_16x16x32_bf16 v[20:23], a[212:215], v[164:167], v[20:23]\n\t"
"ds_read_b64_tr_b16 a[244:245], v222 offset:1024\n\t"
"v_mfma_f32_16x16x32_bf16 v[16:19], a[200:203], v[112:115], v[16:19]\n\t"
"v_exp_f32_e64 v84, v84\n\t"
"v_mfma_f32_16x16x32_bf16 v[20:23], a[216:219], v[112:115], v[20:23]\n\t"
"s_sub_u32 s71, s59, 2\n\t"
"ds_read_b64_tr_b16 a[246:247], v222 offset:5120\n\t"
"v_mfma_f32_16x16x32_bf16 v[68:71], v[216:219], v[64:67], 0\n\t"
"v_exp_f32_e32 v85, v85\n\t"
"v_mfma_f32_16x16x32_bf16 v[76:79], v[216:219], v[72:75], 0\n\t"
"s_cmp_lt_u32 s71, 15\n\t"
"ds_read_b64_tr_b16 a[248:249], v221 offset:1536\n\t"
"v_mfma_f32_16x16x32_bf16 v[16:19], a[204:207], v[160:163], v[16:19]\n\t"
"v_exp_f32_e32 v86, v86\n\t"
"v_mfma_f32_16x16x32_bf16 v[20:23], a[220:223], v[160:163], v[20:23]\n\t"
"ds_read_b64_tr_b16 a[250:251], v221 offset:5632\n\t"
"v_mfma_f32_16x16x32_bf16 v[24:27], a[192:195], v[124:127], v[204:207]\n\t"
"s_cselect_b32 s83, 0x800, 0\n\t"
"v_exp_f32_e64 v87, v87\n\t"
"v_mfma_f32_16x16x32_bf16 v[28:31], a[208:211], v[124:127], v[204:207]\n\t"
"ds_read_b64_tr_b16 a[252:253], v222 offset:1536\n\t"
"v_mfma_f32_16x16x32_bf16 v[24:27], a[196:199], v[172:175], v[24:27]\n\t"
"v_exp_f32_e64 v88, v88\n\t"
"v_mfma_f32_16x16x32_bf16 v[28:31], a[212:215], v[172:175], v[28:31]\n\t"
"ds_read_b64_tr_b16 a[254:255], v222 offset:5632\n\t"
"v_mfma_f32_16x16x32_bf16 v[24:27], a[200:203], v[120:123], v[24:27]\n\t"
"v_exp_f32_e64 v89, v89\n\t"
"v_mfma_f32_16x16x32_bf16 v[28:31], a[216:219], v[120:123], v[28:31]\n\t"
"v_exp_f32_e64 v90, v90\n\t"
"v_mfma_f32_16x16x32_bf16 v[24:27], a[204:207], v[168:171], v[24:27]\n\t"
"v_exp_f32_e64 v91, v91\n\t"
"v_mfma_f32_16x16x32_bf16 v[28:31], a[220:223], v[168:171], v[28:31]\n\t"
"v_exp_f32_e64 v92, v92\n\t"
"v_mfma_f32_16x16x32_bf16 v[32:35], a[192:195], v[132:135], v[208:211]\n\t"
"v_exp_f32_e64 v93, v93\n\t"
"v_mfma_f32_16x16x32_bf16 v[36:39], a[208:211], v[132:135], v[208:211]\n\t"
"v_exp_f32_e32 v94, v94\n\t"
"v_exp_f32_e32 v95, v95\n\t"
"v_mfma_f32_16x16x32_bf16 v[32:35], a[196:199], v[180:183], v[32:35]\n\t"
"s_memrealtime s[74:75]\n\t"
"v_cvt_pk_bf16_f32 v80, v80, v81\n\t"
"v_cvt_pk_bf16_f32 v81, v82, v83\n\t"
"v_mfma_f32_16x16x32_bf16 v[36:39], a[212:215], v[180:183], v[36:39]\n\t"
"v_cvt_pk_bf16_f32 v82, v84, v85\n\t"
"v_cvt_pk_bf16_f32 v83, v86, v87\n\t"
"v_mfma_f32_16x16x32_bf16 v[32:35], a[200:203], v[128:131], v[32:35]\n\t"
"v_cvt_pk_bf16_f32 v88, v88, v89\n\t"
"v_cvt_pk_bf16_f32 v89, v90, v91\n\t"
"v_mfma_f32_16x16x32_bf16 v[36:39], a[216:219], v[128:131], v[36:39]\n\t"
"v_cvt_pk_bf16_f32 v90, v92, v93\n\t"
"v_cvt_pk_bf16_f32 v91, v94, v95\n\t"
"v_mfma_f32_16x16x32_bf16 v[32:35], a[204:207], v[176:179], v[32:35]\n\t"
"v_mfma_f32_16x16x32_bf16 v[36:39], a[220:223], v[176:179], v[36:39]\n\t"
"v_mfma_f32_16x16x32_bf16 v[84:87], v[216:219], v[80:83], 0\n\t"
"v_mfma_f32_16x16x32_bf16 v[92:95], v[216:219], v[88:91], 0\n\t"
"v_mfma_f32_16x16x32_bf16 v[40:43], a[192:195], v[140:143], v[212:215]\n\t"
"s_waitcnt lgkmcnt(0)\n\t"
"v_mfma_f32_16x16x32_bf16 v[44:47], a[208:211], v[140:143], v[212:215]\n\t"
"s_sub_u32 s77, s76, s74\n\t"
"v_max3_f32 v233, v52, v60, v68\n\t"
"v_mfma_f32_16x16x32_bf16 v[40:43], a[196:199], v[188:191], v[40:43]\n\t"
"s_cmp_gt_i32 s77, 0\n\t"
"v_mfma_f32_16x16x32_bf16 v[44:47], a[212:215], v[188:191], v[44:47]\n\t"
"s_cbranch_scc1 PC0%=\n\t"
"PCB0%=:\n\t"
"v_mfma_f32_16x16x32_bf16 v[40:43], a[200:203], v[136:139], v[40:43]\n\t"
"s_mov_b32 m0, s56\n\t"
"v_max3_f32 v233, v233, v76, v84\n\t"
"v_mfma_f32_16x16x32_bf16 v[44:47], a[216:219], v[136:139], v[44:47]\n\t"
"s_waitcnt vmcnt(19) lgkmcnt(0)\n\t"
"v_max_f32_e64 v233, v233, v92\n\t"
"v_mfma_f32_16x16x32_bf16 v[40:43], a[204:207], v[184:187], v[40:43]\n\t"
"s_barrier\n\t"
"v_cmp_nge_f32_e32 vcc, s60, v233\n\t"
"v_mfma_f32_16x16x32_bf16 v[44:47], a[220:223], v[184:187], v[44:47]\n\t"
"s_cbranch_vccnz CS1_%=\n\t"
"CB1_%=:\n\t"
"s_branch MAIN%=\n\t"
"EPI%=:\n\t"
"s_lshl_b32 s83, %18, 15\n\t"
"s_mul_i32 s58, %18, 6144\n\t"
"v_mfma_f32_16x16x32_bf16 v[48:51], a[192:195], v[100:103], v[192:195]\n\t"
"v_add_u32_e32 v221, s70, v221\n\t"
"v_add_u32_e64 v222, s70, v222\n\t"
"v_mfma_f32_16x16x32_bf16 v[52:55], a[208:211], v[100:103], v[192:195]\n\t"
"s_add_u32 m0, s72, 65536\n\t"
"ds_read_b64_tr_b16 a[224:225], v221 offset:0\n\t"
"v_cvt_pk_bf16_f32 v0, v0, v1\n\t"
"v_mfma_f32_16x16x32_bf16 v[48:51], a[196:199], v[148:151], v[48:51]\n\t"
"s_mov_b32 s70, s69\n\t"
"v_cvt_pk_bf16_f32 v1, v2, v3\n\t"
"v_cvt_pk_bf16_f32 v2, v4, v5\n\t"
"v_mfma_f32_16x16x32_bf16 v[52:55], a[212:215], v[148:151], v[52:55]\n\t"
"ds_read_b64_tr_b16 a[226:227], v221 offset:4096\n\t"
"v_cvt_pk_bf16_f32 v3, v6, v7\n\t"
"v_mfma_f32_16x16x32_bf16 v[48:51], a[200:203], v[96:99], v[48:51]\n\t"
"s_mov_b32 s73, s83\n\t"
"v_cvt_pk_bf16_f32 v8, v8, v9\n\t"
"v_cvt_pk_bf16_f32 v9, v10, v11\n\t"
"v_mfma_f32_16x16x32_bf16 v[52:55], a[216:219], v[96:99], v[52:55]\n\t"
"ds_read_b64_tr_b16 a[228:229], v222 offset:0\n\t"
"v_cvt_pk_bf16_f32 v10, v12, v13\n\t"
"v_mfma_f32_16x16x32_bf16 v[48:51], a[204:207], v[144:147], v[48:51]\n\t"
"s_add_u32 s71, s68, 16384\n\t"
"v_cvt_pk_bf16_f32 v11, v14, v15\n\t"
"v_mfma_f32_16x16x32_bf16 v[52:55], a[220:223], v[144:147], v[52:55]\n\t"
"s_and_b32 s73, s73, 0xfffff\n\t"
"ds_read_b64_tr_b16 a[230:231], v222 offset:4096\n\t"
"v_mfma_f32_16x16x32_bf16 v[56:59], a[192:195], v[108:111], v[196:199]\n\t"
"v_exp_f32_e64 v32, v32\n\t"
"v_mfma_f32_16x16x32_bf16 v[60:63], a[208:211], v[108:111], v[196:199]\n\t"
"ds_read_b64_tr_b16 a[232:233], v221 offset:512\n\t"
"v_mfma_f32_16x16x32_bf16 v[56:59], a[196:199], v[156:159], v[56:59]\n\t"
"buffer_load_dwordx4 v242, s[44:47], s73 offen offset:0 lds\n\t"
"v_mfma_f32_16x16x32_bf16 v[60:63], a[212:215], v[156:159], v[60:63]\n\t"
"s_and_b32 s71, s71, 65535\n\t"
"ds_read_b64_tr_b16 a[234:235], v221 offset:4608\n\t"
"v_cvt_pk_bf16_f32 v16, v16, v17\n\t"
"v_mfma_f32_16x16x32_bf16 v[4:7], v[216:219], v[0:3], 0\n\t"
"v_cvt_pk_bf16_f32 v17, v18, v19\n\t"
"v_cvt_pk_bf16_f32 v18, v20, v21\n\t"
"v_mfma_f32_16x16x32_bf16 v[12:15], v[216:219], v[8:11], 0\n\t"
"ds_read_b64_tr_b16 a[236:237], v222 offset:512\n\t"
"v_cvt_pk_bf16_f32 v19, v22, v23\n\t"
"v_mfma_f32_16x16x32_bf16 v[56:59], a[200:203], v[104:107], v[56:59]\n\t"
"buffer_load_dwordx4 v243, s[44:47], s73 offen offset:1024 lds\n\t"
"v_cvt_pk_bf16_f32 v24, v24, v25\n\t"
"v_mfma_f32_16x16x32_bf16 v[60:63], a[216:219], v[104:107], v[60:63]\n\t"
"ds_read_b64_tr_b16 a[238:239], v222 offset:4608\n\t"
"v_cvt_pk_bf16_f32 v25, v26, v27\n\t"
"v_mfma_f32_16x16x32_bf16 v[56:59], a[204:207], v[152:155], v[56:59]\n\t"
"s_sub_u32 s69, s71, s68\n\t"
"buffer_load_dwordx4 v244, s[44:47], s73 offen offset:2048 lds\n\t"
"v_cvt_pk_bf16_f32 v26, v28, v29\n\t"
"v_mfma_f32_16x16x32_bf16 v[60:63], a[220:223], v[152:155], v[60:63]\n\t"
"ds_read_b64_tr_b16 a[240:241], v221 offset:1024\n\t"
"v_cvt_pk_bf16_f32 v27, v30, v31\n\t"
"v_mfma_f32_16x16x32_bf16 v[64:67], a[192:195], v[116:119], v[200:203]\n\t"
"v_exp_f32_e32 v33, v33\n\t"
"v_mfma_f32_16x16x32_bf16 v[68:71], a[208:211], v[116:119], v[200:203]\n\t"
"ds_read_b64_tr_b16 a[242:243], v221 offset:5120\n\t"
"v_mfma_f32_16x16x32_bf16 v[64:67], a[196:199], v[164:167], v[64:67]\n\t"
"buffer_load_dwordx4 v245, s[44:47], s73 offen offset:3072 lds\n\t"
"v_mfma_f32_16x16x32_bf16 v[68:71], a[212:215], v[164:167], v[68:71]\n\t"
"s_mov_b32 s68, s71\n\t"
"ds_read_b64_tr_b16 a[244:245], v222 offset:1024\n\t"
"v_mfma_f32_16x16x32_bf16 v[64:67], a[200:203], v[112:115], v[64:67]\n\t"
"v_exp_f32_e32 v34, v34\n\t"
"v_mfma_f32_16x16x32_bf16 v[68:71], a[216:219], v[112:115], v[68:71]\n\t"
"s_add_u32 m0, s72, 147456\n\t"
"ds_read_b64_tr_b16 a[246:247], v222 offset:5120\n\t"
"v_mfma_f32_16x16x32_bf16 v[20:23], v[216:219], v[16:19], 0\n\t"
"v_exp_f32_e64 v35, v35\n\t"
"v_mfma_f32_16x16x32_bf16 v[28:31], v[216:219], v[24:27], 0\n\t"
"ds_read_b64_tr_b16 a[248:249], v221 offset:1536\n\t"
"v_mfma_f32_16x16x32_bf16 v[64:67], a[204:207], v[160:163], v[64:67]\n\t"
"s_add_u32 s59, s59, 1\n\t"
"v_exp_f32_e32 v36, v36\n\t"
"v_mfma_f32_16x16x32_bf16 v[68:71], a[220:223], v[160:163], v[68:71]\n\t"
"s_add_u32 s73, s83, 131072\n\t"
"ds_read_b64_tr_b16 a[250:251], v221 offset:5632\n\t"
"v_mfma_f32_16x16x32_bf16 v[72:75], a[192:195], v[124:127], v[204:207]\n\t"
"v_exp_f32_e64 v37, v37\n\t"
"v_mfma_f32_16x16x32_bf16 v[76:79], a[208:211], v[124:127], v[204:207]\n\t"
"s_and_b32 s73, s73, 0xfffff\n\t"
"ds_read_b64_tr_b16 a[252:253], v222 offset:1536\n\t"
"v_mfma_f32_16x16x32_bf16 v[72:75], a[196:199], v[172:175], v[72:75]\n\t"
"v_exp_f32_e64 v38, v38\n\t"
"v_mfma_f32_16x16x32_bf16 v[76:79], a[212:215], v[172:175], v[76:79]\n\t"
"ds_read_b64_tr_b16 a[254:255], v222 offset:5632\n\t"
"v_mfma_f32_16x16x32_bf16 v[72:75], a[200:203], v[120:123], v[72:75]\n\t"
"buffer_load_dwordx4 v242, s[44:47], s73 offen offset:0 lds\n\t"
"v_mfma_f32_16x16x32_bf16 v[76:79], a[216:219], v[120:123], v[76:79]\n\t"
"v_exp_f32_e64 v39, v39\n\t"
"v_mfma_f32_16x16x32_bf16 v[72:75], a[204:207], v[168:171], v[72:75]\n\t"
"v_exp_f32_e64 v40, v40\n\t"
"v_mfma_f32_16x16x32_bf16 v[76:79], a[220:223], v[168:171], v[76:79]\n\t"
"buffer_load_dwordx4 v243, s[44:47], s73 offen offset:1024 lds\n\t"
"v_mfma_f32_16x16x32_bf16 v[80:83], a[192:195], v[132:135], v[208:211]\n\t"
"v_exp_f32_e64 v41, v41\n\t"
"v_mfma_f32_16x16x32_bf16 v[84:87], a[208:211], v[132:135], v[208:211]\n\t"
"v_exp_f32_e32 v42, v42\n\t"
"v_exp_f32_e32 v43, v43\n\t"
"v_exp_f32_e32 v44, v44\n\t"
"v_exp_f32_e32 v45, v45\n\t"
"v_exp_f32_e32 v46, v46\n\t"
"v_exp_f32_e32 v47, v47\n\t"
"v_mfma_f32_16x16x32_bf16 v[80:83], a[196:199], v[180:183], v[80:83]\n\t"
"buffer_load_dwordx4 v244, s[44:47], s73 offen offset:2048 lds\n\t"
"v_cvt_pk_bf16_f32 v32, v32, v33\n\t"
"v_cvt_pk_bf16_f32 v33, v34, v35\n\t"
"v_mfma_f32_16x16x32_bf16 v[84:87], a[212:215], v[180:183], v[84:87]\n\t"
"v_cvt_pk_bf16_f32 v34, v36, v37\n\t"
"v_cvt_pk_bf16_f32 v35, v38, v39\n\t"
"v_cvt_pk_bf16_f32 v40, v40, v41\n\t"
"v_mfma_f32_16x16x32_bf16 v[80:83], a[200:203], v[128:131], v[80:83]\n\t"
"v_cvt_pk_bf16_f32 v41, v42, v43\n\t"
"v_cvt_pk_bf16_f32 v42, v44, v45\n\t"
"v_cvt_pk_bf16_f32 v43, v46, v47\n\t"
"v_mfma_f32_16x16x32_bf16 v[84:87], a[216:219], v[128:131], v[84:87]\n\t"
"buffer_load_dwordx4 v245, s[44:47], s73 offen offset:3072 lds\n\t"
"v_mfma_f32_16x16x32_bf16 v[80:83], a[204:207], v[176:179], v[80:83]\n\t"
"v_mfma_f32_16x16x32_bf16 v[84:87], a[220:223], v[176:179], v[84:87]\n\t"
"v_mfma_f32_16x16x32_bf16 v[36:39], v[216:219], v[32:35], 0\n\t"
"v_mfma_f32_16x16x32_bf16 v[44:47], v[216:219], v[40:43], 0\n\t"
"v_mfma_f32_16x16x32_bf16 v[88:91], a[192:195], v[140:143], v[212:215]\n\t"
"v_mfma_f32_16x16x32_bf16 v[92:95], a[208:211], v[140:143], v[212:215]\n\t"
"v_max3_f32 v233, v4, v12, v20\n\t"
"v_mfma_f32_16x16x32_bf16 v[88:91], a[196:199], v[188:191], v[88:91]\n\t"
"v_mfma_f32_16x16x32_bf16 v[92:95], a[212:215], v[188:191], v[92:95]\n\t"
"v_mfma_f32_16x16x32_bf16 v[88:91], a[200:203], v[136:139], v[88:91]\n\t"
"s_nop 0\n\t"
"v_max3_f32 v233, v233, v28, v36\n\t"
"v_mfma_f32_16x16x32_bf16 v[92:95], a[216:219], v[136:139], v[92:95]\n\t"
"s_waitcnt vmcnt(27) lgkmcnt(0)\n\t"
"v_max_f32_e64 v233, v233, v44\n\t"
"v_mfma_f32_16x16x32_bf16 v[88:91], a[204:207], v[184:187], v[88:91]\n\t"
"s_barrier\n\t"
"v_cmp_nge_f32_e32 vcc, s60, v233\n\t"
"v_mfma_f32_16x16x32_bf16 v[92:95], a[220:223], v[184:187], v[92:95]\n\t"
"s_cbranch_vccnz CS2_%=\n\t"
"CB2_%=:\n\t"
"v_mfma_f32_16x16x32_bf16 a[0:3], a[224:227], v[0:3], a[0:3]\n\t"
"s_add_u32 m0, s72, 16384\n\t"
"v_add_u32_e32 v220, s69, v220\n\t"
"v_mfma_f32_16x16x32_bf16 a[4:7], a[228:231], v[0:3], a[4:7]\n\t"
"s_add_u32 s73, s83, 32768\n\t"
"v_mfma_f32_16x16x32_bf16 a[8:11], a[232:235], v[0:3], a[8:11]\n\t"
"v_exp_f32_e64 v48, v48\n\t"
"v_mfma_f32_16x16x32_bf16 a[12:15], a[236:239], v[0:3], a[12:15]\n\t"
"s_and_b32 s73, s73, 0xfffff\n\t"
"v_exp_f32_e64 v49, v49\n\t"
"v_mfma_f32_16x16x32_bf16 a[16:19], a[240:243], v[0:3], a[16:19]\n\t"
"buffer_load_dwordx4 v242, s[44:47], s73 offen offset:0 lds\n\t"
"v_add_f32_e64 v227, v227, v4\n\t"
"v_mfma_f32_16x16x32_bf16 a[20:23], a[244:247], v[0:3], a[20:23]\n\t"
"v_exp_f32_e64 v50, v50\n\t"
"v_mfma_f32_16x16x32_bf16 a[24:27], a[248:251], v[0:3], a[24:27]\n\t"
"buffer_load_dwordx4 v243, s[44:47], s73 offen offset:1024 lds\n\t"
"v_add_f32_e64 v228, v228, v12\n\t"
"v_mfma_f32_16x16x32_bf16 a[28:31], a[252:255], v[0:3], a[28:31]\n\t"
"v_exp_f32_e64 v51, v51\n\t"
"v_mfma_f32_16x16x32_bf16 a[32:35], a[224:227], v[8:11], a[32:35]\n\t"
"buffer_load_dwordx4 v244, s[44:47], s73 offen offset:2048 lds\n\t"
"v_add_f32_e64 v229, v229, v20\n\t"
"v_mfma_f32_16x16x32_bf16 a[36:39], a[228:231], v[8:11], a[36:39]\n\t"
"buffer_load_dwordx4 v245, s[44:47], s73 offen offset:3072 lds\n\t"
"v_add_f32_e64 v230, v230, v28\n\t"
"v_mfma_f32_16x16x32_bf16 a[40:43], a[232:235], v[8:11], a[40:43]\n\t"
"v_exp_f32_e64 v52, v52\n\t"
"v_mfma_f32_16x16x32_bf16 a[44:47], a[236:239], v[8:11], a[44:47]\n\t"
"s_add_u32 m0, s72, 81920\n\t"
"v_exp_f32_e64 v53, v53\n\t"
"v_mfma_f32_16x16x32_bf16 a[48:51], a[240:243], v[8:11], a[48:51]\n\t"
"v_exp_f32_e64 v54, v54\n\t"
"v_mfma_f32_16x16x32_bf16 a[52:55], a[244:247], v[8:11], a[52:55]\n\t"
"s_add_u32 s73, s83, 65536\n\t"
"v_exp_f32_e64 v55, v55\n\t"
"v_mfma_f32_16x16x32_bf16 a[56:59], a[248:251], v[8:11], a[56:59]\n\t"
"s_and_b32 s73, s73, 0xfffff\n\t"
"v_exp_f32_e64 v56, v56\n\t"
"v_mfma_f32_16x16x32_bf16 a[60:63], a[252:255], v[8:11], a[60:63]\n\t"
"v_exp_f32_e64 v57, v57\n\t"
"v_mfma_f32_16x16x32_bf16 a[64:67], a[224:227], v[16:19], a[64:67]\n\t"
"buffer_load_dwordx4 v242, s[44:47], s73 offen offset:0 lds\n\t"
"v_add_f32_e64 v231, v231, v36\n\t"
"v_mfma_f32_16x16x32_bf16 a[68:71], a[228:231], v[16:19], a[68:71]\n\t"
"v_exp_f32_e64 v58, v58\n\t"
"v_mfma_f32_16x16x32_bf16 a[72:75], a[232:235], v[16:19], a[72:75]\n\t"
"buffer_load_dwordx4 v243, s[44:47], s73 offen offset:1024 lds\n\t"
"v_add_f32_e64 v232, v232, v44\n\t"
"v_mfma_f32_16x16x32_bf16 a[76:79], a[236:239], v[16:19], a[76:79]\n\t"
"buffer_load_dwordx4 v244, s[44:47], s73 offen offset:2048 lds\n\t"
"v_mfma_f32_16x16x32_bf16 a[80:83], a[240:243], v[16:19], a[80:83]\n\t"
"v_exp_f32_e64 v59, v59\n\t"
"v_mfma_f32_16x16x32_bf16 a[84:87], a[244:247], v[16:19], a[84:87]\n\t"
"buffer_load_dwordx4 v245, s[44:47], s73 offen offset:3072 lds\n\t"
"v_mfma_f32_16x16x32_bf16 a[88:91], a[248:251], v[16:19], a[88:91]\n\t"
"v_exp_f32_e64 v60, v60\n\t"
"v_mfma_f32_16x16x32_bf16 a[92:95], a[252:255], v[16:19], a[92:95]\n\t"
"v_exp_f32_e64 v61, v61\n\t"
"v_mfma_f32_16x16x32_bf16 a[96:99], a[224:227], v[24:27], a[96:99]\n\t"
"ds_read_b128 a[192:195], v220 offset:0\n\t"
"v_mfma_f32_16x16x32_bf16 a[100:103], a[228:231], v[24:27], a[100:103]\n\t"
"ds_read_b128 a[196:199], v220 offset:512\n\t"
"v_mfma_f32_16x16x32_bf16 a[104:107], a[232:235], v[24:27], a[104:107]\n\t"
"v_exp_f32_e64 v62, v62\n\t"
"v_mfma_f32_16x16x32_bf16 a[108:111], a[236:239], v[24:27], a[108:111]\n\t"
"ds_read_b128 a[200:203], v220 offset:1024\n\t"
"v_mfma_f32_16x16x32_bf16 a[112:115], a[240:243], v[24:27], a[112:115]\n\t"
"v_exp_f32_e64 v63, v63\n\t"
"v_mfma_f32_16x16x32_bf16 a[116:119], a[244:247], v[24:27], a[116:119]\n\t"
"ds_read_b128 a[204:207], v220 offset:1536\n\t"
"v_mfma_f32_16x16x32_bf16 a[120:123], a[248:251], v[24:27], a[120:123]\n\t"
"v_exp_f32_e64 v64, v64\n\t"
"v_mfma_f32_16x16x32_bf16 a[124:127], a[252:255], v[24:27], a[124:127]\n\t"
"ds_read_b128 a[208:211], v220 offset:4096\n\t"
"v_mfma_f32_16x16x32_bf16 a[128:131], a[224:227], v[32:35], a[128:131]\n\t"
"v_exp_f32_e64 v65, v65\n\t"
"v_mfma_f32_16x16x32_bf16 a[132:135], a[228:231], v[32:35], a[132:135]\n\t"
"ds_read_b128 a[212:215], v220 offset:4608\n\t"
"v_mfma_f32_16x16x32_bf16 a[136:139], a[232:235], v[32:35], a[136:139]\n\t"
"v_exp_f32_e64 v66, v66\n\t"
"v_mfma_f32_16x16x32_bf16 a[140:143], a[236:239], v[32:35], a[140:143]\n\t"
"ds_read_b128 a[216:219], v220 offset:5120\n\t"
"v_mfma_f32_16x16x32_bf16 a[144:147], a[240:243], v[32:35], a[144:147]\n\t"
"v_exp_f32_e64 v67, v67\n\t"
"v_mfma_f32_16x16x32_bf16 a[148:151], a[244:247], v[32:35], a[148:151]\n\t"
"ds_read_b128 a[220:223], v220 offset:5632\n\t"
"v_mfma_f32_16x16x32_bf16 a[152:155], a[248:251], v[32:35], a[152:155]\n\t"
"v_exp_f32_e64 v68, v68\n\t"
"v_mfma_f32_16x16x32_bf16 a[156:159], a[252:255], v[32:35], a[156:159]\n\t"
"v_exp_f32_e64 v69, v69\n\t"
"v_mfma_f32_16x16x32_bf16 a[160:163], a[224:227], v[40:43], a[160:163]\n\t"
"v_exp_f32_e64 v70, v70\n\t"
"v_mfma_f32_16x16x32_bf16 a[164:167], a[228:231], v[40:43], a[164:167]\n\t"
"v_exp_f32_e64 v71, v71\n\t"
"v_mfma_f32_16x16x32_bf16 a[168:171], a[232:235], v[40:43], a[168:171]\n\t"
"v_exp_f32_e64 v72, v72\n\t"
"v_mfma_f32_16x16x32_bf16 a[172:175], a[236:239], v[40:43], a[172:175]\n\t"
"v_exp_f32_e64 v73, v73\n\t"
"v_mfma_f32_16x16x32_bf16 a[176:179], a[240:243], v[40:43], a[176:179]\n\t"
"v_exp_f32_e64 v74, v74\n\t"
"v_mfma_f32_16x16x32_bf16 a[180:183], a[244:247], v[40:43], a[180:183]\n\t"
"s_waitcnt lgkmcnt(0)\n\t"
"v_exp_f32_e32 v75, v75\n\t"
"v_mfma_f32_16x16x32_bf16 a[184:187], a[248:251], v[40:43], a[184:187]\n\t"
"v_exp_f32_e64 v76, v76\n\t"
"v_mfma_f32_16x16x32_bf16 a[188:191], a[252:255], v[40:43], a[188:191]\n\t"
"v_exp_f32_e32 v77, v77\n\t"
"v_exp_f32_e32 v78, v78\n\t"
"v_exp_f32_e64 v79, v79\n\t"
"v_mfma_f32_16x16x32_bf16 v[0:3], a[192:195], v[100:103], v[192:195]\n\t"
"v_add_u32_e32 v221, s70, v221\n\t"
"v_add_u32_e32 v222, s70, v222\n\t"
"v_mfma_f32_16x16x32_bf16 v[4:7], a[208:211], v[100:103], v[192:195]\n\t"
"s_nop 0\n\t"
"ds_read_b64_tr_b16 a[224:225], v221 offset:0\n\t"
"v_cvt_pk_bf16_f32 v48, v48, v49\n\t"
"v_mfma_f32_16x16x32_bf16 v[0:3], a[196:199], v[148:151], v[0:3]\n\t"
"s_mov_b32 s70, s69\n\t"
"v_cvt_pk_bf16_f32 v49, v50, v51\n\t"
"v_cvt_pk_bf16_f32 v50, v52, v53\n\t"
"v_mfma_f32_16x16x32_bf16 v[4:7], a[212:215], v[148:151], v[4:7]\n\t"
"ds_read_b64_tr_b16 a[226:227], v221 offset:4096\n\t"
"v_cvt_pk_bf16_f32 v51, v54, v55\n\t"
"v_mfma_f32_16x16x32_bf16 v[0:3], a[200:203], v[96:99], v[0:3]\n\t"
"v_cvt_pk_bf16_f32 v56, v56, v57\n\t"
"v_cvt_pk_bf16_f32 v57, v58, v59\n\t"
"v_mfma_f32_16x16x32_bf16 v[4:7], a[216:219], v[96:99], v[4:7]\n\t"
"ds_read_b64_tr_b16 a[228:229], v222 offset:0\n\t"
"v_cvt_pk_bf16_f32 v58, v60, v61\n\t"
"v_mfma_f32_16x16x32_bf16 v[0:3], a[204:207], v[144:147], v[0:3]\n\t"
"s_add_u32 s71, s68, 16384\n\t"
"v_cvt_pk_bf16_f32 v59, v62, v63\n\t"
"v_mfma_f32_16x16x32_bf16 v[4:7], a[220:223], v[144:147], v[4:7]\n\t"
"ds_read_b64_tr_b16 a[230:231], v222 offset:4096\n\t"
"v_mfma_f32_16x16x32_bf16 v[8:11], a[192:195], v[108:111], v[196:199]\n\t"
"v_exp_f32_e64 v80, v80\n\t"
"v_mfma_f32_16x16x32_bf16 v[12:15], a[208:211], v[108:111], v[196:199]\n\t"
"ds_read_b64_tr_b16 a[232:233], v221 offset:512\n\t"
"v_mfma_f32_16x16x32_bf16 v[8:11], a[196:199], v[156:159], v[8:11]\n\t"
"v_exp_f32_e64 v81, v81\n\t"
"v_mfma_f32_16x16x32_bf16 v[12:15], a[212:215], v[156:159], v[12:15]\n\t"
"s_and_b32 s71, s71, 65535\n\t"
"ds_read_b64_tr_b16 a[234:235], v221 offset:4608\n\t"
"v_cvt_pk_bf16_f32 v64, v64, v65\n\t"
"v_mfma_f32_16x16x32_bf16 v[52:55], v[216:219], v[48:51], 0\n\t"
"v_cvt_pk_bf16_f32 v65, v66, v67\n\t"
"v_cvt_pk_bf16_f32 v66, v68, v69\n\t"
"v_mfma_f32_16x16x32_bf16 v[60:63], v[216:219], v[56:59], 0\n\t"
"ds_read_b64_tr_b16 a[236:237], v222 offset:512\n\t"
"v_cvt_pk_bf16_f32 v67, v70, v71\n\t"
"v_mfma_f32_16x16x32_bf16 v[8:11], a[200:203], v[104:107], v[8:11]\n\t"
"v_cvt_pk_bf16_f32 v72, v72, v73\n\t"
"v_cvt_pk_bf16_f32 v73, v74, v75\n\t"
"v_mfma_f32_16x16x32_bf16 v[12:15], a[216:219], v[104:107], v[12:15]\n\t"
"ds_read_b64_tr_b16 a[238:239], v222 offset:4608\n\t"
"v_cvt_pk_bf16_f32 v74, v76, v77\n\t"
"v_mfma_f32_16x16x32_bf16 v[8:11], a[204:207], v[152:155], v[8:11]\n\t"
"s_sub_u32 s69, s71, s68\n\t"
"v_cvt_pk_bf16_f32 v75, v78, v79\n\t"
"v_mfma_f32_16x16x32_bf16 v[12:15], a[220:223], v[152:155], v[12:15]\n\t"
"ds_read_b64_tr_b16 a[240:241], v221 offset:1024\n\t"
"v_mfma_f32_16x16x32_bf16 v[16:19], a[192:195], v[116:119], v[200:203]\n\t"
"v_exp_f32_e32 v82, v82\n\t"
"v_mfma_f32_16x16x32_bf16 v[20:23], a[208:211], v[116:119], v[200:203]\n\t"
"ds_read_b64_tr_b16 a[242:243], v221 offset:5120\n\t"
"v_mfma_f32_16x16x32_bf16 v[16:19], a[196:199], v[164:167], v[16:19]\n\t"
"v_exp_f32_e64 v83, v83\n\t"
"v_mfma_f32_16x16x32_bf16 v[20:23], a[212:215], v[164:167], v[20:23]\n\t"
"s_mov_b32 s68, s71\n\t"
"ds_read_b64_tr_b16 a[244:245], v222 offset:1024\n\t"
"v_mfma_f32_16x16x32_bf16 v[16:19], a[200:203], v[112:115], v[16:19]\n\t"
"v_exp_f32_e32 v84, v84\n\t"
"v_mfma_f32_16x16x32_bf16 v[20:23], a[216:219], v[112:115], v[20:23]\n\t"
"ds_read_b64_tr_b16 a[246:247], v222 offset:5120\n\t"
"v_mfma_f32_16x16x32_bf16 v[68:71], v[216:219], v[64:67], 0\n\t"
"v_exp_f32_e64 v85, v85\n\t"
"v_mfma_f32_16x16x32_bf16 v[76:79], v[216:219], v[72:75], 0\n\t"
"ds_read_b64_tr_b16 a[248:249], v221 offset:1536\n\t"
"v_mfma_f32_16x16x32_bf16 v[16:19], a[204:207], v[160:163], v[16:19]\n\t"
"s_add_u32 s59, s59, 1\n\t"
"v_exp_f32_e32 v86, v86\n\t"
"v_mfma_f32_16x16x32_bf16 v[20:23], a[220:223], v[160:163], v[20:23]\n\t"
"ds_read_b64_tr_b16 a[250:251], v221 offset:5632\n\t"
"v_mfma_f32_16x16x32_bf16 v[24:27], a[192:195], v[124:127], v[204:207]\n\t"
"v_exp_f32_e64 v87, v87\n\t"
"v_mfma_f32_16x16x32_bf16 v[28:31], a[208:211], v[124:127], v[204:207]\n\t"
"ds_read_b64_tr_b16 a[252:253], v222 offset:1536\n\t"
"v_mfma_f32_16x16x32_bf16 v[24:27], a[196:199], v[172:175], v[24:27]\n\t"
"v_exp_f32_e64 v88, v88\n\t"
"v_mfma_f32_16x16x32_bf16 v[28:31], a[212:215], v[172:175], v[28:31]\n\t"
"ds_read_b64_tr_b16 a[254:255], v222 offset:5632\n\t"
"v_mfma_f32_16x16x32_bf16 v[24:27], a[200:203], v[120:123], v[24:27]\n\t"
"v_exp_f32_e64 v89, v89\n\t"
"v_mfma_f32_16x16x32_bf16 v[28:31], a[216:219], v[120:123], v[28:31]\n\t"
"v_exp_f32_e64 v90, v90\n\t"
"v_mfma_f32_16x16x32_bf16 v[24:27], a[204:207], v[168:171], v[24:27]\n\t"
"v_exp_f32_e64 v91, v91\n\t"
"v_mfma_f32_16x16x32_bf16 v[28:31], a[220:223], v[168:171], v[28:31]\n\t"
"v_exp_f32_e64 v92, v92\n\t"
"v_mfma_f32_16x16x32_bf16 v[32:35], a[192:195], v[132:135], v[208:211]\n\t"
"v_exp_f32_e64 v93, v93\n\t"
"v_mfma_f32_16x16x32_bf16 v[36:39], a[208:211], v[132:135], v[208:211]\n\t"
"v_exp_f32_e32 v94, v94\n\t"
"v_exp_f32_e32 v95, v95\n\t"
"v_mfma_f32_16x16x32_bf16 v[32:35], a[196:199], v[180:183], v[32:35]\n\t"
"v_cvt_pk_bf16_f32 v80, v80, v81\n\t"
"v_cvt_pk_bf16_f32 v81, v82, v83\n\t"
"v_mfma_f32_16x16x32_bf16 v[36:39], a[212:215], v[180:183], v[36:39]\n\t"
"v_cvt_pk_bf16_f32 v82, v84, v85\n\t"
"v_cvt_pk_bf16_f32 v83, v86, v87\n\t"
"v_mfma_f32_16x16x32_bf16 v[32:35], a[200:203], v[128:131], v[32:35]\n\t"
"v_cvt_pk_bf16_f32 v88, v88, v89\n\t"
"v_cvt_pk_bf16_f32 v89, v90, v91\n\t"
"v_mfma_f32_16x16x32_bf16 v[36:39], a[216:219], v[128:131], v[36:39]\n\t"
"v_cvt_pk_bf16_f32 v90, v92, v93\n\t"
"v_cvt_pk_bf16_f32 v91, v94, v95\n\t"
"v_mfma_f32_16x16x32_bf16 v[32:35], a[204:207], v[176:179], v[32:35]\n\t"
"v_mfma_f32_16x16x32_bf16 v[36:39], a[220:223], v[176:179], v[36:39]\n\t"
"v_mfma_f32_16x16x32_bf16 v[84:87], v[216:219], v[80:83], 0\n\t"
"v_mfma_f32_16x16x32_bf16 v[92:95], v[216:219], v[88:91], 0\n\t"
"v_mfma_f32_16x16x32_bf16 v[40:43], a[192:195], v[140:143], v[212:215]\n\t"
"v_mfma_f32_16x16x32_bf16 v[44:47], a[208:211], v[140:143], v[212:215]\n\t"
"v_max3_f32 v233, v52, v60, v68\n\t"
"v_mfma_f32_16x16x32_bf16 v[40:43], a[196:199], v[188:191], v[40:43]\n\t"
"v_mfma_f32_16x16x32_bf16 v[44:47], a[212:215], v[188:191], v[44:47]\n\t"
"v_mfma_f32_16x16x32_bf16 v[40:43], a[200:203], v[136:139], v[40:43]\n\t"
"s_nop 0\n\t"
"v_max3_f32 v233, v233, v76, v84\n\t"
"v_mfma_f32_16x16x32_bf16 v[44:47], a[216:219], v[136:139], v[44:47]\n\t"
"s_waitcnt vmcnt(26) lgkmcnt(0)\n\t"
"v_max_f32_e64 v233, v233, v92\n\t"
"v_mfma_f32_16x16x32_bf16 v[40:43], a[204:207], v[184:187], v[40:43]\n\t"
"s_barrier\n\t"
"v_cmp_nge_f32_e32 vcc, s60, v233\n\t"
"v_mfma_f32_16x16x32_bf16 v[44:47], a[220:223], v[184:187], v[44:47]\n\t"
"s_cbranch_vccnz CS3_%=\n\t"
"CB3_%=:\n\t"
"v_mfma_f32_16x16x32_bf16 a[0:3], a[224:227], v[48:51], a[0:3]\n\t"
"s_add_u32 m0, s72, 32768\n\t"
"v_add_u32_e32 v220, s69, v220\n\t"
"v_mfma_f32_16x16x32_bf16 a[4:7], a[228:231], v[48:51], a[4:7]\n\t"
"s_add_u32 s73, s83, 98304\n\t"
"v_mfma_f32_16x16x32_bf16 a[8:11], a[232:235], v[48:51], a[8:11]\n\t"
"v_exp_f32_e64 v0, v0\n\t"
"v_mfma_f32_16x16x32_bf16 a[12:15], a[236:239], v[48:51], a[12:15]\n\t"
"s_and_b32 s73, s73, 0xfffff\n\t"
"v_exp_f32_e64 v1, v1\n\t"
"v_mfma_f32_16x16x32_bf16 a[16:19], a[240:243], v[48:51], a[16:19]\n\t"
"buffer_load_dwordx4 v242, s[44:47], s73 offen offset:0 lds\n\t"
"v_add_f32_e64 v227, v227, v52\n\t"
"v_mfma_f32_16x16x32_bf16 a[20:23], a[244:247], v[48:51], a[20:23]\n\t"
"v_exp_f32_e64 v2, v2\n\t"
"v_mfma_f32_16x16x32_bf16 a[24:27], a[248:251], v[48:51], a[24:27]\n\t"
"buffer_load_dwordx4 v243, s[44:47], s73 offen offset:1024 lds\n\t"
"v_add_f32_e64 v228, v228, v60\n\t"
"v_mfma_f32_16x16x32_bf16 a[28:31], a[252:255], v[48:51], a[28:31]\n\t"
"v_exp_f32_e64 v3, v3\n\t"
"v_mfma_f32_16x16x32_bf16 a[32:35], a[224:227], v[56:59], a[32:35]\n\t"
"buffer_load_dwordx4 v244, s[44:47], s73 offen offset:2048 lds\n\t"
"v_add_f32_e64 v229, v229, v68\n\t"
"v_mfma_f32_16x16x32_bf16 a[36:39], a[228:231], v[56:59], a[36:39]\n\t"
"buffer_load_dwordx4 v245, s[44:47], s73 offen offset:3072 lds\n\t"
"v_add_f32_e64 v230, v230, v76\n\t"
"v_mfma_f32_16x16x32_bf16 a[40:43], a[232:235], v[56:59], a[40:43]\n\t"
"v_exp_f32_e64 v4, v4\n\t"
"v_mfma_f32_16x16x32_bf16 a[44:47], a[236:239], v[56:59], a[44:47]\n\t"
"s_add_u32 m0, s72, 98304\n\t"
"v_exp_f32_e64 v5, v5\n\t"
"v_mfma_f32_16x16x32_bf16 a[48:51], a[240:243], v[56:59], a[48:51]\n\t"
"v_exp_f32_e64 v6, v6\n\t"
"v_mfma_f32_16x16x32_bf16 a[52:55], a[244:247], v[56:59], a[52:55]\n\t"
"s_add_u32 s73, s83, 163840\n\t"
"v_exp_f32_e64 v7, v7\n\t"
"v_mfma_f32_16x16x32_bf16 a[56:59], a[248:251], v[56:59], a[56:59]\n\t"
"s_and_b32 s73, s73, 0xfffff\n\t"
"v_exp_f32_e64 v8, v8\n\t"
"v_mfma_f32_16x16x32_bf16 a[60:63], a[252:255], v[56:59], a[60:63]\n\t"
"v_exp_f32_e64 v9, v9\n\t"
"v_mfma_f32_16x16x32_bf16 a[64:67], a[224:227], v[64:67], a[64:67]\n\t"
"buffer_load_dwordx4 v242, s[44:47], s73 offen offset:0 lds\n\t"
"v_add_f32_e64 v231, v231, v84\n\t"
"v_mfma_f32_16x16x32_bf16 a[68:71], a[228:231], v[64:67], a[68:71]\n\t"
"v_exp_f32_e64 v10, v10\n\t"
"v_mfma_f32_16x16x32_bf16 a[72:75], a[232:235], v[64:67], a[72:75]\n\t"
"buffer_load_dwordx4 v243, s[44:47], s73 offen offset:1024 lds\n\t"
"v_add_f32_e64 v232, v232, v92\n\t"
"v_mfma_f32_16x16x32_bf16 a[76:79], a[236:239], v[64:67], a[76:79]\n\t"
"buffer_load_dwordx4 v244, s[44:47], s73 offen offset:2048 lds\n\t"
"v_mfma_f32_16x16x32_bf16 a[80:83], a[240:243], v[64:67], a[80:83]\n\t"
"v_exp_f32_e64 v11, v11\n\t"
"v_mfma_f32_16x16x32_bf16 a[84:87], a[244:247], v[64:67], a[84:87]\n\t"
"buffer_load_dwordx4 v245, s[44:47], s73 offen offset:3072 lds\n\t"
"v_mfma_f32_16x16x32_bf16 a[88:91], a[248:251], v[64:67], a[88:91]\n\t"
"v_exp_f32_e64 v12, v12\n\t"
"v_mfma_f32_16x16x32_bf16 a[92:95], a[252:255], v[64:67], a[92:95]\n\t"
"v_exp_f32_e64 v13, v13\n\t"
"v_mfma_f32_16x16x32_bf16 a[96:99], a[224:227], v[72:75], a[96:99]\n\t"
"ds_read_b128 a[192:195], v220 offset:0\n\t"
"v_mfma_f32_16x16x32_bf16 a[100:103], a[228:231], v[72:75], a[100:103]\n\t"
"ds_read_b128 a[196:199], v220 offset:512\n\t"
"v_mfma_f32_16x16x32_bf16 a[104:107], a[232:235], v[72:75], a[104:107]\n\t"
"v_exp_f32_e64 v14, v14\n\t"
"v_mfma_f32_16x16x32_bf16 a[108:111], a[236:239], v[72:75], a[108:111]\n\t"
"ds_read_b128 a[200:203], v220 offset:1024\n\t"
"v_mfma_f32_16x16x32_bf16 a[112:115], a[240:243], v[72:75], a[112:115]\n\t"
"v_exp_f32_e64 v15, v15\n\t"
"v_mfma_f32_16x16x32_bf16 a[116:119], a[244:247], v[72:75], a[116:119]\n\t"
"ds_read_b128 a[204:207], v220 offset:1536\n\t"
"v_mfma_f32_16x16x32_bf16 a[120:123], a[248:251], v[72:75], a[120:123]\n\t"
"v_exp_f32_e64 v16, v16\n\t"
"v_mfma_f32_16x16x32_bf16 a[124:127], a[252:255], v[72:75], a[124:127]\n\t"
"ds_read_b128 a[208:211], v220 offset:4096\n\t"
"v_mfma_f32_16x16x32_bf16 a[128:131], a[224:227], v[80:83], a[128:131]\n\t"
"v_exp_f32_e64 v17, v17\n\t"
"v_mfma_f32_16x16x32_bf16 a[132:135], a[228:231], v[80:83], a[132:135]\n\t"
"ds_read_b128 a[212:215], v220 offset:4608\n\t"
"v_mfma_f32_16x16x32_bf16 a[136:139], a[232:235], v[80:83], a[136:139]\n\t"
"v_exp_f32_e64 v18, v18\n\t"
"v_mfma_f32_16x16x32_bf16 a[140:143], a[236:239], v[80:83], a[140:143]\n\t"
"ds_read_b128 a[216:219], v220 offset:5120\n\t"
"v_mfma_f32_16x16x32_bf16 a[144:147], a[240:243], v[80:83], a[144:147]\n\t"
"v_exp_f32_e64 v19, v19\n\t"
"v_mfma_f32_16x16x32_bf16 a[148:151], a[244:247], v[80:83], a[148:151]\n\t"
"ds_read_b128 a[220:223], v220 offset:5632\n\t"
"v_mfma_f32_16x16x32_bf16 a[152:155], a[248:251], v[80:83], a[152:155]\n\t"
"v_exp_f32_e64 v20, v20\n\t"
"v_mfma_f32_16x16x32_bf16 a[156:159], a[252:255], v[80:83], a[156:159]\n\t"
"v_exp_f32_e64 v21, v21\n\t"
"v_mfma_f32_16x16x32_bf16 a[160:163], a[224:227], v[88:91], a[160:163]\n\t"
"v_exp_f32_e64 v22, v22\n\t"
"v_mfma_f32_16x16x32_bf16 a[164:167], a[228:231], v[88:91], a[164:167]\n\t"
"v_exp_f32_e64 v23, v23\n\t"
"v_mfma_f32_16x16x32_bf16 a[168:171], a[232:235], v[88:91], a[168:171]\n\t"
"v_exp_f32_e64 v24, v24\n\t"
"v_mfma_f32_16x16x32_bf16 a[172:175], a[236:239], v[88:91], a[172:175]\n\t"
"v_exp_f32_e64 v25, v25\n\t"
"v_mfma_f32_16x16x32_bf16 a[176:179], a[240:243], v[88:91], a[176:179]\n\t"
"v_exp_f32_e64 v26, v26\n\t"
"v_mfma_f32_16x16x32_bf16 a[180:183], a[244:247], v[88:91], a[180:183]\n\t"
"s_waitcnt lgkmcnt(0)\n\t"
"v_exp_f32_e32 v27, v27\n\t"
"v_mfma_f32_16x16x32_bf16 a[184:187], a[248:251], v[88:91], a[184:187]\n\t"
"v_exp_f32_e64 v28, v28\n\t"
"v_mfma_f32_16x16x32_bf16 a[188:191], a[252:255], v[88:91], a[188:191]\n\t"
"v_exp_f32_e32 v29, v29\n\t"
"v_exp_f32_e32 v30, v30\n\t"
"v_exp_f32_e64 v31, v31\n\t"
"v_mfma_f32_16x16x32_bf16 v[48:51], a[192:195], v[100:103], v[192:195]\n\t"
"v_add_u32_e32 v221, s70, v221\n\t"
"v_add_u32_e32 v222, s70, v222\n\t"
"v_mfma_f32_16x16x32_bf16 v[52:55], a[208:211], v[100:103], v[192:195]\n\t"
"s_nop 0\n\t"
"ds_read_b64_tr_b16 a[224:225], v221 offset:0\n\t"
"v_cvt_pk_bf16_f32 v0, v0, v1\n\t"
"v_mfma_f32_16x16x32_bf16 v[48:51], a[196:199], v[148:151], v[48:51]\n\t"
"s_mov_b32 s70, s69\n\t"
"v_cvt_pk_bf16_f32 v1, v2, v3\n\t"
"v_cvt_pk_bf16_f32 v2, v4, v5\n\t"
"v_mfma_f32_16x16x32_bf16 v[52:55], a[212:215], v[148:151], v[52:55]\n\t"
"ds_read_b64_tr_b16 a[226:227], v221 offset:4096\n\t"
"v_cvt_pk_bf16_f32 v3, v6, v7\n\t"
"v_mfma_f32_16x16x32_bf16 v[48:51], a[200:203], v[96:99], v[48:51]\n\t"
"v_cvt_pk_bf16_f32 v8, v8, v9\n\t"
"v_cvt_pk_bf16_f32 v9, v10, v11\n\t"
"v_mfma_f32_16x16x32_bf16 v[52:55], a[216:219], v[96:99], v[52:55]\n\t"
"ds_read_b64_tr_b16 a[228:229], v222 offset:0\n\t"
"v_cvt_pk_bf16_f32 v10, v12, v13\n\t"
"v_mfma_f32_16x16x32_bf16 v[48:51], a[204:207], v[144:147], v[48:51]\n\t"
"s_add_u32 s71, s68, 16384\n\t"
"v_cvt_pk_bf16_f32 v11, v14, v15\n\t"
"v_mfma_f32_16x16x32_bf16 v[52:55], a[220:223], v[144:147], v[52:55]\n\t"
"ds_read_b64_tr_b16 a[230:231], v222 offset:4096\n\t"
"v_mfma_f32_16x16x32_bf16 v[56:59], a[192:195], v[108:111], v[196:199]\n\t"
"v_exp_f32_e64 v32, v32\n\t"
"v_mfma_f32_16x16x32_bf16 v[60:63], a[208:211], v[108:111], v[196:199]\n\t"
"ds_read_b64_tr_b16 a[232:233], v221 offset:512\n\t"
"v_mfma_f32_16x16x32_bf16 v[56:59], a[196:199], v[156:159], v[56:59]\n\t"
"v_exp_f32_e64 v33, v33\n\t"
"v_mfma_f32_16x16x32_bf16 v[60:63], a[212:215], v[156:159], v[60:63]\n\t"
"s_and_b32 s71, s71, 65535\n\t"
"ds_read_b64_tr_b16 a[234:235], v221 offset:4608\n\t"
"v_cvt_pk_bf16_f32 v16, v16, v17\n\t"
"v_mfma_f32_16x16x32_bf16 v[4:7], v[216:219], v[0:3], 0\n\t"
"v_cvt_pk_bf16_f32 v17, v18, v19\n\t"
"v_cvt_pk_bf16_f32 v18, v20, v21\n\t"
"v_mfma_f32_16x16x32_bf16 v[12:15], v[216:219], v[8:11], 0\n\t"
"ds_read_b64_tr_b16 a[236:237], v222 offset:512\n\t"
"v_cvt_pk_bf16_f32 v19, v22, v23\n\t"
"v_mfma_f32_16x16x32_bf16 v[56:59], a[200:203], v[104:107], v[56:59]\n\t"
"v_cvt_pk_bf16_f32 v24, v24, v25\n\t"
"v_cvt_pk_bf16_f32 v25, v26, v27\n\t"
"v_mfma_f32_16x16x32_bf16 v[60:63], a[216:219], v[104:107], v[60:63]\n\t"
"ds_read_b64_tr_b16 a[238:239], v222 offset:4608\n\t"
"v_cvt_pk_bf16_f32 v26, v28, v29\n\t"
"v_mfma_f32_16x16x32_bf16 v[56:59], a[204:207], v[152:155], v[56:59]\n\t"
"s_sub_u32 s69, s71, s68\n\t"
"v_cvt_pk_bf16_f32 v27, v30, v31\n\t"
"v_mfma_f32_16x16x32_bf16 v[60:63], a[220:223], v[152:155], v[60:63]\n\t"
"ds_read_b64_tr_b16 a[240:241], v221 offset:1024\n\t"
"v_mfma_f32_16x16x32_bf16 v[64:67], a[192:195], v[116:119], v[200:203]\n\t"
"v_exp_f32_e32 v34, v34\n\t"
"v_mfma_f32_16x16x32_bf16 v[68:71], a[208:211], v[116:119], v[200:203]\n\t"
"ds_read_b64_tr_b16 a[242:243], v221 offset:5120\n\t"
"v_mfma_f32_16x16x32_bf16 v[64:67], a[196:199], v[164:167], v[64:67]\n\t"
"v_exp_f32_e64 v35, v35\n\t"
"v_mfma_f32_16x16x32_bf16 v[68:71], a[212:215], v[164:167], v[68:71]\n\t"
"s_mov_b32 s68, s71\n\t"
"ds_read_b64_tr_b16 a[244:245], v222 offset:1024\n\t"
"v_mfma_f32_16x16x32_bf16 v[64:67], a[200:203], v[112:115], v[64:67]\n\t"
"v_exp_f32_e32 v36, v36\n\t"
"v_mfma_f32_16x16x32_bf16 v[68:71], a[216:219], v[112:115], v[68:71]\n\t"
"ds_read_b64_tr_b16 a[246:247], v222 offset:5120\n\t"
"v_mfma_f32_16x16x32_bf16 v[20:23], v[216:219], v[16:19], 0\n\t"
"v_exp_f32_e64 v37, v37\n\t"
"v_mfma_f32_16x16x32_bf16 v[28:31], v[216:219], v[24:27], 0\n\t"
"ds_read_b64_tr_b16 a[248:249], v221 offset:1536\n\t"
"v_mfma_f32_16x16x32_bf16 v[64:67], a[204:207], v[160:163], v[64:67]\n\t"
"s_add_u32 s59, s59, 1\n\t"
"v_exp_f32_e32 v38, v38\n\t"
"v_mfma_f32_16x16x32_bf16 v[68:71], a[220:223], v[160:163], v[68:71]\n\t"
"ds_read_b64_tr_b16 a[250:251], v221 offset:5632\n\t"
"v_mfma_f32_16x16x32_bf16 v[72:75], a[192:195], v[124:127], v[204:207]\n\t"
"v_exp_f32_e64 v39, v39\n\t"
"v_mfma_f32_16x16x32_bf16 v[76:79], a[208:211], v[124:127], v[204:207]\n\t"
"ds_read_b64_tr_b16 a[252:253], v222 offset:1536\n\t"
"v_mfma_f32_16x16x32_bf16 v[72:75], a[196:199], v[172:175], v[72:75]\n\t"
"v_exp_f32_e64 v40, v40\n\t"
"v_mfma_f32_16x16x32_bf16 v[76:79], a[212:215], v[172:175], v[76:79]\n\t"
"ds_read_b64_tr_b16 a[254:255], v222 offset:5632\n\t"
"v_mfma_f32_16x16x32_bf16 v[72:75], a[200:203], v[120:123], v[72:75]\n\t"
"v_exp_f32_e64 v41, v41\n\t"
"v_mfma_f32_16x16x32_bf16 v[76:79], a[216:219], v[120:123], v[76:79]\n\t"
"v_exp_f32_e64 v42, v42\n\t"
"v_mfma_f32_16x16x32_bf16 v[72:75], a[204:207], v[168:171], v[72:75]\n\t"
"v_exp_f32_e64 v43, v43\n\t"
"v_mfma_f32_16x16x32_bf16 v[76:79], a[220:223], v[168:171], v[76:79]\n\t"
"v_exp_f32_e64 v44, v44\n\t"
"v_mfma_f32_16x16x32_bf16 v[80:83], a[192:195], v[132:135], v[208:211]\n\t"
"v_exp_f32_e64 v45, v45\n\t"
"v_mfma_f32_16x16x32_bf16 v[84:87], a[208:211], v[132:135], v[208:211]\n\t"
"v_exp_f32_e32 v46, v46\n\t"
"v_exp_f32_e32 v47, v47\n\t"
"v_mfma_f32_16x16x32_bf16 v[80:83], a[196:199], v[180:183], v[80:83]\n\t"
"v_cvt_pk_bf16_f32 v32, v32, v33\n\t"
"v_cvt_pk_bf16_f32 v33, v34, v35\n\t"
"v_mfma_f32_16x16x32_bf16 v[84:87], a[212:215], v[180:183], v[84:87]\n\t"
"v_cvt_pk_bf16_f32 v34, v36, v37\n\t"
"v_cvt_pk_bf16_f32 v35, v38, v39\n\t"
"v_mfma_f32_16x16x32_bf16 v[80:83], a[200:203], v[128:131], v[80:83]\n\t"
"v_cvt_pk_bf16_f32 v40, v40, v41\n\t"
"v_cvt_pk_bf16_f32 v41, v42, v43\n\t"
"v_mfma_f32_16x16x32_bf16 v[84:87], a[216:219], v[128:131], v[84:87]\n\t"
"v_cvt_pk_bf16_f32 v42, v44, v45\n\t"
"v_cvt_pk_bf16_f32 v43, v46, v47\n\t"
"v_mfma_f32_16x16x32_bf16 v[80:83], a[204:207], v[176:179], v[80:83]\n\t"
"v_mfma_f32_16x16x32_bf16 v[84:87], a[220:223], v[176:179], v[84:87]\n\t"
"v_mfma_f32_16x16x32_bf16 v[36:39], v[216:219], v[32:35], 0\n\t"
"v_mfma_f32_16x16x32_bf16 v[44:47], v[216:219], v[40:43], 0\n\t"
"v_mfma_f32_16x16x32_bf16 v[88:91], a[192:195], v[140:143], v[212:215]\n\t"
"v_mfma_f32_16x16x32_bf16 v[92:95], a[208:211], v[140:143], v[212:215]\n\t"
"v_max3_f32 v233, v4, v12, v20\n\t"
"v_mfma_f32_16x16x32_bf16 v[88:91], a[196:199], v[188:191], v[88:91]\n\t"
"v_mfma_f32_16x16x32_bf16 v[92:95], a[212:215], v[188:191], v[92:95]\n\t"
"v_mfma_f32_16x16x32_bf16 v[88:91], a[200:203], v[136:139], v[88:91]\n\t"
"s_nop 0\n\t"
"v_max3_f32 v233, v233, v28, v36\n\t"
"v_mfma_f32_16x16x32_bf16 v[92:95], a[216:219], v[136:139], v[92:95]\n\t"
"s_waitcnt vmcnt(25) lgkmcnt(0)\n\t"
"v_max_f32_e64 v233, v233, v44\n\t"
"v_mfma_f32_16x16x32_bf16 v[88:91], a[204:207], v[184:187], v[88:91]\n\t"
"s_barrier\n\t"
"v_cmp_nge_f32_e32 vcc, s60, v233\n\t"
"v_mfma_f32_16x16x32_bf16 v[92:95], a[220:223], v[184:187], v[92:95]\n\t"
"s_cbranch_vccnz CS4_%=\n\t"
"CB4_%=:\n\t"
"v_mfma_f32_16x16x32_bf16 a[0:3], a[224:227], v[0:3], a[0:3]\n\t"
"s_add_u32 m0, s72, 49152\n\t"
"v_add_u32_e32 v221, s70, v221\n\t"
"v_add_u32_e64 v222, s70, v222\n\t"
"v_mfma_f32_16x16x32_bf16 a[4:7], a[228:231], v[0:3], a[4:7]\n\t"
"s_add_u32 s73, s83, 196608\n\t"
"v_mfma_f32_16x16x32_bf16 a[8:11], a[232:235], v[0:3], a[8:11]\n\t"
"s_and_b32 s73, s73, 0xfffff\n\t"
"ds_read_b64_tr_b16 a[192:193], v221 offset:0\n\t"
"v_add_f32_e64 v227, v227, v4\n\t"
"v_mfma_f32_16x16x32_bf16 a[12:15], a[236:239], v[0:3], a[12:15]\n\t"
"buffer_load_dwordx4 v242, s[44:47], s73 offen offset:0 lds\n\t"
"v_add_f32_e64 v228, v228, v12\n\t"
"v_mfma_f32_16x16x32_bf16 a[16:19], a[240:243], v[0:3], a[16:19]\n\t"
"buffer_load_dwordx4 v243, s[44:47], s73 offen offset:1024 lds\n\t"
"v_add_f32_e64 v229, v229, v20\n\t"
"v_mfma_f32_16x16x32_bf16 a[20:23], a[244:247], v[0:3], a[20:23]\n\t"
"buffer_load_dwordx4 v244, s[44:47], s73 offen offset:2048 lds\n\t"
"v_add_f32_e64 v230, v230, v28\n\t"
"v_mfma_f32_16x16x32_bf16 a[24:27], a[248:251], v[0:3], a[24:27]\n\t"
"buffer_load_dwordx4 v245, s[44:47], s73 offen offset:3072 lds\n\t"
"v_add_f32_e64 v231, v231, v36\n\t"
"v_mfma_f32_16x16x32_bf16 a[28:31], a[252:255], v[0:3], a[28:31]\n\t"
"s_add_u32 m0, s72, 114688\n\t"
"ds_read_b64_tr_b16 a[194:195], v221 offset:4096\n\t"
"v_add_f32_e64 v232, v232, v44\n\t"
"v_mfma_f32_16x16x32_bf16 a[32:35], a[224:227], v[8:11], a[32:35]\n\t"
"s_add_u32 s73, s83, 229376\n\t"
"ds_read_b64_tr_b16 a[196:197], v222 offset:0\n\t"
"v_exp_f32_e64 v48, v48\n\t"
"v_mfma_f32_16x16x32_bf16 a[36:39], a[228:231], v[8:11], a[36:39]\n\t"
"s_and_b32 s73, s73, 0xfffff\n\t"
"ds_read_b64_tr_b16 a[198:199], v222 offset:4096\n\t"
"v_exp_f32_e64 v49, v49\n\t"
"v_mfma_f32_16x16x32_bf16 a[40:43], a[232:235], v[8:11], a[40:43]\n\t"
"buffer_load_dwordx4 v242, s[44:47], s73 offen offset:0 lds\n\t"
"v_exp_f32_e64 v50, v50\n\t"
"v_mfma_f32_16x16x32_bf16 a[44:47], a[236:239], v[8:11], a[44:47]\n\t"
"ds_read_b64_tr_b16 a[200:201], v221 offset:512\n\t"
"v_exp_f32_e64 v51, v51\n\t"
"v_mfma_f32_16x16x32_bf16 a[48:51], a[240:243], v[8:11], a[48:51]\n\t"
"buffer_load_dwordx4 v243, s[44:47], s73 offen offset:1024 lds\n\t"
"v_exp_f32_e64 v52, v52\n\t"
"v_mfma_f32_16x16x32_bf16 a[52:55], a[244:247], v[8:11], a[52:55]\n\t"
"buffer_load_dwordx4 v244, s[44:47], s73 offen offset:2048 lds\n\t"
"v_exp_f32_e64 v53, v53\n\t"
"v_mfma_f32_16x16x32_bf16 a[56:59], a[248:251], v[8:11], a[56:59]\n\t"
"buffer_load_dwordx4 v245, s[44:47], s73 offen offset:3072 lds\n\t"
"v_exp_f32_e64 v54, v54\n\t"
"v_mfma_f32_16x16x32_bf16 a[60:63], a[252:255], v[8:11], a[60:63]\n\t"
"s_mov_b32 m0, s72\n\t"
"ds_read_b64_tr_b16 a[202:203], v221 offset:4608\n\t"
"v_exp_f32_e32 v55, v55\n\t"
"v_mfma_f32_16x16x32_bf16 a[64:67], a[224:227], v[16:19], a[64:67]\n\t"
"s_add_u32 s73, s83, 262144\n\t"
"ds_read_b64_tr_b16 a[204:205], v222 offset:512\n\t"
"v_exp_f32_e64 v56, v56\n\t"
"v_mfma_f32_16x16x32_bf16 a[68:71], a[228:231], v[16:19], a[68:71]\n\t"
"s_and_b32 s73, s73, 0xfffff\n\t"
"v_exp_f32_e64 v57, v57\n\t"
"v_mfma_f32_16x16x32_bf16 a[72:75], a[232:235], v[16:19], a[72:75]\n\t"
"buffer_load_dwordx4 v242, s[44:47], s73 offen offset:0 lds\n\t"
"v_exp_f32_e64 v58, v58\n\t"
"v_mfma_f32_16x16x32_bf16 a[76:79], a[236:239], v[16:19], a[76:79]\n\t"
"buffer_load_dwordx4 v243, s[44:47], s73 offen offset:1024 lds\n\t"
"v_exp_f32_e64 v59, v59\n\t"
"v_mfma_f32_16x16x32_bf16 a[80:83], a[240:243], v[16:19], a[80:83]\n\t"
"buffer_load_dwordx4 v244, s[44:47], s73 offen offset:2048 lds\n\t"
"v_exp_f32_e64 v60, v60\n\t"
"v_mfma_f32_16x16x32_bf16 a[84:87], a[244:247], v[16:19], a[84:87]\n\t"
"buffer_load_dwordx4 v245, s[44:47], s73 offen offset:3072 lds\n\t"
"v_exp_f32_e64 v61, v61\n\t"
"v_mfma_f32_16x16x32_bf16 a[88:91], a[248:251], v[16:19], a[88:91]\n\t"
"ds_read_b64_tr_b16 a[206:207], v222 offset:4608\n\t"
"v_exp_f32_e64 v62, v62\n\t"
"v_mfma_f32_16x16x32_bf16 a[92:95], a[252:255], v[16:19], a[92:95]\n\t"
"ds_read_b64_tr_b16 a[208:209], v221 offset:1024\n\t"
"v_exp_f32_e64 v63, v63\n\t"
"v_mfma_f32_16x16x32_bf16 a[96:99], a[224:227], v[24:27], a[96:99]\n\t"
"ds_read_b64_tr_b16 a[210:211], v221 offset:5120\n\t"
"v_exp_f32_e64 v64, v64\n\t"
"v_mfma_f32_16x16x32_bf16 a[100:103], a[228:231], v[24:27], a[100:103]\n\t"
"ds_read_b64_tr_b16 a[212:213], v222 offset:1024\n\t"
"v_exp_f32_e64 v65, v65\n\t"
"v_mfma_f32_16x16x32_bf16 a[104:107], a[232:235], v[24:27], a[104:107]\n\t"
"v_exp_f32_e64 v66, v66\n\t"
"v_mfma_f32_16x16x32_bf16 a[108:111], a[236:239], v[24:27], a[108:111]\n\t"
"ds_read_b64_tr_b16 a[214:215], v222 offset:5120\n\t"
"v_exp_f32_e64 v67, v67\n\t"
"v_mfma_f32_16x16x32_bf16 a[112:115], a[240:243], v[24:27], a[112:115]\n\t"
"v_exp_f32_e64 v68, v68\n\t"
"v_mfma_f32_16x16x32_bf16 a[116:119], a[244:247], v[24:27], a[116:119]\n\t"
"ds_read_b64_tr_b16 a[216:217], v221 offset:1536\n\t"
"v_exp_f32_e64 v69, v69\n\t"
"v_mfma_f32_16x16x32_bf16 a[120:123], a[248:251], v[24:27], a[120:123]\n\t"
"v_exp_f32_e64 v70, v70\n\t"
"v_mfma_f32_16x16x32_bf16 a[124:127], a[252:255], v[24:27], a[124:127]\n\t"
"v_exp_f32_e64 v71, v71\n\t"
"v_mfma_f32_16x16x32_bf16 a[128:131], a[224:227], v[32:35], a[128:131]\n\t"
"ds_read_b64_tr_b16 a[218:219], v221 offset:5632\n\t"
"v_exp_f32_e64 v72, v72\n\t"
"v_mfma_f32_16x16x32_bf16 a[132:135], a[228:231], v[32:35], a[132:135]\n\t"
"v_exp_f32_e64 v73, v73\n\t"
"v_mfma_f32_16x16x32_bf16 a[136:139], a[232:235], v[32:35], a[136:139]\n\t"
"ds_read_b64_tr_b16 a[220:221], v222 offset:1536\n\t"
"v_exp_f32_e64 v74, v74\n\t"
"v_mfma_f32_16x16x32_bf16 a[140:143], a[236:239], v[32:35], a[140:143]\n\t"
"v_exp_f32_e64 v75, v75\n\t"
"v_mfma_f32_16x16x32_bf16 a[144:147], a[240:243], v[32:35], a[144:147]\n\t"
"ds_read_b64_tr_b16 a[222:223], v222 offset:5632\n\t"
"v_exp_f32_e64 v76, v76\n\t"
"v_mfma_f32_16x16x32_bf16 a[148:151], a[244:247], v[32:35], a[148:151]\n\t"
"v_exp_f32_e64 v77, v77\n\t"
"v_mfma_f32_16x16x32_bf16 a[152:155], a[248:251], v[32:35], a[152:155]\n\t"
"v_exp_f32_e64 v78, v78\n\t"
"v_mfma_f32_16x16x32_bf16 a[156:159], a[252:255], v[32:35], a[156:159]\n\t"
"v_exp_f32_e64 v79, v79\n\t"
"v_mfma_f32_16x16x32_bf16 a[160:163], a[224:227], v[40:43], a[160:163]\n\t"
"v_mfma_f32_16x16x32_bf16 a[164:167], a[228:231], v[40:43], a[164:167]\n\t"
"v_mfma_f32_16x16x32_bf16 a[168:171], a[232:235], v[40:43], a[168:171]\n\t"
"v_mfma_f32_16x16x32_bf16 a[172:175], a[236:239], v[40:43], a[172:175]\n\t"
"v_mfma_f32_16x16x32_bf16 a[176:179], a[240:243], v[40:43], a[176:179]\n\t"
"v_mfma_f32_16x16x32_bf16 a[180:183], a[244:247], v[40:43], a[180:183]\n\t"
"s_waitcnt lgkmcnt(0)\n\t"
"v_mfma_f32_16x16x32_bf16 a[184:187], a[248:251], v[40:43], a[184:187]\n\t"
"v_mfma_f32_16x16x32_bf16 a[188:191], a[252:255], v[40:43], a[188:191]\n\t"
"v_cvt_pk_bf16_f32 v48, v48, v49\n\t"
"v_cvt_pk_bf16_f32 v49, v50, v51\n\t"
"v_cvt_pk_bf16_f32 v50, v52, v53\n\t"
"v_cvt_pk_bf16_f32 v51, v54, v55\n\t"
"v_cvt_pk_bf16_f32 v56, v56, v57\n\t"
"v_cvt_pk_bf16_f32 v57, v58, v59\n\t"
"v_cvt_pk_bf16_f32 v58, v60, v61\n\t"
"v_cvt_pk_bf16_f32 v59, v62, v63\n\t"
"v_cvt_pk_bf16_f32 v64, v64, v65\n\t"
"v_cvt_pk_bf16_f32 v65, v66, v67\n\t"
"v_cvt_pk_bf16_f32 v66, v68, v69\n\t"
"v_cvt_pk_bf16_f32 v67, v70, v71\n\t"
"v_cvt_pk_bf16_f32 v72, v72, v73\n\t"
"v_cvt_pk_bf16_f32 v73, v74, v75\n\t"
"v_cvt_pk_bf16_f32 v74, v76, v77\n\t"
"v_cvt_pk_bf16_f32 v75, v78, v79\n\t"
"v_mfma_f32_16x16x32_bf16 v[52:55], v[216:219], v[48:51], 0\n\t"
"v_exp_f32_e32 v80, v80\n\t"
"v_exp_f32_e32 v81, v81\n\t"
"v_exp_f32_e32 v82, v82\n\t"
"v_exp_f32_e64 v83, v83\n\t"
"v_mfma_f32_16x16x32_bf16 v[60:63], v[216:219], v[56:59], 0\n\t"
"v_exp_f32_e32 v84, v84\n\t"
"v_exp_f32_e32 v85, v85\n\t"
"v_exp_f32_e32 v86, v86\n\t"
"v_exp_f32_e32 v87, v87\n\t"
"v_mfma_f32_16x16x32_bf16 v[68:71], v[216:219], v[64:67], 0\n\t"
"v_exp_f32_e32 v88, v88\n\t"
"v_exp_f32_e32 v89, v89\n\t"
"v_exp_f32_e32 v90, v90\n\t"
"v_exp_f32_e32 v91, v91\n\t"
"v_mfma_f32_16x16x32_bf16 v[76:79], v[216:219], v[72:75], 0\n\t"
"v_exp_f32_e32 v92, v92\n\t"
"v_exp_f32_e32 v93, v93\n\t"
"v_exp_f32_e32 v94, v94\n\t"
"v_exp_f32_e32 v95, v95\n\t"
"v_cvt_pk_bf16_f32 v80, v80, v81\n\t"
"v_cvt_pk_bf16_f32 v81, v82, v83\n\t"
"v_cvt_pk_bf16_f32 v82, v84, v85\n\t"
"v_cvt_pk_bf16_f32 v83, v86, v87\n\t"
"v_cvt_pk_bf16_f32 v88, v88, v89\n\t"
"v_cvt_pk_bf16_f32 v89, v90, v91\n\t"
"v_cvt_pk_bf16_f32 v90, v92, v93\n\t"
"v_cvt_pk_bf16_f32 v91, v94, v95\n\t"
"v_mfma_f32_16x16x32_bf16 v[84:87], v[216:219], v[80:83], 0\n\t"
"s_nop 0\n\t"
"v_mfma_f32_16x16x32_bf16 v[92:95], v[216:219], v[88:91], 0\n\t"
"s_nop 3\n\t"
"v_max3_f32 v233, v52, v60, v68\n\t"
"s_nop 0\n\t"
"v_max3_f32 v233, v233, v76, v84\n\t"
"s_nop 0\n\t"
"v_max_f32_e64 v233, v233, v92\n\t"
"v_cmp_nge_f32_e32 vcc, s60, v233\n\t"
"s_cbranch_vccnz CS5_%=\n\t"
"CB5_%=:\n\t"
"v_mfma_f32_16x16x32_bf16 a[0:3], a[192:195], v[48:51], a[0:3]\n\t"
"s_waitcnt vmcnt(12)\n\t"
"v_add_f32_e32 v227, v227, v52\n\t"
"v_mfma_f32_16x16x32_bf16 a[4:7], a[196:199], v[48:51], a[4:7]\n\t"
"ds_read_b64_tr_b16 v[96:97], v248 offset:0\n\t"
"v_mfma_f32_16x16x32_bf16 a[8:11], a[200:203], v[48:51], a[8:11]\n\t"
"ds_read_b64_tr_b16 v[98:99], v248 offset:2048\n\t"
"v_add_f32_e64 v228, v228, v60\n\t"
"v_mfma_f32_16x16x32_bf16 a[12:15], a[204:207], v[48:51], a[12:15]\n\t"
"ds_read_b64_tr_b16 v[100:101], v249 offset:0\n\t"
"v_mfma_f32_16x16x32_bf16 a[16:19], a[208:211], v[48:51], a[16:19]\n\t"
"ds_read_b64_tr_b16 v[102:103], v249 offset:2048\n\t"
"v_add_f32_e64 v229, v229, v68\n\t"
"v_mfma_f32_16x16x32_bf16 a[20:23], a[212:215], v[48:51], a[20:23]\n\t"
"v_mfma_f32_16x16x32_bf16 a[24:27], a[216:219], v[48:51], a[24:27]\n\t"
"ds_read_b64_tr_b16 v[104:105], v248 offset:512\n\t"
"v_add_f32_e64 v230, v230, v76\n\t"
"v_mfma_f32_16x16x32_bf16 a[28:31], a[220:223], v[48:51], a[28:31]\n\t"
"ds_read_b64_tr_b16 v[106:107], v248 offset:2560\n\t"
"v_mfma_f32_16x16x32_bf16 a[32:35], a[192:195], v[56:59], a[32:35]\n\t"
"ds_read_b64_tr_b16 v[108:109], v249 offset:512\n\t"
"v_add_f32_e64 v231, v231, v84\n\t"
"v_mfma_f32_16x16x32_bf16 a[36:39], a[196:199], v[56:59], a[36:39]\n\t"
"ds_read_b64_tr_b16 v[110:111], v249 offset:2560\n\t"
"v_mfma_f32_16x16x32_bf16 a[40:43], a[200:203], v[56:59], a[40:43]\n\t"
"v_add_f32_e64 v232, v232, v92\n\t"
"v_mfma_f32_16x16x32_bf16 a[44:47], a[204:207], v[56:59], a[44:47]\n\t"
"ds_read_b64_tr_b16 v[112:113], v246 offset:16384\n\t"
"v_mfma_f32_16x16x32_bf16 a[48:51], a[208:211], v[56:59], a[48:51]\n\t"
"ds_read_b64_tr_b16 v[114:115], v246 offset:18432\n\t"
"v_rcp_f32_e64 v192, v227\n\t"
"v_mfma_f32_16x16x32_bf16 a[52:55], a[212:215], v[56:59], a[52:55]\n\t"
"ds_read_b64_tr_b16 v[116:117], v247 offset:16384\n\t"
"v_mfma_f32_16x16x32_bf16 a[56:59], a[216:219], v[56:59], a[56:59]\n\t"
"ds_read_b64_tr_b16 v[118:119], v247 offset:18432\n\t"
"v_rcp_f32_e64 v194, v228\n\t"
"v_mfma_f32_16x16x32_bf16 a[60:63], a[220:223], v[56:59], a[60:63]\n\t"
"v_mfma_f32_16x16x32_bf16 a[64:67], a[192:195], v[64:67], a[64:67]\n\t"
"ds_read_b64_tr_b16 v[120:121], v246 offset:16896\n\t"
"v_rcp_f32_e64 v196, v229\n\t"
"v_mfma_f32_16x16x32_bf16 a[68:71], a[196:199], v[64:67], a[68:71]\n\t"
"ds_read_b64_tr_b16 v[122:123], v246 offset:18944\n\t"
"v_mfma_f32_16x16x32_bf16 a[72:75], a[200:203], v[64:67], a[72:75]\n\t"
"ds_read_b64_tr_b16 v[124:125], v247 offset:16896\n\t"
"v_rcp_f32_e64 v198, v230\n\t"
"v_mfma_f32_16x16x32_bf16 a[76:79], a[204:207], v[64:67], a[76:79]\n\t"
"ds_read_b64_tr_b16 v[126:127], v247 offset:18944\n\t"
"v_mfma_f32_16x16x32_bf16 a[80:83], a[208:211], v[64:67], a[80:83]\n\t"
"v_rcp_f32_e64 v200, v231\n\t"
"v_mfma_f32_16x16x32_bf16 a[84:87], a[212:215], v[64:67], a[84:87]\n\t"
"ds_read_b64_tr_b16 v[128:129], v248 offset:16384\n\t"
"v_mfma_f32_16x16x32_bf16 a[88:91], a[216:219], v[64:67], a[88:91]\n\t"
"ds_read_b64_tr_b16 v[130:131], v248 offset:18432\n\t"
"v_rcp_f32_e64 v202, v232\n\t"
"v_mfma_f32_16x16x32_bf16 a[92:95], a[220:223], v[64:67], a[92:95]\n\t"
"ds_read_b64_tr_b16 v[132:133], v249 offset:16384\n\t"
"v_mfma_f32_16x16x32_bf16 a[96:99], a[192:195], v[72:75], a[96:99]\n\t"
"ds_read_b64_tr_b16 v[134:135], v249 offset:18432\n\t"
"v_lshlrev_b32_e64 v240, 4, v252\n\t"
"v_mfma_f32_16x16x32_bf16 a[100:103], a[196:199], v[72:75], a[100:103]\n\t"
"v_mfma_f32_16x16x32_bf16 a[104:107], a[200:203], v[72:75], a[104:107]\n\t"
"ds_read_b64_tr_b16 v[136:137], v248 offset:16896\n\t"
"v_add_u32_e32 v235, 131072, v246\n\t"
"v_mfma_f32_16x16x32_bf16 a[108:111], a[204:207], v[72:75], a[108:111]\n\t"
"ds_read_b64_tr_b16 v[138:139], v248 offset:18944\n\t"
"v_mfma_f32_16x16x32_bf16 a[112:115], a[208:211], v[72:75], a[112:115]\n\t"
"ds_read_b64_tr_b16 v[140:141], v249 offset:16896\n\t"
"v_add_u32_e32 v236, 131072, v247\n\t"
"v_mfma_f32_16x16x32_bf16 a[116:119], a[212:215], v[72:75], a[116:119]\n\t"
"ds_read_b64_tr_b16 v[142:143], v249 offset:18944\n\t"
"v_mfma_f32_16x16x32_bf16 a[120:123], a[216:219], v[72:75], a[120:123]\n\t"
"v_mfma_f32_16x16x32_bf16 a[124:127], a[220:223], v[72:75], a[124:127]\n\t"
"ds_read_b64_tr_b16 v[144:145], v246 offset:32768\n\t"
"v_mfma_f32_16x16x32_bf16 a[128:131], a[192:195], v[80:83], a[128:131]\n\t"
"ds_read_b64_tr_b16 v[146:147], v246 offset:34816\n\t"
"v_mfma_f32_16x16x32_bf16 a[132:135], a[196:199], v[80:83], a[132:135]\n\t"
"ds_read_b64_tr_b16 v[148:149], v247 offset:32768\n\t"
"v_mfma_f32_16x16x32_bf16 a[136:139], a[200:203], v[80:83], a[136:139]\n\t"
"ds_read_b64_tr_b16 v[150:151], v247 offset:34816\n\t"
"v_mfma_f32_16x16x32_bf16 a[140:143], a[204:207], v[80:83], a[140:143]\n\t"
"v_mfma_f32_16x16x32_bf16 a[144:147], a[208:211], v[80:83], a[144:147]\n\t"
"ds_read_b64_tr_b16 v[152:153], v246 offset:33280\n\t"
"v_mfma_f32_16x16x32_bf16 a[148:151], a[212:215], v[80:83], a[148:151]\n\t"
"ds_read_b64_tr_b16 v[154:155], v246 offset:35328\n\t"
"v_mfma_f32_16x16x32_bf16 a[152:155], a[216:219], v[80:83], a[152:155]\n\t"
"ds_read_b64_tr_b16 v[156:157], v247 offset:33280\n\t"
"v_mfma_f32_16x16x32_bf16 a[156:159], a[220:223], v[80:83], a[156:159]\n\t"
"ds_read_b64_tr_b16 v[158:159], v247 offset:35328\n\t"
"v_mfma_f32_16x16x32_bf16 a[160:163], a[192:195], v[88:91], a[160:163]\n\t"
"v_mfma_f32_16x16x32_bf16 a[164:167], a[196:199], v[88:91], a[164:167]\n\t"
"v_mfma_f32_16x16x32_bf16 a[168:171], a[200:203], v[88:91], a[168:171]\n\t"
"v_mfma_f32_16x16x32_bf16 a[172:175], a[204:207], v[88:91], a[172:175]\n\t"
"v_mfma_f32_16x16x32_bf16 a[176:179], a[208:211], v[88:91], a[176:179]\n\t"
"v_mfma_f32_16x16x32_bf16 a[180:183], a[212:215], v[88:91], a[180:183]\n\t"
"v_mfma_f32_16x16x32_bf16 a[184:187], a[216:219], v[88:91], a[184:187]\n\t"
"v_mfma_f32_16x16x32_bf16 a[188:191], a[220:223], v[88:91], a[188:191]\n\t"
"s_waitcnt lgkmcnt(0)\n\t"
"v_accvgpr_read_b32 v0, a0\n\t"
"v_accvgpr_read_b32 v1, a1\n\t"
"v_accvgpr_read_b32 v2, a2\n\t"
"v_accvgpr_read_b32 v3, a3\n\t"
"v_accvgpr_read_b32 v4, a4\n\t"
"v_accvgpr_read_b32 v5, a5\n\t"
"v_accvgpr_read_b32 v6, a6\n\t"
"v_accvgpr_read_b32 v7, a7\n\t"
"v_pk_mul_f32 v[0:1], v[0:1], v[192:193] op_sel_hi:[1,0]\n\t"
"v_pk_mul_f32 v[2:3], v[2:3], v[192:193] op_sel_hi:[1,0]\n\t"
"v_pk_mul_f32 v[4:5], v[4:5], v[192:193] op_sel_hi:[1,0]\n\t"
"v_pk_mul_f32 v[6:7], v[6:7], v[192:193] op_sel_hi:[1,0]\n\t"
"v_cvt_pk_bf16_f32 v8, v0, v1\n\t"
"v_cvt_pk_bf16_f32 v9, v2, v3\n\t"
"v_cvt_pk_bf16_f32 v10, v4, v5\n\t"
"v_cvt_pk_bf16_f32 v11, v6, v7\n\t"
"s_movk_i32 s71, 0x0\n\t"
"s_add_u32 m0, s72, 65536\n\t"
"s_add_u32 s73, s83, 327680\n\t"
"s_and_b32 s73, s73, 0xfffff\n\t"
"buffer_store_dwordx4 v[8:11], v240, s[48:51], s71 offen offset:0 sc1\n\t"
"buffer_load_dwordx4 v242, s[44:47], s73 offen offset:0 lds\n\t"
"v_accvgpr_read_b32 v12, a8\n\t"
"v_accvgpr_read_b32 v13, a9\n\t"
"v_accvgpr_read_b32 v14, a10\n\t"
"v_accvgpr_read_b32 v15, a11\n\t"
"v_accvgpr_read_b32 v16, a12\n\t"
"v_accvgpr_read_b32 v17, a13\n\t"
"v_accvgpr_read_b32 v18, a14\n\t"
"v_accvgpr_read_b32 v19, a15\n\t"
"v_pk_mul_f32 v[12:13], v[12:13], v[192:193] op_sel_hi:[1,0]\n\t"
"v_pk_mul_f32 v[14:15], v[14:15], v[192:193] op_sel_hi:[1,0]\n\t"
"v_pk_mul_f32 v[16:17], v[16:17], v[192:193] op_sel_hi:[1,0]\n\t"
"v_pk_mul_f32 v[18:19], v[18:19], v[192:193] op_sel_hi:[1,0]\n\t"
"v_cvt_pk_bf16_f32 v20, v12, v13\n\t"
"v_cvt_pk_bf16_f32 v21, v14, v15\n\t"
"v_cvt_pk_bf16_f32 v22, v16, v17\n\t"
"v_cvt_pk_bf16_f32 v23, v18, v19\n\t"
"s_movk_i32 s71, 0x1800\n\t"
"buffer_store_dwordx4 v[20:23], v240, s[48:51], s71 offen offset:0 sc1\n\t"
"buffer_load_dwordx4 v243, s[44:47], s73 offen offset:1024 lds\n\t"
"v_accvgpr_read_b32 v24, a16\n\t"
"v_accvgpr_read_b32 v25, a17\n\t"
"v_accvgpr_read_b32 v26, a18\n\t"
"v_accvgpr_read_b32 v27, a19\n\t"
"v_accvgpr_read_b32 v28, a20\n\t"
"v_accvgpr_read_b32 v29, a21\n\t"
"v_accvgpr_read_b32 v30, a22\n\t"
"v_accvgpr_read_b32 v31, a23\n\t"
"v_pk_mul_f32 v[24:25], v[24:25], v[192:193] op_sel_hi:[1,0]\n\t"
"v_pk_mul_f32 v[26:27], v[26:27], v[192:193] op_sel_hi:[1,0]\n\t"
"v_pk_mul_f32 v[28:29], v[28:29], v[192:193] op_sel_hi:[1,0]\n\t"
"v_pk_mul_f32 v[30:31], v[30:31], v[192:193] op_sel_hi:[1,0]\n\t"
"v_cvt_pk_bf16_f32 v32, v24, v25\n\t"
"v_cvt_pk_bf16_f32 v33, v26, v27\n\t"
"v_cvt_pk_bf16_f32 v34, v28, v29\n\t"
"v_cvt_pk_bf16_f32 v35, v30, v31\n\t"
"s_movk_i32 s71, 0x3000\n\t"
"buffer_store_dwordx4 v[32:35], v240, s[48:51], s71 offen offset:0 sc1\n\t"
"buffer_load_dwordx4 v244, s[44:47], s73 offen offset:2048 lds\n\t"
"v_accvgpr_read_b32 v36, a24\n\t"
"v_accvgpr_read_b32 v37, a25\n\t"
"v_accvgpr_read_b32 v38, a26\n\t"
"v_accvgpr_read_b32 v39, a27\n\t"
"v_accvgpr_read_b32 v40, a28\n\t"
"v_accvgpr_read_b32 v41, a29\n\t"
"v_accvgpr_read_b32 v42, a30\n\t"
"v_accvgpr_read_b32 v43, a31\n\t"
"v_pk_mul_f32 v[36:37], v[36:37], v[192:193] op_sel_hi:[1,0]\n\t"
"v_pk_mul_f32 v[38:39], v[38:39], v[192:193] op_sel_hi:[1,0]\n\t"
"v_pk_mul_f32 v[40:41], v[40:41], v[192:193] op_sel_hi:[1,0]\n\t"
"v_pk_mul_f32 v[42:43], v[42:43], v[192:193] op_sel_hi:[1,0]\n\t"
"v_cvt_pk_bf16_f32 v44, v36, v37\n\t"
"v_cvt_pk_bf16_f32 v45, v38, v39\n\t"
"v_cvt_pk_bf16_f32 v46, v40, v41\n\t"
"v_cvt_pk_bf16_f32 v47, v42, v43\n\t"
"s_movk_i32 s71, 0x4800\n\t"
"buffer_store_dwordx4 v[44:47], v240, s[48:51], s71 offen offset:0 sc1\n\t"
"buffer_load_dwordx4 v245, s[44:47], s73 offen offset:3072 lds\n\t"
"v_accvgpr_read_b32 v0, a32\n\t"
"v_accvgpr_read_b32 v1, a33\n\t"
"v_accvgpr_read_b32 v2, a34\n\t"
"v_accvgpr_read_b32 v3, a35\n\t"
"v_accvgpr_read_b32 v4, a36\n\t"
"v_accvgpr_read_b32 v5, a37\n\t"
"v_accvgpr_read_b32 v6, a38\n\t"
"v_accvgpr_read_b32 v7, a39\n\t"
"v_pk_mul_f32 v[0:1], v[0:1], v[194:195] op_sel_hi:[1,0]\n\t"
"v_pk_mul_f32 v[2:3], v[2:3], v[194:195] op_sel_hi:[1,0]\n\t"
"v_pk_mul_f32 v[4:5], v[4:5], v[194:195] op_sel_hi:[1,0]\n\t"
"v_pk_mul_f32 v[6:7], v[6:7], v[194:195] op_sel_hi:[1,0]\n\t"
"v_cvt_pk_bf16_f32 v8, v0, v1\n\t"
"v_cvt_pk_bf16_f32 v9, v2, v3\n\t"
"v_cvt_pk_bf16_f32 v10, v4, v5\n\t"
"v_cvt_pk_bf16_f32 v11, v6, v7\n\t"
"s_movk_i32 s71, 0x400\n\t"
"s_add_u32 m0, s72, 16384\n\t"
"s_add_u32 s73, s83, 360448\n\t"
"s_and_b32 s73, s73, 0xfffff\n\t"
"buffer_store_dwordx4 v[8:11], v240, s[48:51], s71 offen offset:0 sc1\n\t"
"buffer_load_dwordx4 v242, s[44:47], s73 offen offset:0 lds\n\t"
"ds_read_b64_tr_b16 a[0:1], v235 offset:16384\n\t"
"ds_read_b64_tr_b16 a[2:3], v235 offset:18432\n\t"
"ds_read_b64_tr_b16 a[4:5], v236 offset:16384\n\t"
"ds_read_b64_tr_b16 a[6:7], v236 offset:18432\n\t"
"ds_read_b64_tr_b16 a[8:9], v235 offset:16896\n\t"
"ds_read_b64_tr_b16 a[10:11], v235 offset:18944\n\t"
"ds_read_b64_tr_b16 a[12:13], v236 offset:16896\n\t"
"ds_read_b64_tr_b16 a[14:15], v236 offset:18944\n\t"
"v_accvgpr_read_b32 v12, a40\n\t"
"v_accvgpr_read_b32 v13, a41\n\t"
"v_accvgpr_read_b32 v14, a42\n\t"
"v_accvgpr_read_b32 v15, a43\n\t"
"v_accvgpr_read_b32 v16, a44\n\t"
"v_accvgpr_read_b32 v17, a45\n\t"
"v_accvgpr_read_b32 v18, a46\n\t"
"v_accvgpr_read_b32 v19, a47\n\t"
"v_pk_mul_f32 v[12:13], v[12:13], v[194:195] op_sel_hi:[1,0]\n\t"
"v_pk_mul_f32 v[14:15], v[14:15], v[194:195] op_sel_hi:[1,0]\n\t"
"v_pk_mul_f32 v[16:17], v[16:17], v[194:195] op_sel_hi:[1,0]\n\t"
"v_pk_mul_f32 v[18:19], v[18:19], v[194:195] op_sel_hi:[1,0]\n\t"
"v_cvt_pk_bf16_f32 v20, v12, v13\n\t"
"v_cvt_pk_bf16_f32 v21, v14, v15\n\t"
"v_cvt_pk_bf16_f32 v22, v16, v17\n\t"
"v_cvt_pk_bf16_f32 v23, v18, v19\n\t"
"s_movk_i32 s71, 0x1c00\n\t"
"buffer_store_dwordx4 v[20:23], v240, s[48:51], s71 offen offset:0 sc1\n\t"
"buffer_load_dwordx4 v243, s[44:47], s73 offen offset:1024 lds\n\t"
"v_accvgpr_read_b32 v24, a48\n\t"
"v_accvgpr_read_b32 v25, a49\n\t"
"v_accvgpr_read_b32 v26, a50\n\t"
"v_accvgpr_read_b32 v27, a51\n\t"
"v_accvgpr_read_b32 v28, a52\n\t"
"v_accvgpr_read_b32 v29, a53\n\t"
"v_accvgpr_read_b32 v30, a54\n\t"
"v_accvgpr_read_b32 v31, a55\n\t"
"v_pk_mul_f32 v[24:25], v[24:25], v[194:195] op_sel_hi:[1,0]\n\t"
"v_pk_mul_f32 v[26:27], v[26:27], v[194:195] op_sel_hi:[1,0]\n\t"
"v_pk_mul_f32 v[28:29], v[28:29], v[194:195] op_sel_hi:[1,0]\n\t"
"v_pk_mul_f32 v[30:31], v[30:31], v[194:195] op_sel_hi:[1,0]\n\t"
"v_cvt_pk_bf16_f32 v32, v24, v25\n\t"
"v_cvt_pk_bf16_f32 v33, v26, v27\n\t"
"v_cvt_pk_bf16_f32 v34, v28, v29\n\t"
"v_cvt_pk_bf16_f32 v35, v30, v31\n\t"
"s_movk_i32 s71, 0x3400\n\t"
"buffer_store_dwordx4 v[32:35], v240, s[48:51], s71 offen offset:0 sc1\n\t"
"buffer_load_dwordx4 v244, s[44:47], s73 offen offset:2048 lds\n\t"
"v_accvgpr_read_b32 v36, a56\n\t"
"v_accvgpr_read_b32 v37, a57\n\t"
"v_accvgpr_read_b32 v38, a58\n\t"
"v_accvgpr_read_b32 v39, a59\n\t"
"v_accvgpr_read_b32 v40, a60\n\t"
"v_accvgpr_read_b32 v41, a61\n\t"
"v_accvgpr_read_b32 v42, a62\n\t"
"v_accvgpr_read_b32 v43, a63\n\t"
"v_pk_mul_f32 v[36:37], v[36:37], v[194:195] op_sel_hi:[1,0]\n\t"
"v_pk_mul_f32 v[38:39], v[38:39], v[194:195] op_sel_hi:[1,0]\n\t"
"v_pk_mul_f32 v[40:41], v[40:41], v[194:195] op_sel_hi:[1,0]\n\t"
"v_pk_mul_f32 v[42:43], v[42:43], v[194:195] op_sel_hi:[1,0]\n\t"
"v_cvt_pk_bf16_f32 v44, v36, v37\n\t"
"v_cvt_pk_bf16_f32 v45, v38, v39\n\t"
"v_cvt_pk_bf16_f32 v46, v40, v41\n\t"
"v_cvt_pk_bf16_f32 v47, v42, v43\n\t"
"s_movk_i32 s71, 0x4c00\n\t"
"buffer_store_dwordx4 v[44:47], v240, s[48:51], s71 offen offset:0 sc1\n\t"
"buffer_load_dwordx4 v245, s[44:47], s73 offen offset:3072 lds\n\t"
"ds_read_b64_tr_b16 a[16:17], v248 offset:32768\n\t"
"ds_read_b64_tr_b16 a[18:19], v248 offset:34816\n\t"
"ds_read_b64_tr_b16 a[20:21], v249 offset:32768\n\t"
"ds_read_b64_tr_b16 a[22:23], v249 offset:34816\n\t"
"ds_read_b64_tr_b16 a[24:25], v248 offset:33280\n\t"
"ds_read_b64_tr_b16 a[26:27], v248 offset:35328\n\t"
"ds_read_b64_tr_b16 a[28:29], v249 offset:33280\n\t"
"ds_read_b64_tr_b16 a[30:31], v249 offset:35328\n\t"
"v_accvgpr_read_b32 v0, a64\n\t"
"v_accvgpr_read_b32 v1, a65\n\t"
"v_accvgpr_read_b32 v2, a66\n\t"
"v_accvgpr_read_b32 v3, a67\n\t"
"v_accvgpr_read_b32 v4, a68\n\t"
"v_accvgpr_read_b32 v5, a69\n\t"
"v_accvgpr_read_b32 v6, a70\n\t"
"v_accvgpr_read_b32 v7, a71\n\t"
"v_pk_mul_f32 v[0:1], v[0:1], v[196:197] op_sel_hi:[1,0]\n\t"
"v_pk_mul_f32 v[2:3], v[2:3], v[196:197] op_sel_hi:[1,0]\n\t"
"v_pk_mul_f32 v[4:5], v[4:5], v[196:197] op_sel_hi:[1,0]\n\t"
"v_pk_mul_f32 v[6:7], v[6:7], v[196:197] op_sel_hi:[1,0]\n\t"
"v_cvt_pk_bf16_f32 v8, v0, v1\n\t"
"v_cvt_pk_bf16_f32 v9, v2, v3\n\t"
"v_cvt_pk_bf16_f32 v10, v4, v5\n\t"
"v_cvt_pk_bf16_f32 v11, v6, v7\n\t"
"s_movk_i32 s71, 0x800\n\t"
"s_add_u32 m0, s72, 81920\n\t"
"s_add_u32 s73, s83, 393216\n\t"
"s_and_b32 s73, s73, 0xfffff\n\t"
"buffer_store_dwordx4 v[8:11], v240, s[48:51], s71 offen offset:0 sc1\n\t"
"buffer_load_dwordx4 v242, s[44:47], s73 offen offset:0 lds\n\t"
"v_accvgpr_read_b32 v12, a72\n\t"
"v_accvgpr_read_b32 v13, a73\n\t"
"v_accvgpr_read_b32 v14, a74\n\t"
"v_accvgpr_read_b32 v15, a75\n\t"
"v_accvgpr_read_b32 v16, a76\n\t"
"v_accvgpr_read_b32 v17, a77\n\t"
"v_accvgpr_read_b32 v18, a78\n\t"
"v_accvgpr_read_b32 v19, a79\n\t"
"v_pk_mul_f32 v[12:13], v[12:13], v[196:197] op_sel_hi:[1,0]\n\t"
"v_pk_mul_f32 v[14:15], v[14:15], v[196:197] op_sel_hi:[1,0]\n\t"
"v_pk_mul_f32 v[16:17], v[16:17], v[196:197] op_sel_hi:[1,0]\n\t"
"v_pk_mul_f32 v[18:19], v[18:19], v[196:197] op_sel_hi:[1,0]\n\t"
"v_cvt_pk_bf16_f32 v20, v12, v13\n\t"
"v_cvt_pk_bf16_f32 v21, v14, v15\n\t"
"v_cvt_pk_bf16_f32 v22, v16, v17\n\t"
"v_cvt_pk_bf16_f32 v23, v18, v19\n\t"
"s_movk_i32 s71, 0x2000\n\t"
"buffer_store_dwordx4 v[20:23], v240, s[48:51], s71 offen offset:0 sc1\n\t"
"buffer_load_dwordx4 v243, s[44:47], s73 offen offset:1024 lds\n\t"
"v_accvgpr_read_b32 v24, a80\n\t"
"v_accvgpr_read_b32 v25, a81\n\t"
"v_accvgpr_read_b32 v26, a82\n\t"
"v_accvgpr_read_b32 v27, a83\n\t"
"v_accvgpr_read_b32 v28, a84\n\t"
"v_accvgpr_read_b32 v29, a85\n\t"
"v_accvgpr_read_b32 v30, a86\n\t"
"v_accvgpr_read_b32 v31, a87\n\t"
"v_pk_mul_f32 v[24:25], v[24:25], v[196:197] op_sel_hi:[1,0]\n\t"
"v_pk_mul_f32 v[26:27], v[26:27], v[196:197] op_sel_hi:[1,0]\n\t"
"v_pk_mul_f32 v[28:29], v[28:29], v[196:197] op_sel_hi:[1,0]\n\t"
"v_pk_mul_f32 v[30:31], v[30:31], v[196:197] op_sel_hi:[1,0]\n\t"
"v_cvt_pk_bf16_f32 v32, v24, v25\n\t"
"v_cvt_pk_bf16_f32 v33, v26, v27\n\t"
"v_cvt_pk_bf16_f32 v34, v28, v29\n\t"
"v_cvt_pk_bf16_f32 v35, v30, v31\n\t"
"s_movk_i32 s71, 0x3800\n\t"
"buffer_store_dwordx4 v[32:35], v240, s[48:51], s71 offen offset:0 sc1\n\t"
"buffer_load_dwordx4 v244, s[44:47], s73 offen offset:2048 lds\n\t"
"s_waitcnt vmcnt(22)\n\t"
"ds_read_b64_tr_b16 a[32:33], v246 offset:49152\n\t"
"ds_read_b64_tr_b16 a[34:35], v246 offset:51200\n\t"
"ds_read_b64_tr_b16 a[36:37], v247 offset:49152\n\t"
"ds_read_b64_tr_b16 a[38:39], v247 offset:51200\n\t"
"ds_read_b64_tr_b16 a[40:41], v246 offset:49664\n\t"
"ds_read_b64_tr_b16 a[42:43], v246 offset:51712\n\t"
"ds_read_b64_tr_b16 a[44:45], v247 offset:49664\n\t"
"ds_read_b64_tr_b16 a[46:47], v247 offset:51712\n\t"
"v_accvgpr_read_b32 v36, a88\n\t"
"v_accvgpr_read_b32 v37, a89\n\t"
"v_accvgpr_read_b32 v38, a90\n\t"
"v_accvgpr_read_b32 v39, a91\n\t"
"v_accvgpr_read_b32 v40, a92\n\t"
"v_accvgpr_read_b32 v41, a93\n\t"
"v_accvgpr_read_b32 v42, a94\n\t"
"v_accvgpr_read_b32 v43, a95\n\t"
"v_pk_mul_f32 v[36:37], v[36:37], v[196:197] op_sel_hi:[1,0]\n\t"
"v_pk_mul_f32 v[38:39], v[38:39], v[196:197] op_sel_hi:[1,0]\n\t"
"v_pk_mul_f32 v[40:41], v[40:41], v[196:197] op_sel_hi:[1,0]\n\t"
"v_pk_mul_f32 v[42:43], v[42:43], v[196:197] op_sel_hi:[1,0]\n\t"
"v_cvt_pk_bf16_f32 v44, v36, v37\n\t"
"v_cvt_pk_bf16_f32 v45, v38, v39\n\t"
"v_cvt_pk_bf16_f32 v46, v40, v41\n\t"
"v_cvt_pk_bf16_f32 v47, v42, v43\n\t"
"s_movk_i32 s71, 0x5000\n\t"
"buffer_store_dwordx4 v[44:47], v240, s[48:51], s71 offen offset:0 sc1\n\t"
"buffer_load_dwordx4 v245, s[44:47], s73 offen offset:3072 lds\n\t"
"v_accvgpr_read_b32 v0, a96\n\t"
"v_accvgpr_read_b32 v1, a97\n\t"
"v_accvgpr_read_b32 v2, a98\n\t"
"v_accvgpr_read_b32 v3, a99\n\t"
"v_accvgpr_read_b32 v4, a100\n\t"
"v_accvgpr_read_b32 v5, a101\n\t"
"v_accvgpr_read_b32 v6, a102\n\t"
"v_accvgpr_read_b32 v7, a103\n\t"
"v_pk_mul_f32 v[0:1], v[0:1], v[198:199] op_sel_hi:[1,0]\n\t"
"v_pk_mul_f32 v[2:3], v[2:3], v[198:199] op_sel_hi:[1,0]\n\t"
"v_pk_mul_f32 v[4:5], v[4:5], v[198:199] op_sel_hi:[1,0]\n\t"
"v_pk_mul_f32 v[6:7], v[6:7], v[198:199] op_sel_hi:[1,0]\n\t"
"v_cvt_pk_bf16_f32 v8, v0, v1\n\t"
"v_cvt_pk_bf16_f32 v9, v2, v3\n\t"
"v_cvt_pk_bf16_f32 v10, v4, v5\n\t"
"v_cvt_pk_bf16_f32 v11, v6, v7\n\t"
"s_movk_i32 s71, 0xc00\n\t"
"s_add_u32 m0, s72, 32768\n\t"
"s_add_u32 s73, s83, 425984\n\t"
"s_and_b32 s73, s73, 0xfffff\n\t"
"buffer_store_dwordx4 v[8:11], v240, s[48:51], s71 offen offset:0 sc1\n\t"
"buffer_load_dwordx4 v242, s[44:47], s73 offen offset:0 lds\n\t"
"ds_read_b64_tr_b16 a[48:49], v248 offset:49152\n\t"
"ds_read_b64_tr_b16 a[50:51], v248 offset:51200\n\t"
"ds_read_b64_tr_b16 a[52:53], v249 offset:49152\n\t"
"ds_read_b64_tr_b16 a[54:55], v249 offset:51200\n\t"
"ds_read_b64_tr_b16 a[56:57], v248 offset:49664\n\t"
"ds_read_b64_tr_b16 a[58:59], v248 offset:51712\n\t"
"ds_read_b64_tr_b16 a[60:61], v249 offset:49664\n\t"
"ds_read_b64_tr_b16 a[62:63], v249 offset:51712\n\t"
"v_accvgpr_read_b32 v12, a104\n\t"
"v_accvgpr_read_b32 v13, a105\n\t"
"v_accvgpr_read_b32 v14, a106\n\t"
"v_accvgpr_read_b32 v15, a107\n\t"
"v_accvgpr_read_b32 v16, a108\n\t"
"v_accvgpr_read_b32 v17, a109\n\t"
"v_accvgpr_read_b32 v18, a110\n\t"
"v_accvgpr_read_b32 v19, a111\n\t"
"v_pk_mul_f32 v[12:13], v[12:13], v[198:199] op_sel_hi:[1,0]\n\t"
"v_pk_mul_f32 v[14:15], v[14:15], v[198:199] op_sel_hi:[1,0]\n\t"
"v_pk_mul_f32 v[16:17], v[16:17], v[198:199] op_sel_hi:[1,0]\n\t"
"v_pk_mul_f32 v[18:19], v[18:19], v[198:199] op_sel_hi:[1,0]\n\t"
"v_cvt_pk_bf16_f32 v20, v12, v13\n\t"
"v_cvt_pk_bf16_f32 v21, v14, v15\n\t"
"v_cvt_pk_bf16_f32 v22, v16, v17\n\t"
"v_cvt_pk_bf16_f32 v23, v18, v19\n\t"
"s_movk_i32 s71, 0x2400\n\t"
"buffer_store_dwordx4 v[20:23], v240, s[48:51], s71 offen offset:0 sc1\n\t"
"buffer_load_dwordx4 v243, s[44:47], s73 offen offset:1024 lds\n\t"
"v_accvgpr_read_b32 v24, a112\n\t"
"v_accvgpr_read_b32 v25, a113\n\t"
"v_accvgpr_read_b32 v26, a114\n\t"
"v_accvgpr_read_b32 v27, a115\n\t"
"v_accvgpr_read_b32 v28, a116\n\t"
"v_accvgpr_read_b32 v29, a117\n\t"
"v_accvgpr_read_b32 v30, a118\n\t"
"v_accvgpr_read_b32 v31, a119\n\t"
"v_pk_mul_f32 v[24:25], v[24:25], v[198:199] op_sel_hi:[1,0]\n\t"
"v_pk_mul_f32 v[26:27], v[26:27], v[198:199] op_sel_hi:[1,0]\n\t"
"v_pk_mul_f32 v[28:29], v[28:29], v[198:199] op_sel_hi:[1,0]\n\t"
"v_pk_mul_f32 v[30:31], v[30:31], v[198:199] op_sel_hi:[1,0]\n\t"
"v_cvt_pk_bf16_f32 v32, v24, v25\n\t"
"v_cvt_pk_bf16_f32 v33, v26, v27\n\t"
"v_cvt_pk_bf16_f32 v34, v28, v29\n\t"
"v_cvt_pk_bf16_f32 v35, v30, v31\n\t"
"s_movk_i32 s71, 0x3c00\n\t"
"buffer_store_dwordx4 v[32:35], v240, s[48:51], s71 offen offset:0 sc1\n\t"
"buffer_load_dwordx4 v244, s[44:47], s73 offen offset:2048 lds\n\t"
"ds_read_b64_tr_b16 a[64:65], v246 offset:0\n\t"
"ds_read_b64_tr_b16 a[66:67], v246 offset:2048\n\t"
"ds_read_b64_tr_b16 a[68:69], v247 offset:0\n\t"
"ds_read_b64_tr_b16 a[70:71], v247 offset:2048\n\t"
"ds_read_b64_tr_b16 a[72:73], v246 offset:512\n\t"
"ds_read_b64_tr_b16 a[74:75], v246 offset:2560\n\t"
"ds_read_b64_tr_b16 a[76:77], v247 offset:512\n\t"
"ds_read_b64_tr_b16 a[78:79], v247 offset:2560\n\t"
"v_accvgpr_read_b32 v36, a120\n\t"
"v_accvgpr_read_b32 v37, a121\n\t"
"v_accvgpr_read_b32 v38, a122\n\t"
"v_accvgpr_read_b32 v39, a123\n\t"
"v_accvgpr_read_b32 v40, a124\n\t"
"v_accvgpr_read_b32 v41, a125\n\t"
"v_accvgpr_read_b32 v42, a126\n\t"
"v_accvgpr_read_b32 v43, a127\n\t"
"v_pk_mul_f32 v[36:37], v[36:37], v[198:199] op_sel_hi:[1,0]\n\t"
"v_pk_mul_f32 v[38:39], v[38:39], v[198:199] op_sel_hi:[1,0]\n\t"
"v_pk_mul_f32 v[40:41], v[40:41], v[198:199] op_sel_hi:[1,0]\n\t"
"v_pk_mul_f32 v[42:43], v[42:43], v[198:199] op_sel_hi:[1,0]\n\t"
"v_cvt_pk_bf16_f32 v44, v36, v37\n\t"
"v_cvt_pk_bf16_f32 v45, v38, v39\n\t"
"v_cvt_pk_bf16_f32 v46, v40, v41\n\t"
"v_cvt_pk_bf16_f32 v47, v42, v43\n\t"
"s_movk_i32 s71, 0x5400\n\t"
"buffer_store_dwordx4 v[44:47], v240, s[48:51], s71 offen offset:0 sc1\n\t"
"buffer_load_dwordx4 v245, s[44:47], s73 offen offset:3072 lds\n\t"
"v_accvgpr_read_b32 v0, a128\n\t"
"v_accvgpr_read_b32 v1, a129\n\t"
"v_accvgpr_read_b32 v2, a130\n\t"
"v_accvgpr_read_b32 v3, a131\n\t"
"v_accvgpr_read_b32 v4, a132\n\t"
"v_accvgpr_read_b32 v5, a133\n\t"
"v_accvgpr_read_b32 v6, a134\n\t"
"v_accvgpr_read_b32 v7, a135\n\t"
"v_pk_mul_f32 v[0:1], v[0:1], v[200:201] op_sel_hi:[1,0]\n\t"
"v_pk_mul_f32 v[2:3], v[2:3], v[200:201] op_sel_hi:[1,0]\n\t"
"v_pk_mul_f32 v[4:5], v[4:5], v[200:201] op_sel_hi:[1,0]\n\t"
"v_pk_mul_f32 v[6:7], v[6:7], v[200:201] op_sel_hi:[1,0]\n\t"
"v_cvt_pk_bf16_f32 v8, v0, v1\n\t"
"v_cvt_pk_bf16_f32 v9, v2, v3\n\t"
"v_cvt_pk_bf16_f32 v10, v4, v5\n\t"
"v_cvt_pk_bf16_f32 v11, v6, v7\n\t"
"s_movk_i32 s71, 0x1000\n\t"
"buffer_store_dwordx4 v[8:11], v240, s[48:51], s71 offen offset:0 sc1\n\t"
"s_waitcnt lgkmcnt(0)\n\t"
"s_add_u32 m0, s72, 147456\n\t"
"s_add_u32 s73, s83, 458752\n\t"
"s_and_b32 s73, s73, 0xfffff\n\t"
"buffer_load_dwordx4 v242, s[44:47], s73 offen offset:0 lds\n\t"
"buffer_load_dwordx4 v243, s[44:47], s73 offen offset:1024 lds\n\t"
"buffer_load_dwordx4 v244, s[44:47], s73 offen offset:2048 lds\n\t"
"buffer_load_dwordx4 v245, s[44:47], s73 offen offset:3072 lds\n\t"
"v_accvgpr_read_b32 v12, a136\n\t"
"v_accvgpr_read_b32 v13, a137\n\t"
"v_accvgpr_read_b32 v14, a138\n\t"
"v_accvgpr_read_b32 v15, a139\n\t"
"v_accvgpr_read_b32 v16, a140\n\t"
"v_accvgpr_read_b32 v17, a141\n\t"
"v_accvgpr_read_b32 v18, a142\n\t"
"v_accvgpr_read_b32 v19, a143\n\t"
"v_pk_mul_f32 v[12:13], v[12:13], v[200:201] op_sel_hi:[1,0]\n\t"
"v_pk_mul_f32 v[14:15], v[14:15], v[200:201] op_sel_hi:[1,0]\n\t"
"v_pk_mul_f32 v[16:17], v[16:17], v[200:201] op_sel_hi:[1,0]\n\t"
"v_pk_mul_f32 v[18:19], v[18:19], v[200:201] op_sel_hi:[1,0]\n\t"
"v_cvt_pk_bf16_f32 v20, v12, v13\n\t"
"v_cvt_pk_bf16_f32 v21, v14, v15\n\t"
"v_cvt_pk_bf16_f32 v22, v16, v17\n\t"
"v_cvt_pk_bf16_f32 v23, v18, v19\n\t"
"s_movk_i32 s71, 0x2800\n\t"
"buffer_store_dwordx4 v[20:23], v240, s[48:51], s71 offen offset:0 sc1\n\t"
"v_accvgpr_read_b32 v24, a144\n\t"
"v_accvgpr_read_b32 v25, a145\n\t"
"v_accvgpr_read_b32 v26, a146\n\t"
"v_accvgpr_read_b32 v27, a147\n\t"
"v_accvgpr_read_b32 v28, a148\n\t"
"v_accvgpr_read_b32 v29, a149\n\t"
"v_accvgpr_read_b32 v30, a150\n\t"
"v_accvgpr_read_b32 v31, a151\n\t"
"v_pk_mul_f32 v[24:25], v[24:25], v[200:201] op_sel_hi:[1,0]\n\t"
"v_pk_mul_f32 v[26:27], v[26:27], v[200:201] op_sel_hi:[1,0]\n\t"
"v_pk_mul_f32 v[28:29], v[28:29], v[200:201] op_sel_hi:[1,0]\n\t"
"v_pk_mul_f32 v[30:31], v[30:31], v[200:201] op_sel_hi:[1,0]\n\t"
"v_cvt_pk_bf16_f32 v32, v24, v25\n\t"
"v_cvt_pk_bf16_f32 v33, v26, v27\n\t"
"v_cvt_pk_bf16_f32 v34, v28, v29\n\t"
"v_cvt_pk_bf16_f32 v35, v30, v31\n\t"
"s_movk_i32 s71, 0x4000\n\t"
"buffer_store_dwordx4 v[32:35], v240, s[48:51], s71 offen offset:0 sc1\n\t"
"s_add_u32 m0, s72, 98304\n\t"
"s_add_u32 s73, s83, 491520\n\t"
"s_and_b32 s73, s73, 0xfffff\n\t"
"buffer_load_dwordx4 v242, s[44:47], s73 offen offset:0 lds\n\t"
"buffer_load_dwordx4 v243, s[44:47], s73 offen offset:1024 lds\n\t"
"buffer_load_dwordx4 v244, s[44:47], s73 offen offset:2048 lds\n\t"
"buffer_load_dwordx4 v245, s[44:47], s73 offen offset:3072 lds\n\t"
"v_accvgpr_read_b32 v36, a152\n\t"
"v_accvgpr_read_b32 v37, a153\n\t"
"v_accvgpr_read_b32 v38, a154\n\t"
"v_accvgpr_read_b32 v39, a155\n\t"
"v_accvgpr_read_b32 v40, a156\n\t"
"v_accvgpr_read_b32 v41, a157\n\t"
"v_accvgpr_read_b32 v42, a158\n\t"
"v_accvgpr_read_b32 v43, a159\n\t"
"v_pk_mul_f32 v[36:37], v[36:37], v[200:201] op_sel_hi:[1,0]\n\t"
"v_pk_mul_f32 v[38:39], v[38:39], v[200:201] op_sel_hi:[1,0]\n\t"
"v_pk_mul_f32 v[40:41], v[40:41], v[200:201] op_sel_hi:[1,0]\n\t"
"v_pk_mul_f32 v[42:43], v[42:43], v[200:201] op_sel_hi:[1,0]\n\t"
"v_cvt_pk_bf16_f32 v44, v36, v37\n\t"
"v_cvt_pk_bf16_f32 v45, v38, v39\n\t"
"v_cvt_pk_bf16_f32 v46, v40, v41\n\t"
"v_cvt_pk_bf16_f32 v47, v42, v43\n\t"
"s_movk_i32 s71, 0x5800\n\t"
"buffer_store_dwordx4 v[44:47], v240, s[48:51], s71 offen offset:0 sc1\n\t"
"v_accvgpr_read_b32 v0, a160\n\t"
"v_accvgpr_read_b32 v1, a161\n\t"
"v_accvgpr_read_b32 v2, a162\n\t"
"v_accvgpr_read_b32 v3, a163\n\t"
"v_accvgpr_read_b32 v4, a164\n\t"
"v_accvgpr_read_b32 v5, a165\n\t"
"v_accvgpr_read_b32 v6, a166\n\t"
"v_accvgpr_read_b32 v7, a167\n\t"
"v_pk_mul_f32 v[0:1], v[0:1], v[202:203] op_sel_hi:[1,0]\n\t"
"v_pk_mul_f32 v[2:3], v[2:3], v[202:203] op_sel_hi:[1,0]\n\t"
"v_pk_mul_f32 v[4:5], v[4:5], v[202:203] op_sel_hi:[1,0]\n\t"
"v_pk_mul_f32 v[6:7], v[6:7], v[202:203] op_sel_hi:[1,0]\n\t"
"v_cvt_pk_bf16_f32 v8, v0, v1\n\t"
"v_cvt_pk_bf16_f32 v9, v2, v3\n\t"
"v_cvt_pk_bf16_f32 v10, v4, v5\n\t"
"v_cvt_pk_bf16_f32 v11, v6, v7\n\t"
"s_movk_i32 s71, 0x1400\n\t"
"buffer_store_dwordx4 v[8:11], v240, s[48:51], s71 offen offset:0 sc1\n\t"
"s_add_u32 m0, s72, 49152\n\t"
"s_add_u32 s73, s83, 524288\n\t"
"s_and_b32 s73, s73, 0xfffff\n\t"
"buffer_load_dwordx4 v242, s[44:47], s73 offen offset:0 lds\n\t"
"buffer_load_dwordx4 v243, s[44:47], s73 offen offset:1024 lds\n\t"
"buffer_load_dwordx4 v244, s[44:47], s73 offen offset:2048 lds\n\t"
"buffer_load_dwordx4 v245, s[44:47], s73 offen offset:3072 lds\n\t"
"v_accvgpr_read_b32 v12, a168\n\t"
"v_accvgpr_read_b32 v13, a169\n\t"
"v_accvgpr_read_b32 v14, a170\n\t"
"v_accvgpr_read_b32 v15, a171\n\t"
"v_accvgpr_read_b32 v16, a172\n\t"
"v_accvgpr_read_b32 v17, a173\n\t"
"v_accvgpr_read_b32 v18, a174\n\t"
"v_accvgpr_read_b32 v19, a175\n\t"
"v_pk_mul_f32 v[12:13], v[12:13], v[202:203] op_sel_hi:[1,0]\n\t"
"v_pk_mul_f32 v[14:15], v[14:15], v[202:203] op_sel_hi:[1,0]\n\t"
"v_pk_mul_f32 v[16:17], v[16:17], v[202:203] op_sel_hi:[1,0]\n\t"
"v_pk_mul_f32 v[18:19], v[18:19], v[202:203] op_sel_hi:[1,0]\n\t"
"v_cvt_pk_bf16_f32 v20, v12, v13\n\t"
"v_cvt_pk_bf16_f32 v21, v14, v15\n\t"
"v_cvt_pk_bf16_f32 v22, v16, v17\n\t"
"v_cvt_pk_bf16_f32 v23, v18, v19\n\t"
"s_movk_i32 s71, 0x2c00\n\t"
"buffer_store_dwordx4 v[20:23], v240, s[48:51], s71 offen offset:0 sc1\n\t"
"s_add_u32 m0, s72, 114688\n\t"
"s_add_u32 s73, s83, 557056\n\t"
"s_and_b32 s73, s73, 0xfffff\n\t"
"buffer_load_dwordx4 v242, s[44:47], s73 offen offset:0 lds\n\t"
"buffer_load_dwordx4 v243, s[44:47], s73 offen offset:1024 lds\n\t"
"buffer_load_dwordx4 v244, s[44:47], s73 offen offset:2048 lds\n\t"
"buffer_load_dwordx4 v245, s[44:47], s73 offen offset:3072 lds\n\t"
"v_accvgpr_read_b32 v24, a176\n\t"
"v_accvgpr_read_b32 v25, a177\n\t"
"v_accvgpr_read_b32 v26, a178\n\t"
"v_accvgpr_read_b32 v27, a179\n\t"
"v_accvgpr_read_b32 v28, a180\n\t"
"v_accvgpr_read_b32 v29, a181\n\t"
"v_accvgpr_read_b32 v30, a182\n\t"
"v_accvgpr_read_b32 v31, a183\n\t"
"v_pk_mul_f32 v[24:25], v[24:25], v[202:203] op_sel_hi:[1,0]\n\t"
"v_pk_mul_f32 v[26:27], v[26:27], v[202:203] op_sel_hi:[1,0]\n\t"
"v_pk_mul_f32 v[28:29], v[28:29], v[202:203] op_sel_hi:[1,0]\n\t"
"v_pk_mul_f32 v[30:31], v[30:31], v[202:203] op_sel_hi:[1,0]\n\t"
"v_cvt_pk_bf16_f32 v32, v24, v25\n\t"
"v_cvt_pk_bf16_f32 v33, v26, v27\n\t"
"v_cvt_pk_bf16_f32 v34, v28, v29\n\t"
"v_cvt_pk_bf16_f32 v35, v30, v31\n\t"
"s_movk_i32 s71, 0x4400\n\t"
"buffer_store_dwordx4 v[32:35], v240, s[48:51], s71 offen offset:0 sc1\n\t"
"s_mov_b32 m0, s72\n\t"
"s_add_u32 s73, s83, 589824\n\t"
"s_and_b32 s73, s73, 0xfffff\n\t"
"buffer_load_dwordx4 v242, s[44:47], s73 offen offset:0 lds\n\t"
"buffer_load_dwordx4 v243, s[44:47], s73 offen offset:1024 lds\n\t"
"buffer_load_dwordx4 v244, s[44:47], s73 offen offset:2048 lds\n\t"
"buffer_load_dwordx4 v245, s[44:47], s73 offen offset:3072 lds\n\t"
"v_accvgpr_read_b32 v36, a184\n\t"
"v_accvgpr_read_b32 v37, a185\n\t"
"v_accvgpr_read_b32 v38, a186\n\t"
"v_accvgpr_read_b32 v39, a187\n\t"
"v_accvgpr_read_b32 v40, a188\n\t"
"v_accvgpr_read_b32 v41, a189\n\t"
"v_accvgpr_read_b32 v42, a190\n\t"
"v_accvgpr_read_b32 v43, a191\n\t"
"v_pk_mul_f32 v[36:37], v[36:37], v[202:203] op_sel_hi:[1,0]\n\t"
"v_pk_mul_f32 v[38:39], v[38:39], v[202:203] op_sel_hi:[1,0]\n\t"
"v_pk_mul_f32 v[40:41], v[40:41], v[202:203] op_sel_hi:[1,0]\n\t"
"v_pk_mul_f32 v[42:43], v[42:43], v[202:203] op_sel_hi:[1,0]\n\t"
"v_cvt_pk_bf16_f32 v44, v36, v37\n\t"
"v_cvt_pk_bf16_f32 v45, v38, v39\n\t"
"v_cvt_pk_bf16_f32 v46, v40, v41\n\t"
"v_cvt_pk_bf16_f32 v47, v42, v43\n\t"
"s_movk_i32 s71, 0x5c00\n\t"
"buffer_store_dwordx4 v[44:47], v240, s[48:51], s71 offen offset:0 sc1\n\t"
"s_waitcnt vmcnt(0)\n\t"
"s_barrier\n\t"
"s_cmp_lg_u32 s52, 0\n\t"
"s_cbranch_scc1 NOFLAG%=\n\t"
"v_mov_b32_e32 v237, 0\n\t"
"v_add_u32_e64 v233, 1, v255\n\t"
"s_mov_b64 exec, 1\n\t"
"global_store_dword v237, v233, %12 sc1\n\t"
"s_mov_b64 exec, -1\n\t"
"NOFLAG%=:\n\t"
"s_add_u32 m0, s72, 131072\n\t"
"s_add_u32 s73, s83, 294912\n\t"
"s_and_b32 s73, s73, 0xfffff\n\t"
"buffer_load_dwordx4 v242, s[44:47], s73 offen offset:0 lds\n\t"
"buffer_load_dwordx4 v243, s[44:47], s73 offen offset:1024 lds\n\t"
"buffer_load_dwordx4 v244, s[44:47], s73 offen offset:2048 lds\n\t"
"buffer_load_dwordx4 v245, s[44:47], s73 offen offset:3072 lds\n\t"
"s_waitcnt vmcnt(56)\n\t"
"ds_read_b64_tr_b16 a[96:97], v248 offset:0\n\t"
"ds_read_b64_tr_b16 a[98:99], v248 offset:2048\n\t"
"ds_read_b64_tr_b16 a[100:101], v249 offset:0\n\t"
"ds_read_b64_tr_b16 a[102:103], v249 offset:2048\n\t"
"ds_read_b64_tr_b16 a[104:105], v248 offset:512\n\t"
"ds_read_b64_tr_b16 a[106:107], v248 offset:2560\n\t"
"ds_read_b64_tr_b16 a[108:109], v249 offset:512\n\t"
"ds_read_b64_tr_b16 a[110:111], v249 offset:2560\n\t"
"s_waitcnt vmcnt(48)\n\t"
"ds_read_b64_tr_b16 a[112:113], v246 offset:16384\n\t"
"ds_read_b64_tr_b16 a[114:115], v246 offset:18432\n\t"
"ds_read_b64_tr_b16 a[116:117], v247 offset:16384\n\t"
"ds_read_b64_tr_b16 a[118:119], v247 offset:18432\n\t"
"ds_read_b64_tr_b16 a[120:121], v246 offset:16896\n\t"
"ds_read_b64_tr_b16 a[122:123], v246 offset:18944\n\t"
"ds_read_b64_tr_b16 a[124:125], v247 offset:16896\n\t"
"ds_read_b64_tr_b16 a[126:127], v247 offset:18944\n\t"
"s_waitcnt lgkmcnt(8)\n\t"
"s_add_u32 m0, s72, 65536\n\t"
"s_add_u32 s73, s83, 655360\n\t"
"s_and_b32 s73, s73, 0xfffff\n\t"
"buffer_load_dwordx4 v242, s[44:47], s73 offen offset:0 lds\n\t"
"buffer_load_dwordx4 v243, s[44:47], s73 offen offset:1024 lds\n\t"
"buffer_load_dwordx4 v244, s[44:47], s73 offen offset:2048 lds\n\t"
"buffer_load_dwordx4 v245, s[44:47], s73 offen offset:3072 lds\n\t"
"s_waitcnt vmcnt(44)\n\t"
"ds_read_b64_tr_b16 a[128:129], v248 offset:16384\n\t"
"ds_read_b64_tr_b16 a[130:131], v248 offset:18432\n\t"
"ds_read_b64_tr_b16 a[132:133], v249 offset:16384\n\t"
"ds_read_b64_tr_b16 a[134:135], v249 offset:18432\n\t"
"ds_read_b64_tr_b16 a[136:137], v248 offset:16896\n\t"
"ds_read_b64_tr_b16 a[138:139], v248 offset:18944\n\t"
"ds_read_b64_tr_b16 a[140:141], v249 offset:16896\n\t"
"ds_read_b64_tr_b16 a[142:143], v249 offset:18944\n\t"
"s_waitcnt lgkmcnt(8)\n\t"
"s_add_u32 m0, s72, 16384\n\t"
"s_add_u32 s73, s83, 688128\n\t"
"s_and_b32 s73, s73, 0xfffff\n\t"
"buffer_load_dwordx4 v242, s[44:47], s73 offen offset:0 lds\n\t"
"buffer_load_dwordx4 v243, s[44:47], s73 offen offset:1024 lds\n\t"
"buffer_load_dwordx4 v244, s[44:47], s73 offen offset:2048 lds\n\t"
"buffer_load_dwordx4 v245, s[44:47], s73 offen offset:3072 lds\n\t"
"s_waitcnt vmcnt(40)\n\t"
"ds_read_b64_tr_b16 a[144:145], v246 offset:32768\n\t"
"ds_read_b64_tr_b16 a[146:147], v246 offset:34816\n\t"
"ds_read_b64_tr_b16 a[148:149], v247 offset:32768\n\t"
"ds_read_b64_tr_b16 a[150:151], v247 offset:34816\n\t"
"ds_read_b64_tr_b16 a[152:153], v246 offset:33280\n\t"
"ds_read_b64_tr_b16 a[154:155], v246 offset:35328\n\t"
"ds_read_b64_tr_b16 a[156:157], v247 offset:33280\n\t"
"ds_read_b64_tr_b16 a[158:159], v247 offset:35328\n\t"
"s_waitcnt lgkmcnt(8)\n\t"
"s_add_u32 m0, s72, 81920\n\t"
"s_add_u32 s73, s83, 720896\n\t"
"s_and_b32 s73, s73, 0xfffff\n\t"
"buffer_load_dwordx4 v242, s[44:47], s73 offen offset:0 lds\n\t"
"buffer_load_dwordx4 v243, s[44:47], s73 offen offset:1024 lds\n\t"
"buffer_load_dwordx4 v244, s[44:47], s73 offen offset:2048 lds\n\t"
"buffer_load_dwordx4 v245, s[44:47], s73 offen offset:3072 lds\n\t"
"s_waitcnt vmcnt(39)\n\t"
"ds_read_b64_tr_b16 a[160:161], v235 offset:16384\n\t"
"ds_read_b64_tr_b16 a[162:163], v235 offset:18432\n\t"
"ds_read_b64_tr_b16 a[164:165], v236 offset:16384\n\t"
"ds_read_b64_tr_b16 a[166:167], v236 offset:18432\n\t"
"ds_read_b64_tr_b16 a[168:169], v235 offset:16896\n\t"
"ds_read_b64_tr_b16 a[170:171], v235 offset:18944\n\t"
"ds_read_b64_tr_b16 a[172:173], v236 offset:16896\n\t"
"ds_read_b64_tr_b16 a[174:175], v236 offset:18944\n\t"
"s_waitcnt lgkmcnt(8)\n\t"
"s_add_u32 m0, s72, 32768\n\t"
"s_add_u32 s73, s83, 753664\n\t"
"s_and_b32 s73, s73, 0xfffff\n\t"
"buffer_load_dwordx4 v242, s[44:47], s73 offen offset:0 lds\n\t"
"buffer_load_dwordx4 v243, s[44:47], s73 offen offset:1024 lds\n\t"
"buffer_load_dwordx4 v244, s[44:47], s73 offen offset:2048 lds\n\t"
"buffer_load_dwordx4 v245, s[44:47], s73 offen offset:3072 lds\n\t"
"s_waitcnt vmcnt(37)\n\t"
"ds_read_b64_tr_b16 a[176:177], v248 offset:32768\n\t"
"ds_read_b64_tr_b16 a[178:179], v248 offset:34816\n\t"
"ds_read_b64_tr_b16 a[180:181], v249 offset:32768\n\t"
"ds_read_b64_tr_b16 a[182:183], v249 offset:34816\n\t"
"ds_read_b64_tr_b16 a[184:185], v248 offset:33280\n\t"
"ds_read_b64_tr_b16 a[186:187], v248 offset:35328\n\t"
"ds_read_b64_tr_b16 a[188:189], v249 offset:33280\n\t"
"ds_read_b64_tr_b16 a[190:191], v249 offset:35328\n\t"
"s_waitcnt lgkmcnt(8)\n\t"
"s_add_u32 m0, s72, 147456\n\t"
"s_add_u32 s73, s83, 786432\n\t"
"s_and_b32 s73, s73, 0xfffff\n\t"
"buffer_load_dwordx4 v242, s[44:47], s73 offen offset:0 lds\n\t"
"buffer_load_dwordx4 v243, s[44:47], s73 offen offset:1024 lds\n\t"
"buffer_load_dwordx4 v244, s[44:47], s73 offen offset:2048 lds\n\t"
"buffer_load_dwordx4 v245, s[44:47], s73 offen offset:3072 lds\n\t"
"s_waitcnt vmcnt(20)\n\t"
"ds_read_b64_tr_b16 a[80:81], v235 offset:0\n\t"
"ds_read_b64_tr_b16 a[82:83], v235 offset:2048\n\t"
"ds_read_b64_tr_b16 a[84:85], v236 offset:0\n\t"
"ds_read_b64_tr_b16 a[86:87], v236 offset:2048\n\t"
"ds_read_b64_tr_b16 a[88:89], v235 offset:512\n\t"
"ds_read_b64_tr_b16 a[90:91], v235 offset:2560\n\t"
"ds_read_b64_tr_b16 a[92:93], v236 offset:512\n\t"
"ds_read_b64_tr_b16 a[94:95], v236 offset:2560\n\t"
"s_waitcnt lgkmcnt(8)\n\t"
"s_add_u32 m0, s72, 98304\n\t"
"s_add_u32 s73, s83, 819200\n\t"
"s_and_b32 s73, s73, 0xfffff\n\t"
"buffer_load_dwordx4 v242, s[44:47], s73 offen offset:0 lds\n\t"
"buffer_load_dwordx4 v243, s[44:47], s73 offen offset:1024 lds\n\t"
"buffer_load_dwordx4 v244, s[44:47], s73 offen offset:2048 lds\n\t"
"buffer_load_dwordx4 v245, s[44:47], s73 offen offset:3072 lds\n\t"
"s_waitcnt vmcnt(39)\n\t"
"ds_read_b64_tr_b16 a[192:193], v246 offset:49152\n\t"
"ds_read_b64_tr_b16 a[194:195], v246 offset:51200\n\t"
"ds_read_b64_tr_b16 a[196:197], v247 offset:49152\n\t"
"ds_read_b64_tr_b16 a[198:199], v247 offset:51200\n\t"
"ds_read_b64_tr_b16 a[200:201], v246 offset:49664\n\t"
"ds_read_b64_tr_b16 a[202:203], v246 offset:51712\n\t"
"ds_read_b64_tr_b16 a[204:205], v247 offset:49664\n\t"
"ds_read_b64_tr_b16 a[206:207], v247 offset:51712\n\t"
"s_waitcnt lgkmcnt(8)\n\t"
"s_add_u32 m0, s72, 131072\n\t"
"s_add_u32 s73, s83, 622592\n\t"
"s_and_b32 s73, s73, 0xfffff\n\t"
"buffer_load_dwordx4 v242, s[44:47], s73 offen offset:0 lds\n\t"
"buffer_load_dwordx4 v243, s[44:47], s73 offen offset:1024 lds\n\t"
"buffer_load_dwordx4 v244, s[44:47], s73 offen offset:2048 lds\n\t"
"buffer_load_dwordx4 v245, s[44:47], s73 offen offset:3072 lds\n\t"
"s_waitcnt vmcnt(38)\n\t"
"ds_read_b64_tr_b16 a[208:209], v248 offset:49152\n\t"
"ds_read_b64_tr_b16 a[210:211], v248 offset:51200\n\t"
"ds_read_b64_tr_b16 a[212:213], v249 offset:49152\n\t"
"ds_read_b64_tr_b16 a[214:215], v249 offset:51200\n\t"
"ds_read_b64_tr_b16 a[216:217], v248 offset:49664\n\t"
"ds_read_b64_tr_b16 a[218:219], v248 offset:51712\n\t"
"ds_read_b64_tr_b16 a[220:221], v249 offset:49664\n\t"
"ds_read_b64_tr_b16 a[222:223], v249 offset:51712\n\t"
"s_waitcnt lgkmcnt(8)\n\t"
"s_add_u32 m0, s72, 49152\n\t"
"s_add_u32 s73, s83, 851968\n\t"
"s_and_b32 s73, s73, 0xfffff\n\t"
"buffer_load_dwordx4 v242, s[44:47], s73 offen offset:0 lds\n\t"
"buffer_load_dwordx4 v243, s[44:47], s73 offen offset:1024 lds\n\t"
"buffer_load_dwordx4 v244, s[44:47], s73 offen offset:2048 lds\n\t"
"buffer_load_dwordx4 v245, s[44:47], s73 offen offset:3072 lds\n\t"
"s_waitcnt vmcnt(37)\n\t"
"ds_read_b64_tr_b16 a[224:225], v246 offset:0\n\t"
"ds_read_b64_tr_b16 a[226:227], v246 offset:2048\n\t"
"ds_read_b64_tr_b16 a[228:229], v247 offset:0\n\t"
"ds_read_b64_tr_b16 a[230:231], v247 offset:2048\n\t"
"ds_read_b64_tr_b16 a[232:233], v246 offset:512\n\t"
"ds_read_b64_tr_b16 a[234:235], v246 offset:2560\n\t"
"ds_read_b64_tr_b16 a[236:237], v247 offset:512\n\t"
"ds_read_b64_tr_b16 a[238:239], v247 offset:2560\n\t"
"s_waitcnt lgkmcnt(8)\n\t"
"s_add_u32 m0, s72, 114688\n\t"
"s_add_u32 s73, s83, 884736\n\t"
"s_and_b32 s73, s73, 0xfffff\n\t"
"buffer_load_dwordx4 v242, s[44:47], s73 offen offset:0 lds\n\t"
"buffer_load_dwordx4 v243, s[44:47], s73 offen offset:1024 lds\n\t"
"buffer_load_dwordx4 v244, s[44:47], s73 offen offset:2048 lds\n\t"
"buffer_load_dwordx4 v245, s[44:47], s73 offen offset:3072 lds\n\t"
"s_waitcnt vmcnt(8)\n\t"
"ds_read_b64_tr_b16 a[240:241], v235 offset:0\n\t"
"ds_read_b64_tr_b16 a[242:243], v235 offset:2048\n\t"
"ds_read_b64_tr_b16 a[244:245], v236 offset:0\n\t"
"ds_read_b64_tr_b16 a[246:247], v236 offset:2048\n\t"
"ds_read_b64_tr_b16 a[248:249], v235 offset:512\n\t"
"ds_read_b64_tr_b16 a[250:251], v235 offset:2560\n\t"
"ds_read_b64_tr_b16 a[252:253], v236 offset:512\n\t"
"ds_read_b64_tr_b16 a[254:255], v236 offset:2560\n\t"
"s_waitcnt lgkmcnt(8)\n\t"
"s_mov_b32 m0, s72\n\t"
"s_add_u32 s73, s83, 917504\n\t"
"s_and_b32 s73, s73, 0xfffff\n\t"
"buffer_load_dwordx4 v242, s[44:47], s73 offen offset:0 lds\n\t"
"buffer_load_dwordx4 v243, s[44:47], s73 offen offset:1024 lds\n\t"
"buffer_load_dwordx4 v244, s[44:47], s73 offen offset:2048 lds\n\t"
"buffer_load_dwordx4 v245, s[44:47], s73 offen offset:3072 lds\n\t"
"s_waitcnt lgkmcnt(0)\n\t"
"s_add_u32 m0, s72, 131072\n\t"
"s_add_u32 s73, s83, 950272\n\t"
"s_and_b32 s73, s73, 0xfffff\n\t"
"buffer_load_dwordx4 v242, s[44:47], s73 offen offset:0 lds\n\t"
"buffer_load_dwordx4 v243, s[44:47], s73 offen offset:1024 lds\n\t"
"buffer_load_dwordx4 v244, s[44:47], s73 offen offset:2048 lds\n\t"
"buffer_load_dwordx4 v245, s[44:47], s73 offen offset:3072 lds\n\t"
"v_and_b32_e32 v237, 3, v252\n\t"
"v_lshlrev_b32_e32 v239, 9, v237\n\t"
"v_lshlrev_b32_e64 v237, 10, v237\n\t"
"s_mov_b32 s71, 0x10000\n\t"
"s_mov_b64 s[36:37], %11\n\t"
"s_and_b32 s37, s37, 0xffff\n\t"
"s_mov_b32 s38, 194688\n\t"
"v_add_u32_e32 v254, 1, v254\n\t"
"POLL%=:\n\t"
"global_load_dword v238, v237, %13 sc1\n\t"
"s_waitcnt vmcnt(0)\n\t"
"v_cmp_eq_u32_e32 vcc, v253, v238\n\t"
"s_cbranch_vccz GO%=\n\t"
"s_sleep 2\n\t"
"s_sub_u32 s71, s71, 1\n\t"
"s_cmp_lg_u32 s71, 0\n\t"
"s_cbranch_scc1 POLL%=\n\t"
"GO%=:\n\t"
"s_mov_b64 exec, 15\n\t"
"global_store_dword v239, v254, %17 sc1\n\t"
"s_mov_b64 exec, -1\n\t"
"s_mov_b32 s56, s58\n\t"
"s_add_u32 s57, s58, 0x1000\n\t"
"s_add_u32 s58, s58, 6144\n\t"
"s_sub_u32 s59, s58, 196608\n\t"
"s_cselect_b32 s58, s58, s59\n\t"
"buffer_load_dwordx4 v[160:163], v240, s[40:43], s56 offen offset:0 sc1\n\t"
"buffer_load_dwordx4 v[164:167], v240, s[40:43], s56 offen offset:1024 sc1\n\t"
"buffer_load_dwordx4 v[168:171], v240, s[40:43], s56 offen offset:2048 sc1\n\t"
"buffer_load_dwordx4 v[172:175], v240, s[40:43], s56 offen offset:3072 sc1\n\t"
"buffer_load_dwordx4 v[176:179], v240, s[40:43], s57 offen offset:0 sc1\n\t"
"buffer_load_dwordx4 v[180:183], v240, s[40:43], s57 offen offset:1024 sc1\n\t"
"s_mov_b32 s56, s58\n\t"
"s_add_u32 s57, s58, 0x1000\n\t"
"s_add_u32 s58, s58, 6144\n\t"
"s_sub_u32 s59, s58, 196608\n\t"
"s_cselect_b32 s58, s58, s59\n\t"
"buffer_load_dwordx4 v[184:187], v240, s[40:43], s56 offen offset:0 sc1\n\t"
"buffer_load_dwordx4 v[188:191], v240, s[40:43], s56 offen offset:1024 sc1\n\t"
"buffer_load_dwordx4 v[192:195], v240, s[40:43], s56 offen offset:2048 sc1\n\t"
"buffer_load_dwordx4 v[196:199], v240, s[40:43], s56 offen offset:3072 sc1\n\t"
"buffer_load_dwordx4 v[200:203], v240, s[40:43], s57 offen offset:0 sc1\n\t"
"buffer_load_dwordx4 v[204:207], v240, s[40:43], s57 offen offset:1024 sc1\n\t"
"s_mov_b32 s56, s58\n\t"
"s_add_u32 s57, s58, 0x1000\n\t"
"s_add_u32 s58, s58, 6144\n\t"
"s_sub_u32 s59, s58, 196608\n\t"
"s_cselect_b32 s58, s58, s59\n\t"
"buffer_load_dwordx4 v[208:211], v240, s[40:43], s56 offen offset:0 sc1\n\t"
"buffer_load_dwordx4 v[212:215], v240, s[40:43], s56 offen offset:1024 sc1\n\t"
"buffer_load_dwordx4 v[216:219], v240, s[40:43], s56 offen offset:2048 sc1\n\t"
"buffer_load_dwordx4 v[220:223], v240, s[40:43], s56 offen offset:3072 sc1\n\t"
"buffer_load_dwordx4 v[224:227], v240, s[40:43], s57 offen offset:0 sc1\n\t"
"buffer_load_dwordx4 v[228:231], v240, s[40:43], s57 offen offset:1024 sc1\n\t"
"s_waitcnt vmcnt(12) lgkmcnt(0)\n\t"
"v_mfma_f32_16x16x32_bf16 v[0:3], v[96:99], v[160:163], 0\n\t"
"s_mov_b32 s56, s58\n\t"
"v_mfma_f32_16x16x32_bf16 v[4:7], v[100:103], v[160:163], 0\n\t"
"s_add_u32 s57, s58, 0x1000\n\t"
"v_mfma_f32_16x16x32_bf16 v[8:11], v[104:107], v[160:163], 0\n\t"
"v_mfma_f32_16x16x32_bf16 v[12:15], v[108:111], v[160:163], 0\n\t"
"buffer_load_dwordx4 v[160:163], v240, s[40:43], s56 offen offset:0 sc1\n\t"
"v_mfma_f32_16x16x32_bf16 v[16:19], v[96:99], v[164:167], 0\n\t"
"v_mfma_f32_16x16x32_bf16 v[20:23], v[100:103], v[164:167], 0\n\t"
"v_mfma_f32_16x16x32_bf16 v[24:27], v[104:107], v[164:167], 0\n\t"
"v_mfma_f32_16x16x32_bf16 v[28:31], v[108:111], v[164:167], 0\n\t"
"buffer_load_dwordx4 v[164:167], v240, s[40:43], s56 offen offset:1024 sc1\n\t"
"v_mfma_f32_16x16x32_bf16 v[32:35], v[96:99], v[168:171], 0\n\t"
"v_mfma_f32_16x16x32_bf16 v[36:39], v[100:103], v[168:171], 0\n\t"
"v_mfma_f32_16x16x32_bf16 v[40:43], v[104:107], v[168:171], 0\n\t"
"v_mfma_f32_16x16x32_bf16 v[44:47], v[108:111], v[168:171], 0\n\t"
"buffer_load_dwordx4 v[168:171], v240, s[40:43], s56 offen offset:2048 sc1\n\t"
"v_mfma_f32_16x16x32_bf16 v[48:51], v[96:99], v[172:175], 0\n\t"
"v_mfma_f32_16x16x32_bf16 v[52:55], v[100:103], v[172:175], 0\n\t"
"v_mfma_f32_16x16x32_bf16 v[56:59], v[104:107], v[172:175], 0\n\t"
"v_mfma_f32_16x16x32_bf16 v[60:63], v[108:111], v[172:175], 0\n\t"
"buffer_load_dwordx4 v[172:175], v240, s[40:43], s56 offen offset:3072 sc1\n\t"
"v_mfma_f32_16x16x32_bf16 v[64:67], v[96:99], v[176:179], 0\n\t"
"s_add_u32 s58, s58, 6144\n\t"
"v_mfma_f32_16x16x32_bf16 v[68:71], v[100:103], v[176:179], 0\n\t"
"s_sub_u32 s59, s58, 196608\n\t"
"v_mfma_f32_16x16x32_bf16 v[72:75], v[104:107], v[176:179], 0\n\t"
"s_cselect_b32 s58, s58, s59\n\t"
"v_mfma_f32_16x16x32_bf16 v[76:79], v[108:111], v[176:179], 0\n\t"
"buffer_load_dwordx4 v[176:179], v240, s[40:43], s57 offen offset:0 sc1\n\t"
"v_mfma_f32_16x16x32_bf16 v[80:83], v[96:99], v[180:183], 0\n\t"
"v_mfma_f32_16x16x32_bf16 v[84:87], v[100:103], v[180:183], 0\n\t"
"v_mfma_f32_16x16x32_bf16 v[88:91], v[104:107], v[180:183], 0\n\t"
"v_mfma_f32_16x16x32_bf16 v[92:95], v[108:111], v[180:183], 0\n\t"
"buffer_load_dwordx4 v[180:183], v240, s[40:43], s57 offen offset:1024 sc1\n\t"
"s_waitcnt vmcnt(12) lgkmcnt(0)\n\t"
"v_mfma_f32_16x16x32_bf16 v[0:3], v[112:115], v[184:187], v[0:3]\n\t"
"s_mov_b32 s56, s58\n\t"
"ds_read_b64_tr_b16 v[96:97], v248 offset:0\n\t"
"v_mfma_f32_16x16x32_bf16 v[4:7], v[116:119], v[184:187], v[4:7]\n\t"
"s_add_u32 s57, s58, 0x1000\n\t"
"v_mfma_f32_16x16x32_bf16 v[8:11], v[120:123], v[184:187], v[8:11]\n\t"
"ds_read_b64_tr_b16 v[98:99], v248 offset:2048\n\t"
"v_mfma_f32_16x16x32_bf16 v[12:15], v[124:127], v[184:187], v[12:15]\n\t"
"buffer_load_dwordx4 v[184:187], v240, s[40:43], s56 offen offset:0 sc1\n\t"
"v_mfma_f32_16x16x32_bf16 v[16:19], v[112:115], v[188:191], v[16:19]\n\t"
"ds_read_b64_tr_b16 v[100:101], v249 offset:0\n\t"
"v_mfma_f32_16x16x32_bf16 v[20:23], v[116:119], v[188:191], v[20:23]\n\t"
"v_mfma_f32_16x16x32_bf16 v[24:27], v[120:123], v[188:191], v[24:27]\n\t"
"v_mfma_f32_16x16x32_bf16 v[28:31], v[124:127], v[188:191], v[28:31]\n\t"
"buffer_load_dwordx4 v[188:191], v240, s[40:43], s56 offen offset:1024 sc1\n\t"
"v_mfma_f32_16x16x32_bf16 v[32:35], v[112:115], v[192:195], v[32:35]\n\t"
"ds_read_b64_tr_b16 v[102:103], v249 offset:2048\n\t"
"v_mfma_f32_16x16x32_bf16 v[36:39], v[116:119], v[192:195], v[36:39]\n\t"
"ds_read_b64_tr_b16 v[104:105], v248 offset:512\n\t"
"v_mfma_f32_16x16x32_bf16 v[40:43], v[120:123], v[192:195], v[40:43]\n\t"
"v_mfma_f32_16x16x32_bf16 v[44:47], v[124:127], v[192:195], v[44:47]\n\t"
"buffer_load_dwordx4 v[192:195], v240, s[40:43], s56 offen offset:2048 sc1\n\t"
"v_mfma_f32_16x16x32_bf16 v[48:51], v[112:115], v[196:199], v[48:51]\n\t"
"ds_read_b64_tr_b16 v[106:107], v248 offset:2560\n\t"
"v_mfma_f32_16x16x32_bf16 v[52:55], v[116:119], v[196:199], v[52:55]\n\t"
"v_mfma_f32_16x16x32_bf16 v[56:59], v[120:123], v[196:199], v[56:59]\n\t"
"ds_read_b64_tr_b16 v[108:109], v249 offset:512\n\t"
"v_mfma_f32_16x16x32_bf16 v[60:63], v[124:127], v[196:199], v[60:63]\n\t"
"buffer_load_dwordx4 v[196:199], v240, s[40:43], s56 offen offset:3072 sc1\n\t"
"v_mfma_f32_16x16x32_bf16 v[64:67], v[112:115], v[200:203], v[64:67]\n\t"
"s_add_u32 s58, s58, 6144\n\t"
"ds_read_b64_tr_b16 v[110:111], v249 offset:2560\n\t"
"v_mfma_f32_16x16x32_bf16 v[68:71], v[116:119], v[200:203], v[68:71]\n\t"
"s_sub_u32 s59, s58, 196608\n\t"
"v_mfma_f32_16x16x32_bf16 v[72:75], v[120:123], v[200:203], v[72:75]\n\t"
"s_cselect_b32 s58, s58, s59\n\t"
"v_mfma_f32_16x16x32_bf16 v[76:79], v[124:127], v[200:203], v[76:79]\n\t"
"buffer_load_dwordx4 v[200:203], v240, s[40:43], s57 offen offset:0 sc1\n\t"
"v_mfma_f32_16x16x32_bf16 v[80:83], v[112:115], v[204:207], v[80:83]\n\t"
"v_mfma_f32_16x16x32_bf16 v[84:87], v[116:119], v[204:207], v[84:87]\n\t"
"v_mfma_f32_16x16x32_bf16 v[88:91], v[120:123], v[204:207], v[88:91]\n\t"
"v_mfma_f32_16x16x32_bf16 v[92:95], v[124:127], v[204:207], v[92:95]\n\t"
"buffer_load_dwordx4 v[204:207], v240, s[40:43], s57 offen offset:1024 sc1\n\t"
"s_waitcnt vmcnt(12) lgkmcnt(0)\n\t"
"v_mfma_f32_16x16x32_bf16 v[0:3], v[128:131], v[208:211], v[0:3]\n\t"
"s_mov_b32 s56, s58\n\t"
"ds_read_b64_tr_b16 v[112:113], v246 offset:16384\n\t"
"v_mfma_f32_16x16x32_bf16 v[4:7], v[132:135], v[208:211], v[4:7]\n\t"
"s_add_u32 s57, s58, 0x1000\n\t"
"v_mfma_f32_16x16x32_bf16 v[8:11], v[136:139], v[208:211], v[8:11]\n\t"
"s_add_u32 m0, s72, 65536\n\t"
"ds_read_b64_tr_b16 v[114:115], v246 offset:18432\n\t"
"v_mfma_f32_16x16x32_bf16 v[12:15], v[140:143], v[208:211], v[12:15]\n\t"
"s_add_u32 s73, s83, 983040\n\t"
"v_mfma_f32_16x16x32_bf16 v[16:19], v[128:131], v[212:215], v[16:19]\n\t"
"s_and_b32 s73, s73, 0xfffff\n\t"
"ds_read_b64_tr_b16 v[116:117], v247 offset:16384\n\t"
"v_mfma_f32_16x16x32_bf16 v[20:23], v[132:135], v[212:215], v[20:23]\n\t"
"buffer_load_dwordx4 v242, s[44:47], s73 offen offset:0 lds\n\t"
"v_mfma_f32_16x16x32_bf16 v[24:27], v[136:139], v[212:215], v[24:27]\n\t"
"buffer_load_dwordx4 v243, s[44:47], s73 offen offset:1024 lds\n\t"
"v_mfma_f32_16x16x32_bf16 v[28:31], v[140:143], v[212:215], v[28:31]\n\t"
"buffer_load_dwordx4 v244, s[44:47], s73 offen offset:2048 lds\n\t"
"v_mfma_f32_16x16x32_bf16 v[32:35], v[128:131], v[216:219], v[32:35]\n\t"
"buffer_load_dwordx4 v245, s[44:47], s73 offen offset:3072 lds\n\t"
"v_mfma_f32_16x16x32_bf16 v[36:39], v[132:135], v[216:219], v[36:39]\n\t"
"ds_read_b64_tr_b16 v[118:119], v247 offset:18432\n\t"
"v_mfma_f32_16x16x32_bf16 v[40:43], v[136:139], v[216:219], v[40:43]\n\t"
"ds_read_b64_tr_b16 v[120:121], v246 offset:16896\n\t"
"v_mfma_f32_16x16x32_bf16 v[44:47], v[140:143], v[216:219], v[44:47]\n\t"
"ds_read_b64_tr_b16 v[122:123], v246 offset:18944\n\t"
"v_mfma_f32_16x16x32_bf16 v[48:51], v[128:131], v[220:223], v[48:51]\n\t"
"buffer_load_dwordx4 v[208:211], v240, s[40:43], s56 offen offset:0 sc1\n\t"
"v_mfma_f32_16x16x32_bf16 v[52:55], v[132:135], v[220:223], v[52:55]\n\t"
"buffer_load_dwordx4 v[212:215], v240, s[40:43], s56 offen offset:1024 sc1\n\t"
"v_mfma_f32_16x16x32_bf16 v[56:59], v[136:139], v[220:223], v[56:59]\n\t"
"buffer_load_dwordx4 v[216:219], v240, s[40:43], s56 offen offset:2048 sc1\n\t"
"v_mfma_f32_16x16x32_bf16 v[60:63], v[140:143], v[220:223], v[60:63]\n\t"
"buffer_load_dwordx4 v[220:223], v240, s[40:43], s56 offen offset:3072 sc1\n\t"
"v_mfma_f32_16x16x32_bf16 v[64:67], v[128:131], v[224:227], v[64:67]\n\t"
"s_add_u32 s58, s58, 6144\n\t"
"ds_read_b64_tr_b16 v[124:125], v247 offset:16896\n\t"
"v_mfma_f32_16x16x32_bf16 v[68:71], v[132:135], v[224:227], v[68:71]\n\t"
"s_sub_u32 s59, s58, 196608\n\t"
"ds_read_b64_tr_b16 v[126:127], v247 offset:18944\n\t"
"v_mfma_f32_16x16x32_bf16 v[72:75], v[136:139], v[224:227], v[72:75]\n\t"
"s_cselect_b32 s58, s58, s59\n\t"
"v_mfma_f32_16x16x32_bf16 v[76:79], v[140:143], v[224:227], v[76:79]\n\t"
"buffer_load_dwordx4 v[224:227], v240, s[40:43], s57 offen offset:0 sc1\n\t"
"v_mfma_f32_16x16x32_bf16 v[80:83], v[128:131], v[228:231], v[80:83]\n\t"
"v_mfma_f32_16x16x32_bf16 v[84:87], v[132:135], v[228:231], v[84:87]\n\t"
"v_mfma_f32_16x16x32_bf16 v[88:91], v[136:139], v[228:231], v[88:91]\n\t"
"v_mfma_f32_16x16x32_bf16 v[92:95], v[140:143], v[228:231], v[92:95]\n\t"
"buffer_load_dwordx4 v[228:231], v240, s[40:43], s57 offen offset:1024 sc1\n\t"
"s_waitcnt vmcnt(16) lgkmcnt(0)\n\t"
"v_mfma_f32_16x16x32_bf16 v[0:3], v[144:147], v[160:163], v[0:3]\n\t"
"s_mov_b32 s56, s58\n\t"
"ds_read_b64_tr_b16 v[128:129], v248 offset:16384\n\t"
"v_mfma_f32_16x16x32_bf16 v[4:7], v[148:151], v[160:163], v[4:7]\n\t"
"s_add_u32 s57, s58, 0x1000\n\t"
"v_mfma_f32_16x16x32_bf16 v[8:11], v[152:155], v[160:163], v[8:11]\n\t"
"s_add_u32 m0, s72, 16384\n\t"
"ds_read_b64_tr_b16 v[130:131], v248 offset:18432\n\t"
"v_mfma_f32_16x16x32_bf16 v[12:15], v[156:159], v[160:163], v[12:15]\n\t"
"s_add_u32 s73, s83, 1015808\n\t"
"v_mfma_f32_16x16x32_bf16 v[16:19], v[144:147], v[164:167], v[16:19]\n\t"
"s_and_b32 s73, s73, 0xfffff\n\t"
"ds_read_b64_tr_b16 v[132:133], v249 offset:16384\n\t"
"v_mfma_f32_16x16x32_bf16 v[20:23], v[148:151], v[164:167], v[20:23]\n\t"
"buffer_load_dwordx4 v242, s[44:47], s73 offen offset:0 lds\n\t"
"v_mfma_f32_16x16x32_bf16 v[24:27], v[152:155], v[164:167], v[24:27]\n\t"
"buffer_load_dwordx4 v243, s[44:47], s73 offen offset:1024 lds\n\t"
"v_mfma_f32_16x16x32_bf16 v[28:31], v[156:159], v[164:167], v[28:31]\n\t"
"buffer_load_dwordx4 v244, s[44:47], s73 offen offset:2048 lds\n\t"
"v_mfma_f32_16x16x32_bf16 v[32:35], v[144:147], v[168:171], v[32:35]\n\t"
"buffer_load_dwordx4 v245, s[44:47], s73 offen offset:3072 lds\n\t"
"v_mfma_f32_16x16x32_bf16 v[36:39], v[148:151], v[168:171], v[36:39]\n\t"
"ds_read_b64_tr_b16 v[134:135], v249 offset:18432\n\t"
"v_mfma_f32_16x16x32_bf16 v[40:43], v[152:155], v[168:171], v[40:43]\n\t"
"ds_read_b64_tr_b16 v[136:137], v248 offset:16896\n\t"
"v_mfma_f32_16x16x32_bf16 v[44:47], v[156:159], v[168:171], v[44:47]\n\t"
"ds_read_b64_tr_b16 v[138:139], v248 offset:18944\n\t"
"v_mfma_f32_16x16x32_bf16 v[48:51], v[144:147], v[172:175], v[48:51]\n\t"
"buffer_load_dwordx4 v[160:163], v240, s[40:43], s56 offen offset:0 sc1\n\t"
"v_mfma_f32_16x16x32_bf16 v[52:55], v[148:151], v[172:175], v[52:55]\n\t"
"buffer_load_dwordx4 v[164:167], v240, s[40:43], s56 offen offset:1024 sc1\n\t"
"v_mfma_f32_16x16x32_bf16 v[56:59], v[152:155], v[172:175], v[56:59]\n\t"
"buffer_load_dwordx4 v[168:171], v240, s[40:43], s56 offen offset:2048 sc1\n\t"
"v_mfma_f32_16x16x32_bf16 v[60:63], v[156:159], v[172:175], v[60:63]\n\t"
"buffer_load_dwordx4 v[172:175], v240, s[40:43], s56 offen offset:3072 sc1\n\t"
"v_mfma_f32_16x16x32_bf16 v[64:67], v[144:147], v[176:179], v[64:67]\n\t"
"s_add_u32 s58, s58, 6144\n\t"
"ds_read_b64_tr_b16 v[140:141], v249 offset:16896\n\t"
"v_mfma_f32_16x16x32_bf16 v[68:71], v[148:151], v[176:179], v[68:71]\n\t"
"s_sub_u32 s59, s58, 196608\n\t"
"ds_read_b64_tr_b16 v[142:143], v249 offset:18944\n\t"
"v_mfma_f32_16x16x32_bf16 v[72:75], v[152:155], v[176:179], v[72:75]\n\t"
"s_cselect_b32 s58, s58, s59\n\t"
"v_mfma_f32_16x16x32_bf16 v[76:79], v[156:159], v[176:179], v[76:79]\n\t"
"buffer_load_dwordx4 v[176:179], v240, s[40:43], s57 offen offset:0 sc1\n\t"
"v_mfma_f32_16x16x32_bf16 v[80:83], v[144:147], v[180:183], v[80:83]\n\t"
"v_mfma_f32_16x16x32_bf16 v[84:87], v[148:151], v[180:183], v[84:87]\n\t"
"v_mfma_f32_16x16x32_bf16 v[88:91], v[152:155], v[180:183], v[88:91]\n\t"
"v_mfma_f32_16x16x32_bf16 v[92:95], v[156:159], v[180:183], v[92:95]\n\t"
"buffer_load_dwordx4 v[180:183], v240, s[40:43], s57 offen offset:1024 sc1\n\t"
"s_waitcnt vmcnt(20) lgkmcnt(0)\n\t"
"v_mfma_f32_16x16x32_bf16 v[0:3], a[0:3], v[184:187], v[0:3]\n\t"
"s_mov_b32 s56, s58\n\t"
"ds_read_b64_tr_b16 v[144:145], v246 offset:32768\n\t"
"v_mfma_f32_16x16x32_bf16 v[4:7], a[4:7], v[184:187], v[4:7]\n\t"
"s_add_u32 s57, s58, 0x1000\n\t"
"v_mfma_f32_16x16x32_bf16 v[8:11], a[8:11], v[184:187], v[8:11]\n\t"
"ds_read_b64_tr_b16 v[146:147], v246 offset:34816\n\t"
"v_mfma_f32_16x16x32_bf16 v[12:15], a[12:15], v[184:187], v[12:15]\n\t"
"buffer_load_dwordx4 v[184:187], v240, s[40:43], s56 offen offset:0 sc1\n\t"
"v_mfma_f32_16x16x32_bf16 v[16:19], a[0:3], v[188:191], v[16:19]\n\t"
"ds_read_b64_tr_b16 v[148:149], v247 offset:32768\n\t"
"v_mfma_f32_16x16x32_bf16 v[20:23], a[4:7], v[188:191], v[20:23]\n\t"
"v_mfma_f32_16x16x32_bf16 v[24:27], a[8:11], v[188:191], v[24:27]\n\t"
"v_mfma_f32_16x16x32_bf16 v[28:31], a[12:15], v[188:191], v[28:31]\n\t"
"buffer_load_dwordx4 v[188:191], v240, s[40:43], s56 offen offset:1024 sc1\n\t"
"v_mfma_f32_16x16x32_bf16 v[32:35], a[0:3], v[192:195], v[32:35]\n\t"
"ds_read_b64_tr_b16 v[150:151], v247 offset:34816\n\t"
"v_mfma_f32_16x16x32_bf16 v[36:39], a[4:7], v[192:195], v[36:39]\n\t"
"ds_read_b64_tr_b16 v[152:153], v246 offset:33280\n\t"
"v_mfma_f32_16x16x32_bf16 v[40:43], a[8:11], v[192:195], v[40:43]\n\t"
"v_mfma_f32_16x16x32_bf16 v[44:47], a[12:15], v[192:195], v[44:47]\n\t"
"buffer_load_dwordx4 v[192:195], v240, s[40:43], s56 offen offset:2048 sc1\n\t"
"v_mfma_f32_16x16x32_bf16 v[48:51], a[0:3], v[196:199], v[48:51]\n\t"
"ds_read_b64_tr_b16 v[154:155], v246 offset:35328\n\t"
"v_mfma_f32_16x16x32_bf16 v[52:55], a[4:7], v[196:199], v[52:55]\n\t"
"v_mfma_f32_16x16x32_bf16 v[56:59], a[8:11], v[196:199], v[56:59]\n\t"
"ds_read_b64_tr_b16 v[156:157], v247 offset:33280\n\t"
"v_mfma_f32_16x16x32_bf16 v[60:63], a[12:15], v[196:199], v[60:63]\n\t"
"buffer_load_dwordx4 v[196:199], v240, s[40:43], s56 offen offset:3072 sc1\n\t"
"v_mfma_f32_16x16x32_bf16 v[64:67], a[0:3], v[200:203], v[64:67]\n\t"
"s_add_u32 s58, s58, 6144\n\t"
"ds_read_b64_tr_b16 v[158:159], v247 offset:35328\n\t"
"v_mfma_f32_16x16x32_bf16 v[68:71], a[4:7], v[200:203], v[68:71]\n\t"
"s_sub_u32 s59, s58, 196608\n\t"
"v_mfma_f32_16x16x32_bf16 v[72:75], a[8:11], v[200:203], v[72:75]\n\t"
"s_cselect_b32 s58, s58, s59\n\t"
"v_mfma_f32_16x16x32_bf16 v[76:79], a[12:15], v[200:203], v[76:79]\n\t"
"buffer_load_dwordx4 v[200:203], v240, s[40:43], s57 offen offset:0 sc1\n\t"
"v_mfma_f32_16x16x32_bf16 v[80:83], a[0:3], v[204:207], v[80:83]\n\t"
"v_mfma_f32_16x16x32_bf16 v[84:87], a[4:7], v[204:207], v[84:87]\n\t"
"v_mfma_f32_16x16x32_bf16 v[88:91], a[8:11], v[204:207], v[88:91]\n\t"
"v_mfma_f32_16x16x32_bf16 v[92:95], a[12:15], v[204:207], v[92:95]\n\t"
"buffer_load_dwordx4 v[204:207], v240, s[40:43], s57 offen offset:1024 sc1\n\t"
"s_waitcnt vmcnt(16) lgkmcnt(0)\n\t"
"v_mfma_f32_16x16x32_bf16 v[0:3], a[16:19], v[208:211], v[0:3]\n\t"
"s_mov_b32 s56, s58\n\t"
"ds_read_b64_tr_b16 a[0:1], v235 offset:16384\n\t"
"v_mfma_f32_16x16x32_bf16 v[4:7], a[20:23], v[208:211], v[4:7]\n\t"
"s_add_u32 s57, s58, 0x1000\n\t"
"v_mfma_f32_16x16x32_bf16 v[8:11], a[24:27], v[208:211], v[8:11]\n\t"
"ds_read_b64_tr_b16 a[2:3], v235 offset:18432\n\t"
"v_mfma_f32_16x16x32_bf16 v[12:15], a[28:31], v[208:211], v[12:15]\n\t"
"buffer_load_dwordx4 v[208:211], v240, s[40:43], s56 offen offset:0 sc1\n\t"
"v_mfma_f32_16x16x32_bf16 v[16:19], a[16:19], v[212:215], v[16:19]\n\t"
"ds_read_b64_tr_b16 a[4:5], v236 offset:16384\n\t"
"v_mfma_f32_16x16x32_bf16 v[20:23], a[20:23], v[212:215], v[20:23]\n\t"
"v_mfma_f32_16x16x32_bf16 v[24:27], a[24:27], v[212:215], v[24:27]\n\t"
"v_mfma_f32_16x16x32_bf16 v[28:31], a[28:31], v[212:215], v[28:31]\n\t"
"buffer_load_dwordx4 v[212:215], v240, s[40:43], s56 offen offset:1024 sc1\n\t"
"v_mfma_f32_16x16x32_bf16 v[32:35], a[16:19], v[216:219], v[32:35]\n\t"
"ds_read_b64_tr_b16 a[6:7], v236 offset:18432\n\t"
"v_mfma_f32_16x16x32_bf16 v[36:39], a[20:23], v[216:219], v[36:39]\n\t"
"ds_read_b64_tr_b16 a[8:9], v235 offset:16896\n\t"
"v_mfma_f32_16x16x32_bf16 v[40:43], a[24:27], v[216:219], v[40:43]\n\t"
"v_mfma_f32_16x16x32_bf16 v[44:47], a[28:31], v[216:219], v[44:47]\n\t"
"buffer_load_dwordx4 v[216:219], v240, s[40:43], s56 offen offset:2048 sc1\n\t"
"v_mfma_f32_16x16x32_bf16 v[48:51], a[16:19], v[220:223], v[48:51]\n\t"
"ds_read_b64_tr_b16 a[10:11], v235 offset:18944\n\t"
"v_mfma_f32_16x16x32_bf16 v[52:55], a[20:23], v[220:223], v[52:55]\n\t"
"v_mfma_f32_16x16x32_bf16 v[56:59], a[24:27], v[220:223], v[56:59]\n\t"
"ds_read_b64_tr_b16 a[12:13], v236 offset:16896\n\t"
"v_mfma_f32_16x16x32_bf16 v[60:63], a[28:31], v[220:223], v[60:63]\n\t"
"buffer_load_dwordx4 v[220:223], v240, s[40:43], s56 offen offset:3072 sc1\n\t"
"v_mfma_f32_16x16x32_bf16 v[64:67], a[16:19], v[224:227], v[64:67]\n\t"
"s_add_u32 s58, s58, 6144\n\t"
"ds_read_b64_tr_b16 a[14:15], v236 offset:18944\n\t"
"v_mfma_f32_16x16x32_bf16 v[68:71], a[20:23], v[224:227], v[68:71]\n\t"
"s_sub_u32 s59, s58, 196608\n\t"
"v_mfma_f32_16x16x32_bf16 v[72:75], a[24:27], v[224:227], v[72:75]\n\t"
"s_cselect_b32 s58, s58, s59\n\t"
"v_mfma_f32_16x16x32_bf16 v[76:79], a[28:31], v[224:227], v[76:79]\n\t"
"buffer_load_dwordx4 v[224:227], v240, s[40:43], s57 offen offset:0 sc1\n\t"
"v_mfma_f32_16x16x32_bf16 v[80:83], a[16:19], v[228:231], v[80:83]\n\t"
"v_mfma_f32_16x16x32_bf16 v[84:87], a[20:23], v[228:231], v[84:87]\n\t"
"v_mfma_f32_16x16x32_bf16 v[88:91], a[24:27], v[228:231], v[88:91]\n\t"
"v_mfma_f32_16x16x32_bf16 v[92:95], a[28:31], v[228:231], v[92:95]\n\t"
"buffer_load_dwordx4 v[228:231], v240, s[40:43], s57 offen offset:1024 sc1\n\t"
"s_waitcnt vmcnt(12) lgkmcnt(0)\n\t"
"v_mfma_f32_16x16x32_bf16 v[0:3], a[32:35], v[160:163], v[0:3]\n\t"
"s_mov_b32 s56, s58\n\t"
"ds_read_b64_tr_b16 a[16:17], v248 offset:32768\n\t"
"v_mfma_f32_16x16x32_bf16 v[4:7], a[36:39], v[160:163], v[4:7]\n\t"
"s_add_u32 s57, s58, 0x1000\n\t"
"v_mfma_f32_16x16x32_bf16 v[8:11], a[40:43], v[160:163], v[8:11]\n\t"
"ds_read_b64_tr_b16 a[18:19], v248 offset:34816\n\t"
"v_mfma_f32_16x16x32_bf16 v[12:15], a[44:47], v[160:163], v[12:15]\n\t"
"buffer_load_dwordx4 v[160:163], v240, s[40:43], s56 offen offset:0 sc1\n\t"
"v_mfma_f32_16x16x32_bf16 v[16:19], a[32:35], v[164:167], v[16:19]\n\t"
"ds_read_b64_tr_b16 a[20:21], v249 offset:32768\n\t"
"v_mfma_f32_16x16x32_bf16 v[20:23], a[36:39], v[164:167], v[20:23]\n\t"
"v_mfma_f32_16x16x32_bf16 v[24:27], a[40:43], v[164:167], v[24:27]\n\t"
"v_mfma_f32_16x16x32_bf16 v[28:31], a[44:47], v[164:167], v[28:31]\n\t"
"buffer_load_dwordx4 v[164:167], v240, s[40:43], s56 offen offset:1024 sc1\n\t"
"v_mfma_f32_16x16x32_bf16 v[32:35], a[32:35], v[168:171], v[32:35]\n\t"
"ds_read_b64_tr_b16 a[22:23], v249 offset:34816\n\t"
"v_mfma_f32_16x16x32_bf16 v[36:39], a[36:39], v[168:171], v[36:39]\n\t"
"ds_read_b64_tr_b16 a[24:25], v248 offset:33280\n\t"
"v_mfma_f32_16x16x32_bf16 v[40:43], a[40:43], v[168:171], v[40:43]\n\t"
"v_mfma_f32_16x16x32_bf16 v[44:47], a[44:47], v[168:171], v[44:47]\n\t"
"buffer_load_dwordx4 v[168:171], v240, s[40:43], s56 offen offset:2048 sc1\n\t"
"v_mfma_f32_16x16x32_bf16 v[48:51], a[32:35], v[172:175], v[48:51]\n\t"
"ds_read_b64_tr_b16 a[26:27], v248 offset:35328\n\t"
"v_mfma_f32_16x16x32_bf16 v[52:55], a[36:39], v[172:175], v[52:55]\n\t"
"v_mfma_f32_16x16x32_bf16 v[56:59], a[40:43], v[172:175], v[56:59]\n\t"
"ds_read_b64_tr_b16 a[28:29], v249 offset:33280\n\t"
"v_mfma_f32_16x16x32_bf16 v[60:63], a[44:47], v[172:175], v[60:63]\n\t"
"buffer_load_dwordx4 v[172:175], v240, s[40:43], s56 offen offset:3072 sc1\n\t"
"v_mfma_f32_16x16x32_bf16 v[64:67], a[32:35], v[176:179], v[64:67]\n\t"
"s_add_u32 s58, s58, 6144\n\t"
"ds_read_b64_tr_b16 a[30:31], v249 offset:35328\n\t"
"v_mfma_f32_16x16x32_bf16 v[68:71], a[36:39], v[176:179], v[68:71]\n\t"
"s_sub_u32 s59, s58, 196608\n\t"
"v_mfma_f32_16x16x32_bf16 v[72:75], a[40:43], v[176:179], v[72:75]\n\t"
"s_cselect_b32 s58, s58, s59\n\t"
"v_mfma_f32_16x16x32_bf16 v[76:79], a[44:47], v[176:179], v[76:79]\n\t"
"buffer_load_dwordx4 v[176:179], v240, s[40:43], s57 offen offset:0 sc1\n\t"
"v_mfma_f32_16x16x32_bf16 v[80:83], a[32:35], v[180:183], v[80:83]\n\t"
"v_mfma_f32_16x16x32_bf16 v[84:87], a[36:39], v[180:183], v[84:87]\n\t"
"v_mfma_f32_16x16x32_bf16 v[88:91], a[40:43], v[180:183], v[88:91]\n\t"
"v_mfma_f32_16x16x32_bf16 v[92:95], a[44:47], v[180:183], v[92:95]\n\t"
"buffer_load_dwordx4 v[180:183], v240, s[40:43], s57 offen offset:1024 sc1\n\t"
"s_waitcnt vmcnt(12) lgkmcnt(0)\n\t"
"v_mfma_f32_16x16x32_bf16 v[0:3], a[48:51], v[184:187], v[0:3]\n\t"
"s_mov_b32 s56, s58\n\t"
"ds_read_b64_tr_b16 a[32:33], v246 offset:49152\n\t"
"v_mfma_f32_16x16x32_bf16 v[4:7], a[52:55], v[184:187], v[4:7]\n\t"
"s_add_u32 s57, s58, 0x1000\n\t"
"v_mfma_f32_16x16x32_bf16 v[8:11], a[56:59], v[184:187], v[8:11]\n\t"
"ds_read_b64_tr_b16 a[34:35], v246 offset:51200\n\t"
"v_mfma_f32_16x16x32_bf16 v[12:15], a[60:63], v[184:187], v[12:15]\n\t"
"buffer_load_dwordx4 v[184:187], v240, s[40:43], s56 offen offset:0 sc1\n\t"
"v_mfma_f32_16x16x32_bf16 v[16:19], a[48:51], v[188:191], v[16:19]\n\t"
"ds_read_b64_tr_b16 a[36:37], v247 offset:49152\n\t"
"v_mfma_f32_16x16x32_bf16 v[20:23], a[52:55], v[188:191], v[20:23]\n\t"
"v_mfma_f32_16x16x32_bf16 v[24:27], a[56:59], v[188:191], v[24:27]\n\t"
"v_mfma_f32_16x16x32_bf16 v[28:31], a[60:63], v[188:191], v[28:31]\n\t"
"buffer_load_dwordx4 v[188:191], v240, s[40:43], s56 offen offset:1024 sc1\n\t"
"v_mfma_f32_16x16x32_bf16 v[32:35], a[48:51], v[192:195], v[32:35]\n\t"
"ds_read_b64_tr_b16 a[38:39], v247 offset:51200\n\t"
"v_mfma_f32_16x16x32_bf16 v[36:39], a[52:55], v[192:195], v[36:39]\n\t"
"ds_read_b64_tr_b16 a[40:41], v246 offset:49664\n\t"
"v_mfma_f32_16x16x32_bf16 v[40:43], a[56:59], v[192:195], v[40:43]\n\t"
"v_mfma_f32_16x16x32_bf16 v[44:47], a[60:63], v[192:195], v[44:47]\n\t"
"buffer_load_dwordx4 v[192:195], v240, s[40:43], s56 offen offset:2048 sc1\n\t"
"v_mfma_f32_16x16x32_bf16 v[48:51], a[48:51], v[196:199], v[48:51]\n\t"
"ds_read_b64_tr_b16 a[42:43], v246 offset:51712\n\t"
"v_mfma_f32_16x16x32_bf16 v[52:55], a[52:55], v[196:199], v[52:55]\n\t"
"v_mfma_f32_16x16x32_bf16 v[56:59], a[56:59], v[196:199], v[56:59]\n\t"
"ds_read_b64_tr_b16 a[44:45], v247 offset:49664\n\t"
"v_mfma_f32_16x16x32_bf16 v[60:63], a[60:63], v[196:199], v[60:63]\n\t"
"buffer_load_dwordx4 v[196:199], v240, s[40:43], s56 offen offset:3072 sc1\n\t"
"v_mfma_f32_16x16x32_bf16 v[64:67], a[48:51], v[200:203], v[64:67]\n\t"
"s_add_u32 s58, s58, 6144\n\t"
"ds_read_b64_tr_b16 a[46:47], v247 offset:51712\n\t"
"v_mfma_f32_16x16x32_bf16 v[68:71], a[52:55], v[200:203], v[68:71]\n\t"
"s_sub_u32 s59, s58, 196608\n\t"
"v_mfma_f32_16x16x32_bf16 v[72:75], a[56:59], v[200:203], v[72:75]\n\t"
"s_cselect_b32 s58, s58, s59\n\t"
"v_mfma_f32_16x16x32_bf16 v[76:79], a[60:63], v[200:203], v[76:79]\n\t"
"buffer_load_dwordx4 v[200:203], v240, s[40:43], s57 offen offset:0 sc1\n\t"
"v_mfma_f32_16x16x32_bf16 v[80:83], a[48:51], v[204:207], v[80:83]\n\t"
"v_mfma_f32_16x16x32_bf16 v[84:87], a[52:55], v[204:207], v[84:87]\n\t"
"v_mfma_f32_16x16x32_bf16 v[88:91], a[56:59], v[204:207], v[88:91]\n\t"
"v_mfma_f32_16x16x32_bf16 v[92:95], a[60:63], v[204:207], v[92:95]\n\t"
"buffer_load_dwordx4 v[204:207], v240, s[40:43], s57 offen offset:1024 sc1\n\t"
"s_waitcnt vmcnt(12) lgkmcnt(0)\n\t"
"v_mfma_f32_16x16x32_bf16 v[0:3], a[64:67], v[208:211], v[0:3]\n\t"
"s_mov_b32 s56, s58\n\t"
"ds_read_b64_tr_b16 a[48:49], v248 offset:49152\n\t"
"v_mfma_f32_16x16x32_bf16 v[4:7], a[68:71], v[208:211], v[4:7]\n\t"
"s_add_u32 s57, s58, 0x1000\n\t"
"v_mfma_f32_16x16x32_bf16 v[8:11], a[72:75], v[208:211], v[8:11]\n\t"
"ds_read_b64_tr_b16 a[50:51], v248 offset:51200\n\t"
"v_mfma_f32_16x16x32_bf16 v[12:15], a[76:79], v[208:211], v[12:15]\n\t"
"buffer_load_dwordx4 v[208:211], v240, s[40:43], s56 offen offset:0 sc1\n\t"
"v_mfma_f32_16x16x32_bf16 v[16:19], a[64:67], v[212:215], v[16:19]\n\t"
"ds_read_b64_tr_b16 a[52:53], v249 offset:49152\n\t"
"v_mfma_f32_16x16x32_bf16 v[20:23], a[68:71], v[212:215], v[20:23]\n\t"
"v_mfma_f32_16x16x32_bf16 v[24:27], a[72:75], v[212:215], v[24:27]\n\t"
"v_mfma_f32_16x16x32_bf16 v[28:31], a[76:79], v[212:215], v[28:31]\n\t"
"buffer_load_dwordx4 v[212:215], v240, s[40:43], s56 offen offset:1024 sc1\n\t"
"v_mfma_f32_16x16x32_bf16 v[32:35], a[64:67], v[216:219], v[32:35]\n\t"
"ds_read_b64_tr_b16 a[54:55], v249 offset:51200\n\t"
"v_mfma_f32_16x16x32_bf16 v[36:39], a[68:71], v[216:219], v[36:39]\n\t"
"ds_read_b64_tr_b16 a[56:57], v248 offset:49664\n\t"
"v_mfma_f32_16x16x32_bf16 v[40:43], a[72:75], v[216:219], v[40:43]\n\t"
"v_mfma_f32_16x16x32_bf16 v[44:47], a[76:79], v[216:219], v[44:47]\n\t"
"buffer_load_dwordx4 v[216:219], v240, s[40:43], s56 offen offset:2048 sc1\n\t"
"v_mfma_f32_16x16x32_bf16 v[48:51], a[64:67], v[220:223], v[48:51]\n\t"
"ds_read_b64_tr_b16 a[58:59], v248 offset:51712\n\t"
"v_mfma_f32_16x16x32_bf16 v[52:55], a[68:71], v[220:223], v[52:55]\n\t"
"v_mfma_f32_16x16x32_bf16 v[56:59], a[72:75], v[220:223], v[56:59]\n\t"
"ds_read_b64_tr_b16 a[60:61], v249 offset:49664\n\t"
"v_mfma_f32_16x16x32_bf16 v[60:63], a[76:79], v[220:223], v[60:63]\n\t"
"buffer_load_dwordx4 v[220:223], v240, s[40:43], s56 offen offset:3072 sc1\n\t"
"v_mfma_f32_16x16x32_bf16 v[64:67], a[64:67], v[224:227], v[64:67]\n\t"
"s_add_u32 s58, s58, 6144\n\t"
"ds_read_b64_tr_b16 a[62:63], v249 offset:51712\n\t"
"v_mfma_f32_16x16x32_bf16 v[68:71], a[68:71], v[224:227], v[68:71]\n\t"
"s_sub_u32 s59, s58, 196608\n\t"
"v_mfma_f32_16x16x32_bf16 v[72:75], a[72:75], v[224:227], v[72:75]\n\t"
"s_cselect_b32 s58, s58, s59\n\t"
"v_mfma_f32_16x16x32_bf16 v[76:79], a[76:79], v[224:227], v[76:79]\n\t"
"buffer_load_dwordx4 v[224:227], v240, s[40:43], s57 offen offset:0 sc1\n\t"
"v_mfma_f32_16x16x32_bf16 v[80:83], a[64:67], v[228:231], v[80:83]\n\t"
"v_mfma_f32_16x16x32_bf16 v[84:87], a[68:71], v[228:231], v[84:87]\n\t"
"v_mfma_f32_16x16x32_bf16 v[88:91], a[72:75], v[228:231], v[88:91]\n\t"
"v_mfma_f32_16x16x32_bf16 v[92:95], a[76:79], v[228:231], v[92:95]\n\t"
"buffer_load_dwordx4 v[228:231], v240, s[40:43], s57 offen offset:1024 sc1\n\t"
"s_waitcnt vmcnt(12) lgkmcnt(0)\n\t"
"v_mfma_f32_16x16x32_bf16 v[0:3], a[80:83], v[160:163], v[0:3]\n\t"
"s_mov_b32 s56, s58\n\t"
"ds_read_b64_tr_b16 a[64:65], v246 offset:0\n\t"
"v_mfma_f32_16x16x32_bf16 v[4:7], a[84:87], v[160:163], v[4:7]\n\t"
"s_add_u32 s57, s58, 0x1000\n\t"
"v_mfma_f32_16x16x32_bf16 v[8:11], a[88:91], v[160:163], v[8:11]\n\t"
"ds_read_b64_tr_b16 a[66:67], v246 offset:2048\n\t"
"v_mfma_f32_16x16x32_bf16 v[12:15], a[92:95], v[160:163], v[12:15]\n\t"
"buffer_load_dwordx4 v[160:163], v240, s[40:43], s56 offen offset:0 sc1\n\t"
"v_mfma_f32_16x16x32_bf16 v[16:19], a[80:83], v[164:167], v[16:19]\n\t"
"ds_read_b64_tr_b16 a[68:69], v247 offset:0\n\t"
"v_mfma_f32_16x16x32_bf16 v[20:23], a[84:87], v[164:167], v[20:23]\n\t"
"v_mfma_f32_16x16x32_bf16 v[24:27], a[88:91], v[164:167], v[24:27]\n\t"
"v_mfma_f32_16x16x32_bf16 v[28:31], a[92:95], v[164:167], v[28:31]\n\t"
"buffer_load_dwordx4 v[164:167], v240, s[40:43], s56 offen offset:1024 sc1\n\t"
"v_mfma_f32_16x16x32_bf16 v[32:35], a[80:83], v[168:171], v[32:35]\n\t"
"ds_read_b64_tr_b16 a[70:71], v247 offset:2048\n\t"
"v_mfma_f32_16x16x32_bf16 v[36:39], a[84:87], v[168:171], v[36:39]\n\t"
"ds_read_b64_tr_b16 a[72:73], v246 offset:512\n\t"
"v_mfma_f32_16x16x32_bf16 v[40:43], a[88:91], v[168:171], v[40:43]\n\t"
"v_mfma_f32_16x16x32_bf16 v[44:47], a[92:95], v[168:171], v[44:47]\n\t"
"buffer_load_dwordx4 v[168:171], v240, s[40:43], s56 offen offset:2048 sc1\n\t"
"v_mfma_f32_16x16x32_bf16 v[48:51], a[80:83], v[172:175], v[48:51]\n\t"
"ds_read_b64_tr_b16 a[74:75], v246 offset:2560\n\t"
"v_mfma_f32_16x16x32_bf16 v[52:55], a[84:87], v[172:175], v[52:55]\n\t"
"v_mfma_f32_16x16x32_bf16 v[56:59], a[88:91], v[172:175], v[56:59]\n\t"
"ds_read_b64_tr_b16 a[76:77], v247 offset:512\n\t"
"v_mfma_f32_16x16x32_bf16 v[60:63], a[92:95], v[172:175], v[60:63]\n\t"
"buffer_load_dwordx4 v[172:175], v240, s[40:43], s56 offen offset:3072 sc1\n\t"
"v_mfma_f32_16x16x32_bf16 v[64:67], a[80:83], v[176:179], v[64:67]\n\t"
"s_add_u32 s58, s58, 6144\n\t"
"ds_read_b64_tr_b16 a[78:79], v247 offset:2560\n\t"
"v_mfma_f32_16x16x32_bf16 v[68:71], a[84:87], v[176:179], v[68:71]\n\t"
"s_sub_u32 s59, s58, 196608\n\t"
"v_mfma_f32_16x16x32_bf16 v[72:75], a[88:91], v[176:179], v[72:75]\n\t"
"s_cselect_b32 s58, s58, s59\n\t"
"v_mfma_f32_16x16x32_bf16 v[76:79], a[92:95], v[176:179], v[76:79]\n\t"
"buffer_load_dwordx4 v[176:179], v240, s[40:43], s57 offen offset:0 sc1\n\t"
"v_mfma_f32_16x16x32_bf16 v[80:83], a[80:83], v[180:183], v[80:83]\n\t"
"v_mfma_f32_16x16x32_bf16 v[84:87], a[84:87], v[180:183], v[84:87]\n\t"
"v_mfma_f32_16x16x32_bf16 v[88:91], a[88:91], v[180:183], v[88:91]\n\t"
"v_mfma_f32_16x16x32_bf16 v[92:95], a[92:95], v[180:183], v[92:95]\n\t"
"buffer_load_dwordx4 v[180:183], v240, s[40:43], s57 offen offset:1024 sc1\n\t"
"s_waitcnt vmcnt(12) lgkmcnt(0)\n\t"
"v_mfma_f32_16x16x32_bf16 v[0:3], a[96:99], v[184:187], v[0:3]\n\t"
"s_mov_b32 s56, s58\n\t"
"ds_read_b64_tr_b16 a[80:81], v235 offset:0\n\t"
"v_mfma_f32_16x16x32_bf16 v[4:7], a[100:103], v[184:187], v[4:7]\n\t"
"s_add_u32 s57, s58, 0x1000\n\t"
"v_mfma_f32_16x16x32_bf16 v[8:11], a[104:107], v[184:187], v[8:11]\n\t"
"ds_read_b64_tr_b16 a[82:83], v235 offset:2048\n\t"
"v_mfma_f32_16x16x32_bf16 v[12:15], a[108:111], v[184:187], v[12:15]\n\t"
"buffer_load_dwordx4 v[184:187], v240, s[40:43], s56 offen offset:0 sc1\n\t"
"v_mfma_f32_16x16x32_bf16 v[16:19], a[96:99], v[188:191], v[16:19]\n\t"
"ds_read_b64_tr_b16 a[84:85], v236 offset:0\n\t"
"v_mfma_f32_16x16x32_bf16 v[20:23], a[100:103], v[188:191], v[20:23]\n\t"
"v_mfma_f32_16x16x32_bf16 v[24:27], a[104:107], v[188:191], v[24:27]\n\t"
"v_mfma_f32_16x16x32_bf16 v[28:31], a[108:111], v[188:191], v[28:31]\n\t"
"buffer_load_dwordx4 v[188:191], v240, s[40:43], s56 offen offset:1024 sc1\n\t"
"v_mfma_f32_16x16x32_bf16 v[32:35], a[96:99], v[192:195], v[32:35]\n\t"
"ds_read_b64_tr_b16 a[86:87], v236 offset:2048\n\t"
"v_mfma_f32_16x16x32_bf16 v[36:39], a[100:103], v[192:195], v[36:39]\n\t"
"ds_read_b64_tr_b16 a[88:89], v235 offset:512\n\t"
"v_mfma_f32_16x16x32_bf16 v[40:43], a[104:107], v[192:195], v[40:43]\n\t"
"v_mfma_f32_16x16x32_bf16 v[44:47], a[108:111], v[192:195], v[44:47]\n\t"
"buffer_load_dwordx4 v[192:195], v240, s[40:43], s56 offen offset:2048 sc1\n\t"
"v_mfma_f32_16x16x32_bf16 v[48:51], a[96:99], v[196:199], v[48:51]\n\t"
"ds_read_b64_tr_b16 a[90:91], v235 offset:2560\n\t"
"v_mfma_f32_16x16x32_bf16 v[52:55], a[100:103], v[196:199], v[52:55]\n\t"
"v_mfma_f32_16x16x32_bf16 v[56:59], a[104:107], v[196:199], v[56:59]\n\t"
"ds_read_b64_tr_b16 a[92:93], v236 offset:512\n\t"
"v_mfma_f32_16x16x32_bf16 v[60:63], a[108:111], v[196:199], v[60:63]\n\t"
"buffer_load_dwordx4 v[196:199], v240, s[40:43], s56 offen offset:3072 sc1\n\t"
"v_mfma_f32_16x16x32_bf16 v[64:67], a[96:99], v[200:203], v[64:67]\n\t"
"s_add_u32 s58, s58, 6144\n\t"
"ds_read_b64_tr_b16 a[94:95], v236 offset:2560\n\t"
"v_mfma_f32_16x16x32_bf16 v[68:71], a[100:103], v[200:203], v[68:71]\n\t"
"s_sub_u32 s59, s58, 196608\n\t"
"v_mfma_f32_16x16x32_bf16 v[72:75], a[104:107], v[200:203], v[72:75]\n\t"
"s_cselect_b32 s58, s58, s59\n\t"
"v_mfma_f32_16x16x32_bf16 v[76:79], a[108:111], v[200:203], v[76:79]\n\t"
"buffer_load_dwordx4 v[200:203], v240, s[40:43], s57 offen offset:0 sc1\n\t"
"v_mfma_f32_16x16x32_bf16 v[80:83], a[96:99], v[204:207], v[80:83]\n\t"
"v_mfma_f32_16x16x32_bf16 v[84:87], a[100:103], v[204:207], v[84:87]\n\t"
"v_mfma_f32_16x16x32_bf16 v[88:91], a[104:107], v[204:207], v[88:91]\n\t"
"v_mfma_f32_16x16x32_bf16 v[92:95], a[108:111], v[204:207], v[92:95]\n\t"
"buffer_load_dwordx4 v[204:207], v240, s[40:43], s57 offen offset:1024 sc1\n\t"
"s_waitcnt vmcnt(12) lgkmcnt(0)\n\t"
"v_mfma_f32_16x16x32_bf16 v[0:3], a[112:115], v[208:211], v[0:3]\n\t"
"s_mov_b32 s56, s58\n\t"
"ds_read_b64_tr_b16 a[96:97], v248 offset:0\n\t"
"v_mfma_f32_16x16x32_bf16 v[4:7], a[116:119], v[208:211], v[4:7]\n\t"
"s_add_u32 s57, s58, 0x1000\n\t"
"v_mfma_f32_16x16x32_bf16 v[8:11], a[120:123], v[208:211], v[8:11]\n\t"
"ds_read_b64_tr_b16 a[98:99], v248 offset:2048\n\t"
"v_mfma_f32_16x16x32_bf16 v[12:15], a[124:127], v[208:211], v[12:15]\n\t"
"buffer_load_dwordx4 v[208:211], v240, s[40:43], s56 offen offset:0 sc1\n\t"
"v_mfma_f32_16x16x32_bf16 v[16:19], a[112:115], v[212:215], v[16:19]\n\t"
"ds_read_b64_tr_b16 a[100:101], v249 offset:0\n\t"
"v_mfma_f32_16x16x32_bf16 v[20:23], a[116:119], v[212:215], v[20:23]\n\t"
"v_mfma_f32_16x16x32_bf16 v[24:27], a[120:123], v[212:215], v[24:27]\n\t"
"v_mfma_f32_16x16x32_bf16 v[28:31], a[124:127], v[212:215], v[28:31]\n\t"
"buffer_load_dwordx4 v[212:215], v240, s[40:43], s56 offen offset:1024 sc1\n\t"
"v_mfma_f32_16x16x32_bf16 v[32:35], a[112:115], v[216:219], v[32:35]\n\t"
"ds_read_b64_tr_b16 a[102:103], v249 offset:2048\n\t"
"v_mfma_f32_16x16x32_bf16 v[36:39], a[116:119], v[216:219], v[36:39]\n\t"
"ds_read_b64_tr_b16 a[104:105], v248 offset:512\n\t"
"v_mfma_f32_16x16x32_bf16 v[40:43], a[120:123], v[216:219], v[40:43]\n\t"
"v_mfma_f32_16x16x32_bf16 v[44:47], a[124:127], v[216:219], v[44:47]\n\t"
"buffer_load_dwordx4 v[216:219], v240, s[40:43], s56 offen offset:2048 sc1\n\t"
"v_mfma_f32_16x16x32_bf16 v[48:51], a[112:115], v[220:223], v[48:51]\n\t"
"ds_read_b64_tr_b16 a[106:107], v248 offset:2560\n\t"
"v_mfma_f32_16x16x32_bf16 v[52:55], a[116:119], v[220:223], v[52:55]\n\t"
"v_mfma_f32_16x16x32_bf16 v[56:59], a[120:123], v[220:223], v[56:59]\n\t"
"ds_read_b64_tr_b16 a[108:109], v249 offset:512\n\t"
"v_mfma_f32_16x16x32_bf16 v[60:63], a[124:127], v[220:223], v[60:63]\n\t"
"buffer_load_dwordx4 v[220:223], v240, s[40:43], s56 offen offset:3072 sc1\n\t"
"v_mfma_f32_16x16x32_bf16 v[64:67], a[112:115], v[224:227], v[64:67]\n\t"
"s_add_u32 s58, s58, 6144\n\t"
"ds_read_b64_tr_b16 a[110:111], v249 offset:2560\n\t"
"v_mfma_f32_16x16x32_bf16 v[68:71], a[116:119], v[224:227], v[68:71]\n\t"
"s_sub_u32 s59, s58, 196608\n\t"
"v_mfma_f32_16x16x32_bf16 v[72:75], a[120:123], v[224:227], v[72:75]\n\t"
"s_cselect_b32 s58, s58, s59\n\t"
"v_mfma_f32_16x16x32_bf16 v[76:79], a[124:127], v[224:227], v[76:79]\n\t"
"buffer_load_dwordx4 v[224:227], v240, s[40:43], s57 offen offset:0 sc1\n\t"
"v_mfma_f32_16x16x32_bf16 v[80:83], a[112:115], v[228:231], v[80:83]\n\t"
"v_mfma_f32_16x16x32_bf16 v[84:87], a[116:119], v[228:231], v[84:87]\n\t"
"v_mfma_f32_16x16x32_bf16 v[88:91], a[120:123], v[228:231], v[88:91]\n\t"
"v_mfma_f32_16x16x32_bf16 v[92:95], a[124:127], v[228:231], v[92:95]\n\t"
"buffer_load_dwordx4 v[228:231], v240, s[40:43], s57 offen offset:1024 sc1\n\t"
"s_waitcnt vmcnt(12) lgkmcnt(0)\n\t"
"v_mfma_f32_16x16x32_bf16 v[0:3], a[128:131], v[160:163], v[0:3]\n\t"
"s_mov_b32 s56, s58\n\t"
"ds_read_b64_tr_b16 a[112:113], v246 offset:16384\n\t"
"v_mfma_f32_16x16x32_bf16 v[4:7], a[132:135], v[160:163], v[4:7]\n\t"
"s_add_u32 s57, s58, 0x1000\n\t"
"v_mfma_f32_16x16x32_bf16 v[8:11], a[136:139], v[160:163], v[8:11]\n\t"
"ds_read_b64_tr_b16 a[114:115], v246 offset:18432\n\t"
"v_mfma_f32_16x16x32_bf16 v[12:15], a[140:143], v[160:163], v[12:15]\n\t"
"buffer_load_dwordx4 v[160:163], v240, s[40:43], s56 offen offset:0 sc1\n\t"
"v_mfma_f32_16x16x32_bf16 v[16:19], a[128:131], v[164:167], v[16:19]\n\t"
"ds_read_b64_tr_b16 a[116:117], v247 offset:16384\n\t"
"v_mfma_f32_16x16x32_bf16 v[20:23], a[132:135], v[164:167], v[20:23]\n\t"
"v_mfma_f32_16x16x32_bf16 v[24:27], a[136:139], v[164:167], v[24:27]\n\t"
"v_mfma_f32_16x16x32_bf16 v[28:31], a[140:143], v[164:167], v[28:31]\n\t"
"buffer_load_dwordx4 v[164:167], v240, s[40:43], s56 offen offset:1024 sc1\n\t"
"v_mfma_f32_16x16x32_bf16 v[32:35], a[128:131], v[168:171], v[32:35]\n\t"
"ds_read_b64_tr_b16 a[118:119], v247 offset:18432\n\t"
"v_mfma_f32_16x16x32_bf16 v[36:39], a[132:135], v[168:171], v[36:39]\n\t"
"ds_read_b64_tr_b16 a[120:121], v246 offset:16896\n\t"
"v_mfma_f32_16x16x32_bf16 v[40:43], a[136:139], v[168:171], v[40:43]\n\t"
"v_mfma_f32_16x16x32_bf16 v[44:47], a[140:143], v[168:171], v[44:47]\n\t"
"buffer_load_dwordx4 v[168:171], v240, s[40:43], s56 offen offset:2048 sc1\n\t"
"v_mfma_f32_16x16x32_bf16 v[48:51], a[128:131], v[172:175], v[48:51]\n\t"
"ds_read_b64_tr_b16 a[122:123], v246 offset:18944\n\t"
"v_mfma_f32_16x16x32_bf16 v[52:55], a[132:135], v[172:175], v[52:55]\n\t"
"v_mfma_f32_16x16x32_bf16 v[56:59], a[136:139], v[172:175], v[56:59]\n\t"
"ds_read_b64_tr_b16 a[124:125], v247 offset:16896\n\t"
"v_mfma_f32_16x16x32_bf16 v[60:63], a[140:143], v[172:175], v[60:63]\n\t"
"buffer_load_dwordx4 v[172:175], v240, s[40:43], s56 offen offset:3072 sc1\n\t"
"v_mfma_f32_16x16x32_bf16 v[64:67], a[128:131], v[176:179], v[64:67]\n\t"
"s_add_u32 s58, s58, 6144\n\t"
"ds_read_b64_tr_b16 a[126:127], v247 offset:18944\n\t"
"v_mfma_f32_16x16x32_bf16 v[68:71], a[132:135], v[176:179], v[68:71]\n\t"
"s_sub_u32 s59, s58, 196608\n\t"
"v_mfma_f32_16x16x32_bf16 v[72:75], a[136:139], v[176:179], v[72:75]\n\t"
"s_cselect_b32 s58, s58, s59\n\t"
"v_mfma_f32_16x16x32_bf16 v[76:79], a[140:143], v[176:179], v[76:79]\n\t"
"buffer_load_dwordx4 v[176:179], v240, s[40:43], s57 offen offset:0 sc1\n\t"
"v_mfma_f32_16x16x32_bf16 v[80:83], a[128:131], v[180:183], v[80:83]\n\t"
"v_mfma_f32_16x16x32_bf16 v[84:87], a[132:135], v[180:183], v[84:87]\n\t"
"v_mfma_f32_16x16x32_bf16 v[88:91], a[136:139], v[180:183], v[88:91]\n\t"
"v_mfma_f32_16x16x32_bf16 v[92:95], a[140:143], v[180:183], v[92:95]\n\t"
"buffer_load_dwordx4 v[180:183], v240, s[40:43], s57 offen offset:1024 sc1\n\t"
"s_waitcnt vmcnt(12) lgkmcnt(0)\n\t"
"v_mfma_f32_16x16x32_bf16 v[0:3], a[144:147], v[184:187], v[0:3]\n\t"
"s_mov_b32 s56, s58\n\t"
"v_mfma_f32_16x16x32_bf16 v[4:7], a[148:151], v[184:187], v[4:7]\n\t"
"s_add_u32 s57, s58, 0x1000\n\t"
"v_mfma_f32_16x16x32_bf16 v[8:11], a[152:155], v[184:187], v[8:11]\n\t"
"v_mfma_f32_16x16x32_bf16 v[12:15], a[156:159], v[184:187], v[12:15]\n\t"
"buffer_load_dwordx4 v[184:187], v240, s[40:43], s56 offen offset:0 sc1\n\t"
"v_mfma_f32_16x16x32_bf16 v[16:19], a[144:147], v[188:191], v[16:19]\n\t"
"v_mfma_f32_16x16x32_bf16 v[20:23], a[148:151], v[188:191], v[20:23]\n\t"
"v_mfma_f32_16x16x32_bf16 v[24:27], a[152:155], v[188:191], v[24:27]\n\t"
"v_mfma_f32_16x16x32_bf16 v[28:31], a[156:159], v[188:191], v[28:31]\n\t"
"buffer_load_dwordx4 v[188:191], v240, s[40:43], s56 offen offset:1024 sc1\n\t"
"v_mfma_f32_16x16x32_bf16 v[32:35], a[144:147], v[192:195], v[32:35]\n\t"
"v_mfma_f32_16x16x32_bf16 v[36:39], a[148:151], v[192:195], v[36:39]\n\t"
"v_mfma_f32_16x16x32_bf16 v[40:43], a[152:155], v[192:195], v[40:43]\n\t"
"v_mfma_f32_16x16x32_bf16 v[44:47], a[156:159], v[192:195], v[44:47]\n\t"
"buffer_load_dwordx4 v[192:195], v240, s[40:43], s56 offen offset:2048 sc1\n\t"
"v_mfma_f32_16x16x32_bf16 v[48:51], a[144:147], v[196:199], v[48:51]\n\t"
"v_mfma_f32_16x16x32_bf16 v[52:55], a[148:151], v[196:199], v[52:55]\n\t"
"v_mfma_f32_16x16x32_bf16 v[56:59], a[152:155], v[196:199], v[56:59]\n\t"
"v_mfma_f32_16x16x32_bf16 v[60:63], a[156:159], v[196:199], v[60:63]\n\t"
"buffer_load_dwordx4 v[196:199], v240, s[40:43], s56 offen offset:3072 sc1\n\t"
"v_mfma_f32_16x16x32_bf16 v[64:67], a[144:147], v[200:203], v[64:67]\n\t"
"s_add_u32 s58, s58, 6144\n\t"
"v_mfma_f32_16x16x32_bf16 v[68:71], a[148:151], v[200:203], v[68:71]\n\t"
"s_sub_u32 s59, s58, 196608\n\t"
"v_mfma_f32_16x16x32_bf16 v[72:75], a[152:155], v[200:203], v[72:75]\n\t"
"s_cselect_b32 s58, s58, s59\n\t"
"v_mfma_f32_16x16x32_bf16 v[76:79], a[156:159], v[200:203], v[76:79]\n\t"
"buffer_load_dwordx4 v[200:203], v240, s[40:43], s57 offen offset:0 sc1\n\t"
"v_mfma_f32_16x16x32_bf16 v[80:83], a[144:147], v[204:207], v[80:83]\n\t"
"v_mfma_f32_16x16x32_bf16 v[84:87], a[148:151], v[204:207], v[84:87]\n\t"
"v_mfma_f32_16x16x32_bf16 v[88:91], a[152:155], v[204:207], v[88:91]\n\t"
"v_mfma_f32_16x16x32_bf16 v[92:95], a[156:159], v[204:207], v[92:95]\n\t"
"buffer_load_dwordx4 v[204:207], v240, s[40:43], s57 offen offset:1024 sc1\n\t"
"s_waitcnt vmcnt(12) lgkmcnt(0)\n\t"
"v_mfma_f32_16x16x32_bf16 v[0:3], a[160:163], v[208:211], v[0:3]\n\t"
"s_mov_b32 s56, s58\n\t"
"v_mfma_f32_16x16x32_bf16 v[4:7], a[164:167], v[208:211], v[4:7]\n\t"
"s_add_u32 s57, s58, 0x1000\n\t"
"v_mfma_f32_16x16x32_bf16 v[8:11], a[168:171], v[208:211], v[8:11]\n\t"
"v_mfma_f32_16x16x32_bf16 v[12:15], a[172:175], v[208:211], v[12:15]\n\t"
"buffer_load_dwordx4 v[208:211], v240, s[40:43], s56 offen offset:0 sc1\n\t"
"v_mfma_f32_16x16x32_bf16 v[16:19], a[160:163], v[212:215], v[16:19]\n\t"
"v_mfma_f32_16x16x32_bf16 v[20:23], a[164:167], v[212:215], v[20:23]\n\t"
"v_mfma_f32_16x16x32_bf16 v[24:27], a[168:171], v[212:215], v[24:27]\n\t"
"v_mfma_f32_16x16x32_bf16 v[28:31], a[172:175], v[212:215], v[28:31]\n\t"
"buffer_load_dwordx4 v[212:215], v240, s[40:43], s56 offen offset:1024 sc1\n\t"
"v_mfma_f32_16x16x32_bf16 v[32:35], a[160:163], v[216:219], v[32:35]\n\t"
"v_mfma_f32_16x16x32_bf16 v[36:39], a[164:167], v[216:219], v[36:39]\n\t"
"v_mfma_f32_16x16x32_bf16 v[40:43], a[168:171], v[216:219], v[40:43]\n\t"
"v_mfma_f32_16x16x32_bf16 v[44:47], a[172:175], v[216:219], v[44:47]\n\t"
"buffer_load_dwordx4 v[216:219], v240, s[40:43], s56 offen offset:2048 sc1\n\t"
"v_mfma_f32_16x16x32_bf16 v[48:51], a[160:163], v[220:223], v[48:51]\n\t"
"v_mfma_f32_16x16x32_bf16 v[52:55], a[164:167], v[220:223], v[52:55]\n\t"
"v_mfma_f32_16x16x32_bf16 v[56:59], a[168:171], v[220:223], v[56:59]\n\t"
"v_mfma_f32_16x16x32_bf16 v[60:63], a[172:175], v[220:223], v[60:63]\n\t"
"buffer_load_dwordx4 v[220:223], v240, s[40:43], s56 offen offset:3072 sc1\n\t"
"v_mfma_f32_16x16x32_bf16 v[64:67], a[160:163], v[224:227], v[64:67]\n\t"
"s_add_u32 s58, s58, 6144\n\t"
"v_mfma_f32_16x16x32_bf16 v[68:71], a[164:167], v[224:227], v[68:71]\n\t"
"s_sub_u32 s59, s58, 196608\n\t"
"v_mfma_f32_16x16x32_bf16 v[72:75], a[168:171], v[224:227], v[72:75]\n\t"
"s_cselect_b32 s58, s58, s59\n\t"
"v_mfma_f32_16x16x32_bf16 v[76:79], a[172:175], v[224:227], v[76:79]\n\t"
"buffer_load_dwordx4 v[224:227], v240, s[40:43], s57 offen offset:0 sc1\n\t"
"v_mfma_f32_16x16x32_bf16 v[80:83], a[160:163], v[228:231], v[80:83]\n\t"
"v_mfma_f32_16x16x32_bf16 v[84:87], a[164:167], v[228:231], v[84:87]\n\t"
"v_mfma_f32_16x16x32_bf16 v[88:91], a[168:171], v[228:231], v[88:91]\n\t"
"v_mfma_f32_16x16x32_bf16 v[92:95], a[172:175], v[228:231], v[92:95]\n\t"
"buffer_load_dwordx4 v[228:231], v240, s[40:43], s57 offen offset:1024 sc1\n\t"
"s_waitcnt vmcnt(12) lgkmcnt(0)\n\t"
"v_mfma_f32_16x16x32_bf16 v[0:3], a[176:179], v[160:163], v[0:3]\n\t"
"s_mov_b32 s56, s58\n\t"
"v_mfma_f32_16x16x32_bf16 v[4:7], a[180:183], v[160:163], v[4:7]\n\t"
"s_add_u32 s57, s58, 0x1000\n\t"
"v_mfma_f32_16x16x32_bf16 v[8:11], a[184:187], v[160:163], v[8:11]\n\t"
"v_mfma_f32_16x16x32_bf16 v[12:15], a[188:191], v[160:163], v[12:15]\n\t"
"buffer_load_dwordx4 v[160:163], v240, s[40:43], s56 offen offset:0 sc1\n\t"
"v_mfma_f32_16x16x32_bf16 v[16:19], a[176:179], v[164:167], v[16:19]\n\t"
"v_mfma_f32_16x16x32_bf16 v[20:23], a[180:183], v[164:167], v[20:23]\n\t"
"v_mfma_f32_16x16x32_bf16 v[24:27], a[184:187], v[164:167], v[24:27]\n\t"
"v_mfma_f32_16x16x32_bf16 v[28:31], a[188:191], v[164:167], v[28:31]\n\t"
"buffer_load_dwordx4 v[164:167], v240, s[40:43], s56 offen offset:1024 sc1\n\t"
"v_mfma_f32_16x16x32_bf16 v[32:35], a[176:179], v[168:171], v[32:35]\n\t"
"v_mfma_f32_16x16x32_bf16 v[36:39], a[180:183], v[168:171], v[36:39]\n\t"
"v_mfma_f32_16x16x32_bf16 v[40:43], a[184:187], v[168:171], v[40:43]\n\t"
"v_mfma_f32_16x16x32_bf16 v[44:47], a[188:191], v[168:171], v[44:47]\n\t"
"buffer_load_dwordx4 v[168:171], v240, s[40:43], s56 offen offset:2048 sc1\n\t"
"v_mfma_f32_16x16x32_bf16 v[48:51], a[176:179], v[172:175], v[48:51]\n\t"
"v_mfma_f32_16x16x32_bf16 v[52:55], a[180:183], v[172:175], v[52:55]\n\t"
"v_mfma_f32_16x16x32_bf16 v[56:59], a[184:187], v[172:175], v[56:59]\n\t"
"v_mfma_f32_16x16x32_bf16 v[60:63], a[188:191], v[172:175], v[60:63]\n\t"
"buffer_load_dwordx4 v[172:175], v240, s[40:43], s56 offen offset:3072 sc1\n\t"
"v_mfma_f32_16x16x32_bf16 v[64:67], a[176:179], v[176:179], v[64:67]\n\t"
"s_add_u32 s58, s58, 6144\n\t"
"v_mfma_f32_16x16x32_bf16 v[68:71], a[180:183], v[176:179], v[68:71]\n\t"
"s_sub_u32 s59, s58, 196608\n\t"
"v_mfma_f32_16x16x32_bf16 v[72:75], a[184:187], v[176:179], v[72:75]\n\t"
"s_cselect_b32 s58, s58, s59\n\t"
"v_mfma_f32_16x16x32_bf16 v[76:79], a[188:191], v[176:179], v[76:79]\n\t"
"buffer_load_dwordx4 v[176:179], v240, s[40:43], s57 offen offset:0 sc1\n\t"
"v_mfma_f32_16x16x32_bf16 v[80:83], a[176:179], v[180:183], v[80:83]\n\t"
"v_mfma_f32_16x16x32_bf16 v[84:87], a[180:183], v[180:183], v[84:87]\n\t"
"v_mfma_f32_16x16x32_bf16 v[88:91], a[184:187], v[180:183], v[88:91]\n\t"
"v_mfma_f32_16x16x32_bf16 v[92:95], a[188:191], v[180:183], v[92:95]\n\t"
"buffer_load_dwordx4 v[180:183], v240, s[40:43], s57 offen offset:1024 sc1\n\t"
"s_waitcnt vmcnt(12) lgkmcnt(0)\n\t"
"v_mfma_f32_16x16x32_bf16 v[0:3], a[192:195], v[184:187], v[0:3]\n\t"
"s_mov_b32 s56, s58\n\t"
"v_mfma_f32_16x16x32_bf16 v[4:7], a[196:199], v[184:187], v[4:7]\n\t"
"s_add_u32 s57, s58, 0x1000\n\t"
"v_mfma_f32_16x16x32_bf16 v[8:11], a[200:203], v[184:187], v[8:11]\n\t"
"v_mfma_f32_16x16x32_bf16 v[12:15], a[204:207], v[184:187], v[12:15]\n\t"
"buffer_load_dwordx4 v[184:187], v240, s[40:43], s56 offen offset:0 sc1\n\t"
"v_mfma_f32_16x16x32_bf16 v[16:19], a[192:195], v[188:191], v[16:19]\n\t"
"v_mfma_f32_16x16x32_bf16 v[20:23], a[196:199], v[188:191], v[20:23]\n\t"
"v_mfma_f32_16x16x32_bf16 v[24:27], a[200:203], v[188:191], v[24:27]\n\t"
"v_mfma_f32_16x16x32_bf16 v[28:31], a[204:207], v[188:191], v[28:31]\n\t"
"buffer_load_dwordx4 v[188:191], v240, s[40:43], s56 offen offset:1024 sc1\n\t"
"v_mfma_f32_16x16x32_bf16 v[32:35], a[192:195], v[192:195], v[32:35]\n\t"
"v_mfma_f32_16x16x32_bf16 v[36:39], a[196:199], v[192:195], v[36:39]\n\t"
"v_mfma_f32_16x16x32_bf16 v[40:43], a[200:203], v[192:195], v[40:43]\n\t"
"v_mfma_f32_16x16x32_bf16 v[44:47], a[204:207], v[192:195], v[44:47]\n\t"
"buffer_load_dwordx4 v[192:195], v240, s[40:43], s56 offen offset:2048 sc1\n\t"
"v_mfma_f32_16x16x32_bf16 v[48:51], a[192:195], v[196:199], v[48:51]\n\t"
"v_mfma_f32_16x16x32_bf16 v[52:55], a[196:199], v[196:199], v[52:55]\n\t"
"v_mfma_f32_16x16x32_bf16 v[56:59], a[200:203], v[196:199], v[56:59]\n\t"
"v_mfma_f32_16x16x32_bf16 v[60:63], a[204:207], v[196:199], v[60:63]\n\t"
"buffer_load_dwordx4 v[196:199], v240, s[40:43], s56 offen offset:3072 sc1\n\t"
"v_mfma_f32_16x16x32_bf16 v[64:67], a[192:195], v[200:203], v[64:67]\n\t"
"s_add_u32 s58, s58, 6144\n\t"
"v_mfma_f32_16x16x32_bf16 v[68:71], a[196:199], v[200:203], v[68:71]\n\t"
"s_sub_u32 s59, s58, 196608\n\t"
"v_mfma_f32_16x16x32_bf16 v[72:75], a[200:203], v[200:203], v[72:75]\n\t"
"s_cselect_b32 s58, s58, s59\n\t"
"v_mfma_f32_16x16x32_bf16 v[76:79], a[204:207], v[200:203], v[76:79]\n\t"
"buffer_load_dwordx4 v[200:203], v240, s[40:43], s57 offen offset:0 sc1\n\t"
"v_mfma_f32_16x16x32_bf16 v[80:83], a[192:195], v[204:207], v[80:83]\n\t"
"v_mfma_f32_16x16x32_bf16 v[84:87], a[196:199], v[204:207], v[84:87]\n\t"
"v_mfma_f32_16x16x32_bf16 v[88:91], a[200:203], v[204:207], v[88:91]\n\t"
"v_mfma_f32_16x16x32_bf16 v[92:95], a[204:207], v[204:207], v[92:95]\n\t"
"buffer_load_dwordx4 v[204:207], v240, s[40:43], s57 offen offset:1024 sc1\n\t"
"s_waitcnt vmcnt(12) lgkmcnt(0)\n\t"
"v_mfma_f32_16x16x32_bf16 v[0:3], a[208:211], v[208:211], v[0:3]\n\t"
"s_mov_b32 s56, s58\n\t"
"v_mfma_f32_16x16x32_bf16 v[4:7], a[212:215], v[208:211], v[4:7]\n\t"
"s_add_u32 s57, s58, 0x1000\n\t"
"v_mfma_f32_16x16x32_bf16 v[8:11], a[216:219], v[208:211], v[8:11]\n\t"
"v_mfma_f32_16x16x32_bf16 v[12:15], a[220:223], v[208:211], v[12:15]\n\t"
"buffer_load_dwordx4 v[208:211], v240, s[40:43], s56 offen offset:0 sc1\n\t"
"v_mfma_f32_16x16x32_bf16 v[16:19], a[208:211], v[212:215], v[16:19]\n\t"
"v_mfma_f32_16x16x32_bf16 v[20:23], a[212:215], v[212:215], v[20:23]\n\t"
"v_mfma_f32_16x16x32_bf16 v[24:27], a[216:219], v[212:215], v[24:27]\n\t"
"v_mfma_f32_16x16x32_bf16 v[28:31], a[220:223], v[212:215], v[28:31]\n\t"
"buffer_load_dwordx4 v[212:215], v240, s[40:43], s56 offen offset:1024 sc1\n\t"
"v_mfma_f32_16x16x32_bf16 v[32:35], a[208:211], v[216:219], v[32:35]\n\t"
"v_mfma_f32_16x16x32_bf16 v[36:39], a[212:215], v[216:219], v[36:39]\n\t"
"v_mfma_f32_16x16x32_bf16 v[40:43], a[216:219], v[216:219], v[40:43]\n\t"
"v_mfma_f32_16x16x32_bf16 v[44:47], a[220:223], v[216:219], v[44:47]\n\t"
"buffer_load_dwordx4 v[216:219], v240, s[40:43], s56 offen offset:2048 sc1\n\t"
"v_mfma_f32_16x16x32_bf16 v[48:51], a[208:211], v[220:223], v[48:51]\n\t"
"v_mfma_f32_16x16x32_bf16 v[52:55], a[212:215], v[220:223], v[52:55]\n\t"
"v_mfma_f32_16x16x32_bf16 v[56:59], a[216:219], v[220:223], v[56:59]\n\t"
"v_mfma_f32_16x16x32_bf16 v[60:63], a[220:223], v[220:223], v[60:63]\n\t"
"buffer_load_dwordx4 v[220:223], v240, s[40:43], s56 offen offset:3072 sc1\n\t"
"v_mfma_f32_16x16x32_bf16 v[64:67], a[208:211], v[224:227], v[64:67]\n\t"
"s_add_u32 s58, s58, 6144\n\t"
"v_mfma_f32_16x16x32_bf16 v[68:71], a[212:215], v[224:227], v[68:71]\n\t"
"s_sub_u32 s59, s58, 196608\n\t"
"v_mfma_f32_16x16x32_bf16 v[72:75], a[216:219], v[224:227], v[72:75]\n\t"
"s_cselect_b32 s58, s58, s59\n\t"
"v_mfma_f32_16x16x32_bf16 v[76:79], a[220:223], v[224:227], v[76:79]\n\t"
"buffer_load_dwordx4 v[224:227], v240, s[40:43], s57 offen offset:0 sc1\n\t"
"v_mfma_f32_16x16x32_bf16 v[80:83], a[208:211], v[228:231], v[80:83]\n\t"
"v_mfma_f32_16x16x32_bf16 v[84:87], a[212:215], v[228:231], v[84:87]\n\t"
"v_mfma_f32_16x16x32_bf16 v[88:91], a[216:219], v[228:231], v[88:91]\n\t"
"v_mfma_f32_16x16x32_bf16 v[92:95], a[220:223], v[228:231], v[92:95]\n\t"
"buffer_load_dwordx4 v[228:231], v240, s[40:43], s57 offen offset:1024 sc1\n\t"
"s_waitcnt vmcnt(12) lgkmcnt(0)\n\t"
"v_mfma_f32_16x16x32_bf16 v[0:3], a[224:227], v[160:163], v[0:3]\n\t"
"s_mov_b32 s56, s58\n\t"
"v_mfma_f32_16x16x32_bf16 v[4:7], a[228:231], v[160:163], v[4:7]\n\t"
"s_add_u32 s57, s58, 0x1000\n\t"
"v_mfma_f32_16x16x32_bf16 v[8:11], a[232:235], v[160:163], v[8:11]\n\t"
"v_mfma_f32_16x16x32_bf16 v[12:15], a[236:239], v[160:163], v[12:15]\n\t"
"buffer_load_dwordx4 v[160:163], v240, s[40:43], s56 offen offset:0 sc1\n\t"
"v_mfma_f32_16x16x32_bf16 v[16:19], a[224:227], v[164:167], v[16:19]\n\t"
"v_mfma_f32_16x16x32_bf16 v[20:23], a[228:231], v[164:167], v[20:23]\n\t"
"v_mfma_f32_16x16x32_bf16 v[24:27], a[232:235], v[164:167], v[24:27]\n\t"
"v_mfma_f32_16x16x32_bf16 v[28:31], a[236:239], v[164:167], v[28:31]\n\t"
"buffer_load_dwordx4 v[164:167], v240, s[40:43], s56 offen offset:1024 sc1\n\t"
"v_mfma_f32_16x16x32_bf16 v[32:35], a[224:227], v[168:171], v[32:35]\n\t"
"v_mfma_f32_16x16x32_bf16 v[36:39], a[228:231], v[168:171], v[36:39]\n\t"
"v_mfma_f32_16x16x32_bf16 v[40:43], a[232:235], v[168:171], v[40:43]\n\t"
"v_mfma_f32_16x16x32_bf16 v[44:47], a[236:239], v[168:171], v[44:47]\n\t"
"buffer_load_dwordx4 v[168:171], v240, s[40:43], s56 offen offset:2048 sc1\n\t"
"v_mfma_f32_16x16x32_bf16 v[48:51], a[224:227], v[172:175], v[48:51]\n\t"
"v_mfma_f32_16x16x32_bf16 v[52:55], a[228:231], v[172:175], v[52:55]\n\t"
"v_mfma_f32_16x16x32_bf16 v[56:59], a[232:235], v[172:175], v[56:59]\n\t"
"v_mfma_f32_16x16x32_bf16 v[60:63], a[236:239], v[172:175], v[60:63]\n\t"
"buffer_load_dwordx4 v[172:175], v240, s[40:43], s56 offen offset:3072 sc1\n\t"
"v_mfma_f32_16x16x32_bf16 v[64:67], a[224:227], v[176:179], v[64:67]\n\t"
"s_add_u32 s58, s58, 6144\n\t"
"v_mfma_f32_16x16x32_bf16 v[68:71], a[228:231], v[176:179], v[68:71]\n\t"
"s_sub_u32 s59, s58, 196608\n\t"
"v_mfma_f32_16x16x32_bf16 v[72:75], a[232:235], v[176:179], v[72:75]\n\t"
"s_cselect_b32 s58, s58, s59\n\t"
"v_mfma_f32_16x16x32_bf16 v[76:79], a[236:239], v[176:179], v[76:79]\n\t"
"buffer_load_dwordx4 v[176:179], v240, s[40:43], s57 offen offset:0 sc1\n\t"
"v_mfma_f32_16x16x32_bf16 v[80:83], a[224:227], v[180:183], v[80:83]\n\t"
"v_mfma_f32_16x16x32_bf16 v[84:87], a[228:231], v[180:183], v[84:87]\n\t"
"v_mfma_f32_16x16x32_bf16 v[88:91], a[232:235], v[180:183], v[88:91]\n\t"
"v_mfma_f32_16x16x32_bf16 v[92:95], a[236:239], v[180:183], v[92:95]\n\t"
"buffer_load_dwordx4 v[180:183], v240, s[40:43], s57 offen offset:1024 sc1\n\t"
"s_waitcnt vmcnt(12) lgkmcnt(0)\n\t"
"v_mfma_f32_16x16x32_bf16 v[0:3], a[240:243], v[184:187], v[0:3]\n\t"
"s_mov_b32 s56, s58\n\t"
"v_mfma_f32_16x16x32_bf16 v[4:7], a[244:247], v[184:187], v[4:7]\n\t"
"s_add_u32 s57, s58, 0x1000\n\t"
"v_mfma_f32_16x16x32_bf16 v[8:11], a[248:251], v[184:187], v[8:11]\n\t"
"v_mfma_f32_16x16x32_bf16 v[12:15], a[252:255], v[184:187], v[12:15]\n\t"
"buffer_load_dwordx4 v[184:187], v240, s[40:43], s56 offen offset:0 sc1\n\t"
"v_mfma_f32_16x16x32_bf16 v[16:19], a[240:243], v[188:191], v[16:19]\n\t"
"v_mfma_f32_16x16x32_bf16 v[20:23], a[244:247], v[188:191], v[20:23]\n\t"
"v_mfma_f32_16x16x32_bf16 v[24:27], a[248:251], v[188:191], v[24:27]\n\t"
"v_mfma_f32_16x16x32_bf16 v[28:31], a[252:255], v[188:191], v[28:31]\n\t"
"buffer_load_dwordx4 v[188:191], v240, s[40:43], s56 offen offset:1024 sc1\n\t"
"v_mfma_f32_16x16x32_bf16 v[32:35], a[240:243], v[192:195], v[32:35]\n\t"
"v_mfma_f32_16x16x32_bf16 v[36:39], a[244:247], v[192:195], v[36:39]\n\t"
"v_mfma_f32_16x16x32_bf16 v[40:43], a[248:251], v[192:195], v[40:43]\n\t"
"v_mfma_f32_16x16x32_bf16 v[44:47], a[252:255], v[192:195], v[44:47]\n\t"
"buffer_load_dwordx4 v[192:195], v240, s[40:43], s56 offen offset:2048 sc1\n\t"
"v_mfma_f32_16x16x32_bf16 v[48:51], a[240:243], v[196:199], v[48:51]\n\t"
"v_mfma_f32_16x16x32_bf16 v[52:55], a[244:247], v[196:199], v[52:55]\n\t"
"v_mfma_f32_16x16x32_bf16 v[56:59], a[248:251], v[196:199], v[56:59]\n\t"
"v_mfma_f32_16x16x32_bf16 v[60:63], a[252:255], v[196:199], v[60:63]\n\t"
"buffer_load_dwordx4 v[196:199], v240, s[40:43], s56 offen offset:3072 sc1\n\t"
"v_mfma_f32_16x16x32_bf16 v[64:67], a[240:243], v[200:203], v[64:67]\n\t"
"s_add_u32 s58, s58, 6144\n\t"
"v_mfma_f32_16x16x32_bf16 v[68:71], a[244:247], v[200:203], v[68:71]\n\t"
"s_sub_u32 s59, s58, 196608\n\t"
"v_mfma_f32_16x16x32_bf16 v[72:75], a[248:251], v[200:203], v[72:75]\n\t"
"s_cselect_b32 s58, s58, s59\n\t"
"v_mfma_f32_16x16x32_bf16 v[76:79], a[252:255], v[200:203], v[76:79]\n\t"
"buffer_load_dwordx4 v[200:203], v240, s[40:43], s57 offen offset:0 sc1\n\t"
"v_mfma_f32_16x16x32_bf16 v[80:83], a[240:243], v[204:207], v[80:83]\n\t"
"v_mfma_f32_16x16x32_bf16 v[84:87], a[244:247], v[204:207], v[84:87]\n\t"
"v_mfma_f32_16x16x32_bf16 v[88:91], a[248:251], v[204:207], v[88:91]\n\t"
"v_mfma_f32_16x16x32_bf16 v[92:95], a[252:255], v[204:207], v[92:95]\n\t"
"buffer_load_dwordx4 v[204:207], v240, s[40:43], s57 offen offset:1024 sc1\n\t"
"s_waitcnt vmcnt(12) lgkmcnt(0)\n\t"
"v_mfma_f32_16x16x32_bf16 v[0:3], v[96:99], v[208:211], v[0:3]\n\t"
"s_mov_b32 s56, s58\n\t"
"v_mfma_f32_16x16x32_bf16 v[4:7], v[100:103], v[208:211], v[4:7]\n\t"
"s_add_u32 s57, s58, 0x1000\n\t"
"v_mfma_f32_16x16x32_bf16 v[8:11], v[104:107], v[208:211], v[8:11]\n\t"
"v_mfma_f32_16x16x32_bf16 v[12:15], v[108:111], v[208:211], v[12:15]\n\t"
"buffer_load_dwordx4 v[208:211], v240, s[40:43], s56 offen offset:0 sc1\n\t"
"v_mfma_f32_16x16x32_bf16 v[16:19], v[96:99], v[212:215], v[16:19]\n\t"
"v_mfma_f32_16x16x32_bf16 v[20:23], v[100:103], v[212:215], v[20:23]\n\t"
"v_mfma_f32_16x16x32_bf16 v[24:27], v[104:107], v[212:215], v[24:27]\n\t"
"v_mfma_f32_16x16x32_bf16 v[28:31], v[108:111], v[212:215], v[28:31]\n\t"
"buffer_load_dwordx4 v[212:215], v240, s[40:43], s56 offen offset:1024 sc1\n\t"
"v_mfma_f32_16x16x32_bf16 v[32:35], v[96:99], v[216:219], v[32:35]\n\t"
"v_mfma_f32_16x16x32_bf16 v[36:39], v[100:103], v[216:219], v[36:39]\n\t"
"v_mfma_f32_16x16x32_bf16 v[40:43], v[104:107], v[216:219], v[40:43]\n\t"
"v_mfma_f32_16x16x32_bf16 v[44:47], v[108:111], v[216:219], v[44:47]\n\t"
"buffer_load_dwordx4 v[216:219], v240, s[40:43], s56 offen offset:2048 sc1\n\t"
"v_mfma_f32_16x16x32_bf16 v[48:51], v[96:99], v[220:223], v[48:51]\n\t"
"v_mfma_f32_16x16x32_bf16 v[52:55], v[100:103], v[220:223], v[52:55]\n\t"
"v_mfma_f32_16x16x32_bf16 v[56:59], v[104:107], v[220:223], v[56:59]\n\t"
"v_mfma_f32_16x16x32_bf16 v[60:63], v[108:111], v[220:223], v[60:63]\n\t"
"buffer_load_dwordx4 v[220:223], v240, s[40:43], s56 offen offset:3072 sc1\n\t"
"v_mfma_f32_16x16x32_bf16 v[64:67], v[96:99], v[224:227], v[64:67]\n\t"
"s_add_u32 s58, s58, 6144\n\t"
"v_mfma_f32_16x16x32_bf16 v[68:71], v[100:103], v[224:227], v[68:71]\n\t"
"s_sub_u32 s59, s58, 196608\n\t"
"v_mfma_f32_16x16x32_bf16 v[72:75], v[104:107], v[224:227], v[72:75]\n\t"
"s_cselect_b32 s58, s58, s59\n\t"
"v_mfma_f32_16x16x32_bf16 v[76:79], v[108:111], v[224:227], v[76:79]\n\t"
"buffer_load_dwordx4 v[224:227], v240, s[40:43], s57 offen offset:0 sc1\n\t"
"v_mfma_f32_16x16x32_bf16 v[80:83], v[96:99], v[228:231], v[80:83]\n\t"
"v_mfma_f32_16x16x32_bf16 v[84:87], v[100:103], v[228:231], v[84:87]\n\t"
"v_mfma_f32_16x16x32_bf16 v[88:91], v[104:107], v[228:231], v[88:91]\n\t"
"v_mfma_f32_16x16x32_bf16 v[92:95], v[108:111], v[228:231], v[92:95]\n\t"
"buffer_load_dwordx4 v[228:231], v240, s[40:43], s57 offen offset:1024 sc1\n\t"
"s_waitcnt vmcnt(12) lgkmcnt(0)\n\t"
"v_mfma_f32_16x16x32_bf16 v[0:3], v[112:115], v[160:163], v[0:3]\n\t"
"s_mov_b32 s56, s58\n\t"
"v_mfma_f32_16x16x32_bf16 v[4:7], v[116:119], v[160:163], v[4:7]\n\t"
"s_add_u32 s57, s58, 0x1000\n\t"
"v_mfma_f32_16x16x32_bf16 v[8:11], v[120:123], v[160:163], v[8:11]\n\t"
"v_mfma_f32_16x16x32_bf16 v[12:15], v[124:127], v[160:163], v[12:15]\n\t"
"buffer_load_dwordx4 v[160:163], v240, s[40:43], s56 offen offset:0 sc1\n\t"
"v_mfma_f32_16x16x32_bf16 v[16:19], v[112:115], v[164:167], v[16:19]\n\t"
"v_mfma_f32_16x16x32_bf16 v[20:23], v[116:119], v[164:167], v[20:23]\n\t"
"v_mfma_f32_16x16x32_bf16 v[24:27], v[120:123], v[164:167], v[24:27]\n\t"
"v_mfma_f32_16x16x32_bf16 v[28:31], v[124:127], v[164:167], v[28:31]\n\t"
"buffer_load_dwordx4 v[164:167], v240, s[40:43], s56 offen offset:1024 sc1\n\t"
"v_mfma_f32_16x16x32_bf16 v[32:35], v[112:115], v[168:171], v[32:35]\n\t"
"v_mfma_f32_16x16x32_bf16 v[36:39], v[116:119], v[168:171], v[36:39]\n\t"
"v_mfma_f32_16x16x32_bf16 v[40:43], v[120:123], v[168:171], v[40:43]\n\t"
"v_mfma_f32_16x16x32_bf16 v[44:47], v[124:127], v[168:171], v[44:47]\n\t"
"buffer_load_dwordx4 v[168:171], v240, s[40:43], s56 offen offset:2048 sc1\n\t"
"v_mfma_f32_16x16x32_bf16 v[48:51], v[112:115], v[172:175], v[48:51]\n\t"
"v_mfma_f32_16x16x32_bf16 v[52:55], v[116:119], v[172:175], v[52:55]\n\t"
"v_mfma_f32_16x16x32_bf16 v[56:59], v[120:123], v[172:175], v[56:59]\n\t"
"v_mfma_f32_16x16x32_bf16 v[60:63], v[124:127], v[172:175], v[60:63]\n\t"
"buffer_load_dwordx4 v[172:175], v240, s[40:43], s56 offen offset:3072 sc1\n\t"
"v_mfma_f32_16x16x32_bf16 v[64:67], v[112:115], v[176:179], v[64:67]\n\t"
"s_add_u32 s58, s58, 6144\n\t"
"v_mfma_f32_16x16x32_bf16 v[68:71], v[116:119], v[176:179], v[68:71]\n\t"
"s_sub_u32 s59, s58, 196608\n\t"
"v_mfma_f32_16x16x32_bf16 v[72:75], v[120:123], v[176:179], v[72:75]\n\t"
"s_cselect_b32 s58, s58, s59\n\t"
"v_mfma_f32_16x16x32_bf16 v[76:79], v[124:127], v[176:179], v[76:79]\n\t"
"buffer_load_dwordx4 v[176:179], v240, s[40:43], s57 offen offset:0 sc1\n\t"
"v_mfma_f32_16x16x32_bf16 v[80:83], v[112:115], v[180:183], v[80:83]\n\t"
"v_mfma_f32_16x16x32_bf16 v[84:87], v[116:119], v[180:183], v[84:87]\n\t"
"v_mfma_f32_16x16x32_bf16 v[88:91], v[120:123], v[180:183], v[88:91]\n\t"
"v_mfma_f32_16x16x32_bf16 v[92:95], v[124:127], v[180:183], v[92:95]\n\t"
"buffer_load_dwordx4 v[180:183], v240, s[40:43], s57 offen offset:1024 sc1\n\t"
"s_waitcnt vmcnt(12) lgkmcnt(0)\n\t"
"v_mfma_f32_16x16x32_bf16 v[0:3], v[128:131], v[184:187], v[0:3]\n\t"
"s_mov_b32 s56, s58\n\t"
"v_mfma_f32_16x16x32_bf16 v[4:7], v[132:135], v[184:187], v[4:7]\n\t"
"s_add_u32 s57, s58, 0x1000\n\t"
"v_mfma_f32_16x16x32_bf16 v[8:11], v[136:139], v[184:187], v[8:11]\n\t"
"v_mfma_f32_16x16x32_bf16 v[12:15], v[140:143], v[184:187], v[12:15]\n\t"
"buffer_load_dwordx4 v[184:187], v240, s[40:43], s56 offen offset:0 sc1\n\t"
"v_mfma_f32_16x16x32_bf16 v[16:19], v[128:131], v[188:191], v[16:19]\n\t"
"v_mfma_f32_16x16x32_bf16 v[20:23], v[132:135], v[188:191], v[20:23]\n\t"
"v_mfma_f32_16x16x32_bf16 v[24:27], v[136:139], v[188:191], v[24:27]\n\t"
"v_mfma_f32_16x16x32_bf16 v[28:31], v[140:143], v[188:191], v[28:31]\n\t"
"buffer_load_dwordx4 v[188:191], v240, s[40:43], s56 offen offset:1024 sc1\n\t"
"v_mfma_f32_16x16x32_bf16 v[32:35], v[128:131], v[192:195], v[32:35]\n\t"
"v_mfma_f32_16x16x32_bf16 v[36:39], v[132:135], v[192:195], v[36:39]\n\t"
"v_mfma_f32_16x16x32_bf16 v[40:43], v[136:139], v[192:195], v[40:43]\n\t"
"v_mfma_f32_16x16x32_bf16 v[44:47], v[140:143], v[192:195], v[44:47]\n\t"
"buffer_load_dwordx4 v[192:195], v240, s[40:43], s56 offen offset:2048 sc1\n\t"
"v_mfma_f32_16x16x32_bf16 v[48:51], v[128:131], v[196:199], v[48:51]\n\t"
"v_mfma_f32_16x16x32_bf16 v[52:55], v[132:135], v[196:199], v[52:55]\n\t"
"v_mfma_f32_16x16x32_bf16 v[56:59], v[136:139], v[196:199], v[56:59]\n\t"
"v_mfma_f32_16x16x32_bf16 v[60:63], v[140:143], v[196:199], v[60:63]\n\t"
"buffer_load_dwordx4 v[196:199], v240, s[40:43], s56 offen offset:3072 sc1\n\t"
"v_mfma_f32_16x16x32_bf16 v[64:67], v[128:131], v[200:203], v[64:67]\n\t"
"s_add_u32 s58, s58, 6144\n\t"
"v_mfma_f32_16x16x32_bf16 v[68:71], v[132:135], v[200:203], v[68:71]\n\t"
"s_sub_u32 s59, s58, 196608\n\t"
"v_mfma_f32_16x16x32_bf16 v[72:75], v[136:139], v[200:203], v[72:75]\n\t"
"s_cselect_b32 s58, s58, s59\n\t"
"v_mfma_f32_16x16x32_bf16 v[76:79], v[140:143], v[200:203], v[76:79]\n\t"
"buffer_load_dwordx4 v[200:203], v240, s[40:43], s57 offen offset:0 sc1\n\t"
"v_mfma_f32_16x16x32_bf16 v[80:83], v[128:131], v[204:207], v[80:83]\n\t"
"v_mfma_f32_16x16x32_bf16 v[84:87], v[132:135], v[204:207], v[84:87]\n\t"
"v_mfma_f32_16x16x32_bf16 v[88:91], v[136:139], v[204:207], v[88:91]\n\t"
"v_mfma_f32_16x16x32_bf16 v[92:95], v[140:143], v[204:207], v[92:95]\n\t"
"buffer_load_dwordx4 v[204:207], v240, s[40:43], s57 offen offset:1024 sc1\n\t"
"s_waitcnt vmcnt(12) lgkmcnt(0)\n\t"
"v_mfma_f32_16x16x32_bf16 v[0:3], v[144:147], v[208:211], v[0:3]\n\t"
"s_mov_b32 s56, s58\n\t"
"v_mfma_f32_16x16x32_bf16 v[4:7], v[148:151], v[208:211], v[4:7]\n\t"
"s_add_u32 s57, s58, 0x1000\n\t"
"v_mfma_f32_16x16x32_bf16 v[8:11], v[152:155], v[208:211], v[8:11]\n\t"
"v_mfma_f32_16x16x32_bf16 v[12:15], v[156:159], v[208:211], v[12:15]\n\t"
"buffer_load_dwordx4 v[208:211], v240, s[40:43], s56 offen offset:0 sc1\n\t"
"v_mfma_f32_16x16x32_bf16 v[16:19], v[144:147], v[212:215], v[16:19]\n\t"
"v_mfma_f32_16x16x32_bf16 v[20:23], v[148:151], v[212:215], v[20:23]\n\t"
"v_mfma_f32_16x16x32_bf16 v[24:27], v[152:155], v[212:215], v[24:27]\n\t"
"v_mfma_f32_16x16x32_bf16 v[28:31], v[156:159], v[212:215], v[28:31]\n\t"
"buffer_load_dwordx4 v[212:215], v240, s[40:43], s56 offen offset:1024 sc1\n\t"
"v_mfma_f32_16x16x32_bf16 v[32:35], v[144:147], v[216:219], v[32:35]\n\t"
"v_mfma_f32_16x16x32_bf16 v[36:39], v[148:151], v[216:219], v[36:39]\n\t"
"v_mfma_f32_16x16x32_bf16 v[40:43], v[152:155], v[216:219], v[40:43]\n\t"
"v_mfma_f32_16x16x32_bf16 v[44:47], v[156:159], v[216:219], v[44:47]\n\t"
"buffer_load_dwordx4 v[216:219], v240, s[40:43], s56 offen offset:2048 sc1\n\t"
"v_mfma_f32_16x16x32_bf16 v[48:51], v[144:147], v[220:223], v[48:51]\n\t"
"v_mfma_f32_16x16x32_bf16 v[52:55], v[148:151], v[220:223], v[52:55]\n\t"
"v_mfma_f32_16x16x32_bf16 v[56:59], v[152:155], v[220:223], v[56:59]\n\t"
"v_mfma_f32_16x16x32_bf16 v[60:63], v[156:159], v[220:223], v[60:63]\n\t"
"buffer_load_dwordx4 v[220:223], v240, s[40:43], s56 offen offset:3072 sc1\n\t"
"v_mfma_f32_16x16x32_bf16 v[64:67], v[144:147], v[224:227], v[64:67]\n\t"
"s_add_u32 s58, s58, 6144\n\t"
"v_mfma_f32_16x16x32_bf16 v[68:71], v[148:151], v[224:227], v[68:71]\n\t"
"s_sub_u32 s59, s58, 196608\n\t"
"v_mfma_f32_16x16x32_bf16 v[72:75], v[152:155], v[224:227], v[72:75]\n\t"
"s_cselect_b32 s58, s58, s59\n\t"
"v_mfma_f32_16x16x32_bf16 v[76:79], v[156:159], v[224:227], v[76:79]\n\t"
"buffer_load_dwordx4 v[224:227], v240, s[40:43], s57 offen offset:0 sc1\n\t"
"v_mfma_f32_16x16x32_bf16 v[80:83], v[144:147], v[228:231], v[80:83]\n\t"
"v_mfma_f32_16x16x32_bf16 v[84:87], v[148:151], v[228:231], v[84:87]\n\t"
"v_mfma_f32_16x16x32_bf16 v[88:91], v[152:155], v[228:231], v[88:91]\n\t"
"v_mfma_f32_16x16x32_bf16 v[92:95], v[156:159], v[228:231], v[92:95]\n\t"
"buffer_load_dwordx4 v[228:231], v240, s[40:43], s57 offen offset:1024 sc1\n\t"
"s_waitcnt vmcnt(12) lgkmcnt(0)\n\t"
"v_mfma_f32_16x16x32_bf16 v[0:3], a[0:3], v[160:163], v[0:3]\n\t"
"s_mov_b32 s56, s58\n\t"
"v_mfma_f32_16x16x32_bf16 v[4:7], a[4:7], v[160:163], v[4:7]\n\t"
"s_add_u32 s57, s58, 0x1000\n\t"
"v_mfma_f32_16x16x32_bf16 v[8:11], a[8:11], v[160:163], v[8:11]\n\t"
"v_mfma_f32_16x16x32_bf16 v[12:15], a[12:15], v[160:163], v[12:15]\n\t"
"buffer_load_dwordx4 v[160:163], v240, s[40:43], s56 offen offset:0 sc1\n\t"
"v_mfma_f32_16x16x32_bf16 v[16:19], a[0:3], v[164:167], v[16:19]\n\t"
"v_mfma_f32_16x16x32_bf16 v[20:23], a[4:7], v[164:167], v[20:23]\n\t"
"v_mfma_f32_16x16x32_bf16 v[24:27], a[8:11], v[164:167], v[24:27]\n\t"
"v_mfma_f32_16x16x32_bf16 v[28:31], a[12:15], v[164:167], v[28:31]\n\t"
"buffer_load_dwordx4 v[164:167], v240, s[40:43], s56 offen offset:1024 sc1\n\t"
"v_mfma_f32_16x16x32_bf16 v[32:35], a[0:3], v[168:171], v[32:35]\n\t"
"v_mfma_f32_16x16x32_bf16 v[36:39], a[4:7], v[168:171], v[36:39]\n\t"
"v_mfma_f32_16x16x32_bf16 v[40:43], a[8:11], v[168:171], v[40:43]\n\t"
"v_mfma_f32_16x16x32_bf16 v[44:47], a[12:15], v[168:171], v[44:47]\n\t"
"buffer_load_dwordx4 v[168:171], v240, s[40:43], s56 offen offset:2048 sc1\n\t"
"v_mfma_f32_16x16x32_bf16 v[48:51], a[0:3], v[172:175], v[48:51]\n\t"
"v_mfma_f32_16x16x32_bf16 v[52:55], a[4:7], v[172:175], v[52:55]\n\t"
"v_mfma_f32_16x16x32_bf16 v[56:59], a[8:11], v[172:175], v[56:59]\n\t"
"v_mfma_f32_16x16x32_bf16 v[60:63], a[12:15], v[172:175], v[60:63]\n\t"
"buffer_load_dwordx4 v[172:175], v240, s[40:43], s56 offen offset:3072 sc1\n\t"
"v_mfma_f32_16x16x32_bf16 v[64:67], a[0:3], v[176:179], v[64:67]\n\t"
"s_add_u32 s58, s58, 6144\n\t"
"v_mfma_f32_16x16x32_bf16 v[68:71], a[4:7], v[176:179], v[68:71]\n\t"
"s_sub_u32 s59, s58, 196608\n\t"
"v_mfma_f32_16x16x32_bf16 v[72:75], a[8:11], v[176:179], v[72:75]\n\t"
"s_cselect_b32 s58, s58, s59\n\t"
"v_mfma_f32_16x16x32_bf16 v[76:79], a[12:15], v[176:179], v[76:79]\n\t"
"buffer_load_dwordx4 v[176:179], v240, s[40:43], s57 offen offset:0 sc1\n\t"
"v_mfma_f32_16x16x32_bf16 v[80:83], a[0:3], v[180:183], v[80:83]\n\t"
"v_mfma_f32_16x16x32_bf16 v[84:87], a[4:7], v[180:183], v[84:87]\n\t"
"v_mfma_f32_16x16x32_bf16 v[88:91], a[8:11], v[180:183], v[88:91]\n\t"
"v_mfma_f32_16x16x32_bf16 v[92:95], a[12:15], v[180:183], v[92:95]\n\t"
"buffer_load_dwordx4 v[180:183], v240, s[40:43], s57 offen offset:1024 sc1\n\t"
"s_waitcnt vmcnt(12) lgkmcnt(0)\n\t"
"v_mfma_f32_16x16x32_bf16 v[0:3], a[16:19], v[184:187], v[0:3]\n\t"
"s_mov_b32 s56, s58\n\t"
"v_mfma_f32_16x16x32_bf16 v[4:7], a[20:23], v[184:187], v[4:7]\n\t"
"s_add_u32 s57, s58, 0x1000\n\t"
"v_mfma_f32_16x16x32_bf16 v[8:11], a[24:27], v[184:187], v[8:11]\n\t"
"v_mfma_f32_16x16x32_bf16 v[12:15], a[28:31], v[184:187], v[12:15]\n\t"
"buffer_load_dwordx4 v[184:187], v240, s[40:43], s56 offen offset:0 sc1\n\t"
"v_mfma_f32_16x16x32_bf16 v[16:19], a[16:19], v[188:191], v[16:19]\n\t"
"v_mfma_f32_16x16x32_bf16 v[20:23], a[20:23], v[188:191], v[20:23]\n\t"
"v_mfma_f32_16x16x32_bf16 v[24:27], a[24:27], v[188:191], v[24:27]\n\t"
"v_mfma_f32_16x16x32_bf16 v[28:31], a[28:31], v[188:191], v[28:31]\n\t"
"buffer_load_dwordx4 v[188:191], v240, s[40:43], s56 offen offset:1024 sc1\n\t"
"v_mfma_f32_16x16x32_bf16 v[32:35], a[16:19], v[192:195], v[32:35]\n\t"
"v_mfma_f32_16x16x32_bf16 v[36:39], a[20:23], v[192:195], v[36:39]\n\t"
"v_mfma_f32_16x16x32_bf16 v[40:43], a[24:27], v[192:195], v[40:43]\n\t"
"v_mfma_f32_16x16x32_bf16 v[44:47], a[28:31], v[192:195], v[44:47]\n\t"
"buffer_load_dwordx4 v[192:195], v240, s[40:43], s56 offen offset:2048 sc1\n\t"
"v_mfma_f32_16x16x32_bf16 v[48:51], a[16:19], v[196:199], v[48:51]\n\t"
"v_mfma_f32_16x16x32_bf16 v[52:55], a[20:23], v[196:199], v[52:55]\n\t"
"v_mfma_f32_16x16x32_bf16 v[56:59], a[24:27], v[196:199], v[56:59]\n\t"
"v_mfma_f32_16x16x32_bf16 v[60:63], a[28:31], v[196:199], v[60:63]\n\t"
"buffer_load_dwordx4 v[196:199], v240, s[40:43], s56 offen offset:3072 sc1\n\t"
"v_mfma_f32_16x16x32_bf16 v[64:67], a[16:19], v[200:203], v[64:67]\n\t"
"s_add_u32 s58, s58, 6144\n\t"
"v_mfma_f32_16x16x32_bf16 v[68:71], a[20:23], v[200:203], v[68:71]\n\t"
"s_sub_u32 s59, s58, 196608\n\t"
"v_mfma_f32_16x16x32_bf16 v[72:75], a[24:27], v[200:203], v[72:75]\n\t"
"s_cselect_b32 s58, s58, s59\n\t"
"v_mfma_f32_16x16x32_bf16 v[76:79], a[28:31], v[200:203], v[76:79]\n\t"
"buffer_load_dwordx4 v[200:203], v240, s[40:43], s57 offen offset:0 sc1\n\t"
"v_mfma_f32_16x16x32_bf16 v[80:83], a[16:19], v[204:207], v[80:83]\n\t"
"v_mfma_f32_16x16x32_bf16 v[84:87], a[20:23], v[204:207], v[84:87]\n\t"
"v_mfma_f32_16x16x32_bf16 v[88:91], a[24:27], v[204:207], v[88:91]\n\t"
"v_mfma_f32_16x16x32_bf16 v[92:95], a[28:31], v[204:207], v[92:95]\n\t"
"buffer_load_dwordx4 v[204:207], v240, s[40:43], s57 offen offset:1024 sc1\n\t"
"s_waitcnt vmcnt(12) lgkmcnt(0)\n\t"
"v_mfma_f32_16x16x32_bf16 v[0:3], a[32:35], v[208:211], v[0:3]\n\t"
"s_mov_b32 s56, s58\n\t"
"v_mfma_f32_16x16x32_bf16 v[4:7], a[36:39], v[208:211], v[4:7]\n\t"
"s_add_u32 s57, s58, 0x1000\n\t"
"v_mfma_f32_16x16x32_bf16 v[8:11], a[40:43], v[208:211], v[8:11]\n\t"
"v_mfma_f32_16x16x32_bf16 v[12:15], a[44:47], v[208:211], v[12:15]\n\t"
"buffer_load_dwordx4 v[208:211], v240, s[40:43], s56 offen offset:0 sc1\n\t"
"v_mfma_f32_16x16x32_bf16 v[16:19], a[32:35], v[212:215], v[16:19]\n\t"
"v_mfma_f32_16x16x32_bf16 v[20:23], a[36:39], v[212:215], v[20:23]\n\t"
"v_mfma_f32_16x16x32_bf16 v[24:27], a[40:43], v[212:215], v[24:27]\n\t"
"v_mfma_f32_16x16x32_bf16 v[28:31], a[44:47], v[212:215], v[28:31]\n\t"
"buffer_load_dwordx4 v[212:215], v240, s[40:43], s56 offen offset:1024 sc1\n\t"
"v_mfma_f32_16x16x32_bf16 v[32:35], a[32:35], v[216:219], v[32:35]\n\t"
"v_mfma_f32_16x16x32_bf16 v[36:39], a[36:39], v[216:219], v[36:39]\n\t"
"v_mfma_f32_16x16x32_bf16 v[40:43], a[40:43], v[216:219], v[40:43]\n\t"
"v_mfma_f32_16x16x32_bf16 v[44:47], a[44:47], v[216:219], v[44:47]\n\t"
"buffer_load_dwordx4 v[216:219], v240, s[40:43], s56 offen offset:2048 sc1\n\t"
"v_mfma_f32_16x16x32_bf16 v[48:51], a[32:35], v[220:223], v[48:51]\n\t"
"v_mfma_f32_16x16x32_bf16 v[52:55], a[36:39], v[220:223], v[52:55]\n\t"
"v_mfma_f32_16x16x32_bf16 v[56:59], a[40:43], v[220:223], v[56:59]\n\t"
"v_mfma_f32_16x16x32_bf16 v[60:63], a[44:47], v[220:223], v[60:63]\n\t"
"buffer_load_dwordx4 v[220:223], v240, s[40:43], s56 offen offset:3072 sc1\n\t"
"v_mfma_f32_16x16x32_bf16 v[64:67], a[32:35], v[224:227], v[64:67]\n\t"
"s_add_u32 s58, s58, 6144\n\t"
"v_mfma_f32_16x16x32_bf16 v[68:71], a[36:39], v[224:227], v[68:71]\n\t"
"s_sub_u32 s59, s58, 196608\n\t"
"v_mfma_f32_16x16x32_bf16 v[72:75], a[40:43], v[224:227], v[72:75]\n\t"
"s_cselect_b32 s58, s58, s59\n\t"
"v_mfma_f32_16x16x32_bf16 v[76:79], a[44:47], v[224:227], v[76:79]\n\t"
"buffer_load_dwordx4 v[224:227], v240, s[40:43], s57 offen offset:0 sc1\n\t"
"v_mfma_f32_16x16x32_bf16 v[80:83], a[32:35], v[228:231], v[80:83]\n\t"
"v_mfma_f32_16x16x32_bf16 v[84:87], a[36:39], v[228:231], v[84:87]\n\t"
"v_mfma_f32_16x16x32_bf16 v[88:91], a[40:43], v[228:231], v[88:91]\n\t"
"v_mfma_f32_16x16x32_bf16 v[92:95], a[44:47], v[228:231], v[92:95]\n\t"
"buffer_load_dwordx4 v[228:231], v240, s[40:43], s57 offen offset:1024 sc1\n\t"
"s_waitcnt vmcnt(12) lgkmcnt(0)\n\t"
"v_mfma_f32_16x16x32_bf16 v[0:3], a[48:51], v[160:163], v[0:3]\n\t"
"s_mov_b32 s56, s58\n\t"
"v_mfma_f32_16x16x32_bf16 v[4:7], a[52:55], v[160:163], v[4:7]\n\t"
"s_add_u32 s57, s58, 0x1000\n\t"
"v_mfma_f32_16x16x32_bf16 v[8:11], a[56:59], v[160:163], v[8:11]\n\t"
"v_mfma_f32_16x16x32_bf16 v[12:15], a[60:63], v[160:163], v[12:15]\n\t"
"buffer_load_dwordx4 v[160:163], v240, s[40:43], s56 offen offset:0 sc1\n\t"
"v_mfma_f32_16x16x32_bf16 v[16:19], a[48:51], v[164:167], v[16:19]\n\t"
"v_mfma_f32_16x16x32_bf16 v[20:23], a[52:55], v[164:167], v[20:23]\n\t"
"v_mfma_f32_16x16x32_bf16 v[24:27], a[56:59], v[164:167], v[24:27]\n\t"
"v_mfma_f32_16x16x32_bf16 v[28:31], a[60:63], v[164:167], v[28:31]\n\t"
"buffer_load_dwordx4 v[164:167], v240, s[40:43], s56 offen offset:1024 sc1\n\t"
"v_mfma_f32_16x16x32_bf16 v[32:35], a[48:51], v[168:171], v[32:35]\n\t"
"v_mfma_f32_16x16x32_bf16 v[36:39], a[52:55], v[168:171], v[36:39]\n\t"
"v_mfma_f32_16x16x32_bf16 v[40:43], a[56:59], v[168:171], v[40:43]\n\t"
"v_mfma_f32_16x16x32_bf16 v[44:47], a[60:63], v[168:171], v[44:47]\n\t"
"buffer_load_dwordx4 v[168:171], v240, s[40:43], s56 offen offset:2048 sc1\n\t"
"v_mfma_f32_16x16x32_bf16 v[48:51], a[48:51], v[172:175], v[48:51]\n\t"
"v_mfma_f32_16x16x32_bf16 v[52:55], a[52:55], v[172:175], v[52:55]\n\t"
"v_mfma_f32_16x16x32_bf16 v[56:59], a[56:59], v[172:175], v[56:59]\n\t"
"v_mfma_f32_16x16x32_bf16 v[60:63], a[60:63], v[172:175], v[60:63]\n\t"
"buffer_load_dwordx4 v[172:175], v240, s[40:43], s56 offen offset:3072 sc1\n\t"
"v_mfma_f32_16x16x32_bf16 v[64:67], a[48:51], v[176:179], v[64:67]\n\t"
"s_add_u32 s58, s58, 6144\n\t"
"v_mfma_f32_16x16x32_bf16 v[68:71], a[52:55], v[176:179], v[68:71]\n\t"
"s_sub_u32 s59, s58, 196608\n\t"
"v_mfma_f32_16x16x32_bf16 v[72:75], a[56:59], v[176:179], v[72:75]\n\t"
"s_cselect_b32 s58, s58, s59\n\t"
"v_mfma_f32_16x16x32_bf16 v[76:79], a[60:63], v[176:179], v[76:79]\n\t"
"buffer_load_dwordx4 v[176:179], v240, s[40:43], s57 offen offset:0 sc1\n\t"
"v_mfma_f32_16x16x32_bf16 v[80:83], a[48:51], v[180:183], v[80:83]\n\t"
"v_mfma_f32_16x16x32_bf16 v[84:87], a[52:55], v[180:183], v[84:87]\n\t"
"v_mfma_f32_16x16x32_bf16 v[88:91], a[56:59], v[180:183], v[88:91]\n\t"
"v_mfma_f32_16x16x32_bf16 v[92:95], a[60:63], v[180:183], v[92:95]\n\t"
"buffer_load_dwordx4 v[180:183], v240, s[40:43], s57 offen offset:1024 sc1\n\t"
"s_waitcnt vmcnt(12) lgkmcnt(0)\n\t"
"v_mfma_f32_16x16x32_bf16 v[0:3], a[64:67], v[184:187], v[0:3]\n\t"
"s_mov_b32 s56, s58\n\t"
"v_mfma_f32_16x16x32_bf16 v[4:7], a[68:71], v[184:187], v[4:7]\n\t"
"s_add_u32 s57, s58, 0x1000\n\t"
"v_mfma_f32_16x16x32_bf16 v[8:11], a[72:75], v[184:187], v[8:11]\n\t"
"v_mfma_f32_16x16x32_bf16 v[12:15], a[76:79], v[184:187], v[12:15]\n\t"
"buffer_load_dwordx4 v[184:187], v240, s[40:43], s56 offen offset:0 sc1\n\t"
"v_mfma_f32_16x16x32_bf16 v[16:19], a[64:67], v[188:191], v[16:19]\n\t"
"v_mfma_f32_16x16x32_bf16 v[20:23], a[68:71], v[188:191], v[20:23]\n\t"
"v_mfma_f32_16x16x32_bf16 v[24:27], a[72:75], v[188:191], v[24:27]\n\t"
"v_mfma_f32_16x16x32_bf16 v[28:31], a[76:79], v[188:191], v[28:31]\n\t"
"buffer_load_dwordx4 v[188:191], v240, s[40:43], s56 offen offset:1024 sc1\n\t"
"v_mfma_f32_16x16x32_bf16 v[32:35], a[64:67], v[192:195], v[32:35]\n\t"
"v_mfma_f32_16x16x32_bf16 v[36:39], a[68:71], v[192:195], v[36:39]\n\t"
"v_mfma_f32_16x16x32_bf16 v[40:43], a[72:75], v[192:195], v[40:43]\n\t"
"v_mfma_f32_16x16x32_bf16 v[44:47], a[76:79], v[192:195], v[44:47]\n\t"
"buffer_load_dwordx4 v[192:195], v240, s[40:43], s56 offen offset:2048 sc1\n\t"
"v_mfma_f32_16x16x32_bf16 v[48:51], a[64:67], v[196:199], v[48:51]\n\t"
"v_mfma_f32_16x16x32_bf16 v[52:55], a[68:71], v[196:199], v[52:55]\n\t"
"v_mfma_f32_16x16x32_bf16 v[56:59], a[72:75], v[196:199], v[56:59]\n\t"
"v_mfma_f32_16x16x32_bf16 v[60:63], a[76:79], v[196:199], v[60:63]\n\t"
"buffer_load_dwordx4 v[196:199], v240, s[40:43], s56 offen offset:3072 sc1\n\t"
"v_mfma_f32_16x16x32_bf16 v[64:67], a[64:67], v[200:203], v[64:67]\n\t"
"s_add_u32 s58, s58, 6144\n\t"
"v_mfma_f32_16x16x32_bf16 v[68:71], a[68:71], v[200:203], v[68:71]\n\t"
"s_sub_u32 s59, s58, 196608\n\t"
"v_mfma_f32_16x16x32_bf16 v[72:75], a[72:75], v[200:203], v[72:75]\n\t"
"s_cselect_b32 s58, s58, s59\n\t"
"v_mfma_f32_16x16x32_bf16 v[76:79], a[76:79], v[200:203], v[76:79]\n\t"
"buffer_load_dwordx4 v[200:203], v240, s[40:43], s57 offen offset:0 sc1\n\t"
"v_mfma_f32_16x16x32_bf16 v[80:83], a[64:67], v[204:207], v[80:83]\n\t"
"v_mfma_f32_16x16x32_bf16 v[84:87], a[68:71], v[204:207], v[84:87]\n\t"
"v_mfma_f32_16x16x32_bf16 v[88:91], a[72:75], v[204:207], v[88:91]\n\t"
"v_mfma_f32_16x16x32_bf16 v[92:95], a[76:79], v[204:207], v[92:95]\n\t"
"buffer_load_dwordx4 v[204:207], v240, s[40:43], s57 offen offset:1024 sc1\n\t"
"s_waitcnt vmcnt(12) lgkmcnt(0)\n\t"
"v_mfma_f32_16x16x32_bf16 v[0:3], a[80:83], v[208:211], v[0:3]\n\t"
"v_mfma_f32_16x16x32_bf16 v[4:7], a[84:87], v[208:211], v[4:7]\n\t"
"v_mfma_f32_16x16x32_bf16 v[8:11], a[88:91], v[208:211], v[8:11]\n\t"
"v_mfma_f32_16x16x32_bf16 v[12:15], a[92:95], v[208:211], v[12:15]\n\t"
"v_mfma_f32_16x16x32_bf16 v[16:19], a[80:83], v[212:215], v[16:19]\n\t"
"v_mfma_f32_16x16x32_bf16 v[20:23], a[84:87], v[212:215], v[20:23]\n\t"
"v_mfma_f32_16x16x32_bf16 v[24:27], a[88:91], v[212:215], v[24:27]\n\t"
"v_mfma_f32_16x16x32_bf16 v[28:31], a[92:95], v[212:215], v[28:31]\n\t"
"v_mfma_f32_16x16x32_bf16 v[32:35], a[80:83], v[216:219], v[32:35]\n\t"
"v_mfma_f32_16x16x32_bf16 v[36:39], a[84:87], v[216:219], v[36:39]\n\t"
"v_mfma_f32_16x16x32_bf16 v[40:43], a[88:91], v[216:219], v[40:43]\n\t"
"v_mfma_f32_16x16x32_bf16 v[44:47], a[92:95], v[216:219], v[44:47]\n\t"
"v_mfma_f32_16x16x32_bf16 v[48:51], a[80:83], v[220:223], v[48:51]\n\t"
"v_mfma_f32_16x16x32_bf16 v[52:55], a[84:87], v[220:223], v[52:55]\n\t"
"v_mfma_f32_16x16x32_bf16 v[56:59], a[88:91], v[220:223], v[56:59]\n\t"
"v_mfma_f32_16x16x32_bf16 v[60:63], a[92:95], v[220:223], v[60:63]\n\t"
"v_mfma_f32_16x16x32_bf16 v[64:67], a[80:83], v[224:227], v[64:67]\n\t"
"v_mfma_f32_16x16x32_bf16 v[68:71], a[84:87], v[224:227], v[68:71]\n\t"
"v_mfma_f32_16x16x32_bf16 v[72:75], a[88:91], v[224:227], v[72:75]\n\t"
"v_mfma_f32_16x16x32_bf16 v[76:79], a[92:95], v[224:227], v[76:79]\n\t"
"v_mfma_f32_16x16x32_bf16 v[80:83], a[80:83], v[228:231], v[80:83]\n\t"
"v_mfma_f32_16x16x32_bf16 v[84:87], a[84:87], v[228:231], v[84:87]\n\t"
"v_mfma_f32_16x16x32_bf16 v[88:91], a[88:91], v[228:231], v[88:91]\n\t"
"v_mfma_f32_16x16x32_bf16 v[92:95], a[92:95], v[228:231], v[92:95]\n\t"
"s_waitcnt vmcnt(6) lgkmcnt(0)\n\t"
"v_mfma_f32_16x16x32_bf16 v[0:3], a[96:99], v[160:163], v[0:3]\n\t"
"v_mfma_f32_16x16x32_bf16 v[4:7], a[100:103], v[160:163], v[4:7]\n\t"
"v_mfma_f32_16x16x32_bf16 v[8:11], a[104:107], v[160:163], v[8:11]\n\t"
"v_mfma_f32_16x16x32_bf16 v[12:15], a[108:111], v[160:163], v[12:15]\n\t"
"v_mfma_f32_16x16x32_bf16 v[16:19], a[96:99], v[164:167], v[16:19]\n\t"
"v_mfma_f32_16x16x32_bf16 v[20:23], a[100:103], v[164:167], v[20:23]\n\t"
"v_mfma_f32_16x16x32_bf16 v[24:27], a[104:107], v[164:167], v[24:27]\n\t"
"v_mfma_f32_16x16x32_bf16 v[28:31], a[108:111], v[164:167], v[28:31]\n\t"
"v_mfma_f32_16x16x32_bf16 v[32:35], a[96:99], v[168:171], v[32:35]\n\t"
"v_mfma_f32_16x16x32_bf16 v[36:39], a[100:103], v[168:171], v[36:39]\n\t"
"v_mfma_f32_16x16x32_bf16 v[40:43], a[104:107], v[168:171], v[40:43]\n\t"
"v_mfma_f32_16x16x32_bf16 v[44:47], a[108:111], v[168:171], v[44:47]\n\t"
"v_mfma_f32_16x16x32_bf16 v[48:51], a[96:99], v[172:175], v[48:51]\n\t"
"v_mfma_f32_16x16x32_bf16 v[52:55], a[100:103], v[172:175], v[52:55]\n\t"
"v_mfma_f32_16x16x32_bf16 v[56:59], a[104:107], v[172:175], v[56:59]\n\t"
"v_mfma_f32_16x16x32_bf16 v[60:63], a[108:111], v[172:175], v[60:63]\n\t"
"v_mfma_f32_16x16x32_bf16 v[64:67], a[96:99], v[176:179], v[64:67]\n\t"
"v_mfma_f32_16x16x32_bf16 v[68:71], a[100:103], v[176:179], v[68:71]\n\t"
"v_mfma_f32_16x16x32_bf16 v[72:75], a[104:107], v[176:179], v[72:75]\n\t"
"v_mfma_f32_16x16x32_bf16 v[76:79], a[108:111], v[176:179], v[76:79]\n\t"
"v_mfma_f32_16x16x32_bf16 v[80:83], a[96:99], v[180:183], v[80:83]\n\t"
"v_mfma_f32_16x16x32_bf16 v[84:87], a[100:103], v[180:183], v[84:87]\n\t"
"v_mfma_f32_16x16x32_bf16 v[88:91], a[104:107], v[180:183], v[88:91]\n\t"
"v_mfma_f32_16x16x32_bf16 v[92:95], a[108:111], v[180:183], v[92:95]\n\t"
"s_waitcnt vmcnt(0) lgkmcnt(0)\n\t"
"v_mfma_f32_16x16x32_bf16 v[0:3], a[112:115], v[184:187], v[0:3]\n\t"
"v_mfma_f32_16x16x32_bf16 v[4:7], a[116:119], v[184:187], v[4:7]\n\t"
"v_mfma_f32_16x16x32_bf16 v[8:11], a[120:123], v[184:187], v[8:11]\n\t"
"v_mfma_f32_16x16x32_bf16 v[12:15], a[124:127], v[184:187], v[12:15]\n\t"
"v_mfma_f32_16x16x32_bf16 v[16:19], a[112:115], v[188:191], v[16:19]\n\t"
"v_mfma_f32_16x16x32_bf16 v[20:23], a[116:119], v[188:191], v[20:23]\n\t"
"v_mfma_f32_16x16x32_bf16 v[24:27], a[120:123], v[188:191], v[24:27]\n\t"
"v_mfma_f32_16x16x32_bf16 v[28:31], a[124:127], v[188:191], v[28:31]\n\t"
"v_mfma_f32_16x16x32_bf16 v[32:35], a[112:115], v[192:195], v[32:35]\n\t"
"v_mfma_f32_16x16x32_bf16 v[36:39], a[116:119], v[192:195], v[36:39]\n\t"
"v_mfma_f32_16x16x32_bf16 v[40:43], a[120:123], v[192:195], v[40:43]\n\t"
"v_mfma_f32_16x16x32_bf16 v[44:47], a[124:127], v[192:195], v[44:47]\n\t"
"v_mfma_f32_16x16x32_bf16 v[48:51], a[112:115], v[196:199], v[48:51]\n\t"
"v_mfma_f32_16x16x32_bf16 v[52:55], a[116:119], v[196:199], v[52:55]\n\t"
"v_mfma_f32_16x16x32_bf16 v[56:59], a[120:123], v[196:199], v[56:59]\n\t"
"v_mfma_f32_16x16x32_bf16 v[60:63], a[124:127], v[196:199], v[60:63]\n\t"
"v_mfma_f32_16x16x32_bf16 v[64:67], a[112:115], v[200:203], v[64:67]\n\t"
"v_mfma_f32_16x16x32_bf16 v[68:71], a[116:119], v[200:203], v[68:71]\n\t"
"v_mfma_f32_16x16x32_bf16 v[72:75], a[120:123], v[200:203], v[72:75]\n\t"
"v_mfma_f32_16x16x32_bf16 v[76:79], a[124:127], v[200:203], v[76:79]\n\t"
"v_mfma_f32_16x16x32_bf16 v[80:83], a[112:115], v[204:207], v[80:83]\n\t"
"v_mfma_f32_16x16x32_bf16 v[84:87], a[116:119], v[204:207], v[84:87]\n\t"
"v_mfma_f32_16x16x32_bf16 v[88:91], a[120:123], v[204:207], v[88:91]\n\t"
"v_mfma_f32_16x16x32_bf16 v[92:95], a[124:127], v[204:207], v[92:95]\n\t"
"v_and_b32_e32 v238, 15, v252\n\t"
"v_lshlrev_b32_e64 v104, 2, v238\n\t"
"global_load_dword v234, v104, %16 sc1\n\t"
"v_lshrrev_b32_e32 v239, 4, v252\n\t"
"v_and_b32_e32 v237, 7, v252\n\t"
"v_or_b32_e32 v96, 0, v239\n\t"
"v_xor_b32_e32 v96, v96, v237\n\t"
"v_lshlrev_b32_e64 v96, 4, v96\n\t"
"v_lshl_add_u32 v96, v238, 8, v96\n\t"
"v_add_u32_e64 v96, s72, v96\n\t"
"v_add_u32_e32 v98, 65536, v96\n\t"
"v_or_b32_e32 v97, 4, v239\n\t"
"v_xor_b32_e32 v97, v97, v237\n\t"
"v_lshlrev_b32_e64 v97, 4, v97\n\t"
"v_lshl_add_u32 v97, v238, 8, v97\n\t"
"v_add_u32_e64 v97, s72, v97\n\t"
"v_add_u32_e32 v99, 65536, v97\n\t"
"ds_write_b128 v96, v[0:3] offset:0\n\t"
"ds_write_b128 v97, v[4:7] offset:0\n\t"
"ds_write_b128 v96, v[8:11] offset:128\n\t"
"ds_write_b128 v97, v[12:15] offset:128\n\t"
"ds_write_b128 v96, v[16:19] offset:16384\n\t"
"ds_write_b128 v97, v[20:23] offset:16384\n\t"
"ds_write_b128 v96, v[24:27] offset:16512\n\t"
"ds_write_b128 v97, v[28:31] offset:16512\n\t"
"ds_write_b128 v96, v[32:35] offset:32768\n\t"
"ds_write_b128 v97, v[36:39] offset:32768\n\t"
"ds_write_b128 v96, v[40:43] offset:32896\n\t"
"ds_write_b128 v97, v[44:47] offset:32896\n\t"
"ds_write_b128 v96, v[48:51] offset:49152\n\t"
"ds_write_b128 v97, v[52:55] offset:49152\n\t"
"ds_write_b128 v96, v[56:59] offset:49280\n\t"
"ds_write_b128 v97, v[60:63] offset:49280\n\t"
"ds_write_b128 v98, v[64:67] offset:0\n\t"
"ds_write_b128 v99, v[68:71] offset:0\n\t"
"ds_write_b128 v98, v[72:75] offset:128\n\t"
"ds_write_b128 v99, v[76:79] offset:128\n\t"
"ds_write_b128 v98, v[80:83] offset:16384\n\t"
"ds_write_b128 v99, v[84:87] offset:16384\n\t"
"ds_write_b128 v98, v[88:91] offset:16512\n\t"
"ds_write_b128 v99, v[92:95] offset:16512\n\t"
"s_lshl_b32 s71, s52, 3\n\t"
"v_lshrrev_b32_e32 v238, 3, v252\n\t"
"v_add_u32_e32 v238, s71, v238\n\t"
"v_lshlrev_b32_e32 v100, 1, v237\n\t"
"v_and_b32_e32 v101, 7, v238\n\t"
"v_xor_b32_e32 v100, v100, v101\n\t"
"v_xor_b32_e32 v101, 1, v100\n\t"
"v_and_b32_e32 v239, 15, v238\n\t"
"s_lshl_b32 s71, s81, 14\n\t"
"s_add_u32 s71, s71, s53\n\t"
"v_lshlrev_b32_e64 v100, 4, v100\n\t"
"v_lshl_add_u32 v100, v239, 8, v100\n\t"
"v_add_u32_e64 v100, s71, v100\n\t"
"v_add_u32_e32 v102, 65536, v100\n\t"
"v_lshlrev_b32_e64 v101, 4, v101\n\t"
"v_lshl_add_u32 v101, v239, 8, v101\n\t"
"v_add_u32_e64 v101, s71, v101\n\t"
"v_add_u32_e32 v103, 65536, v101\n\t"
"v_lshlrev_b32_e64 v240, 11, v238\n\t"
"v_lshl_add_u32 v240, v237, 4, v240\n\t"
"s_waitcnt lgkmcnt(0)\n\t"
"s_barrier\n\t"
"ds_read_b128 v[0:3], v100 offset:0\n\t"
"ds_read_b128 v[4:7], v101 offset:0\n\t"
"ds_read_b128 v[8:11], v100 offset:4096\n\t"
"ds_read_b128 v[12:15], v101 offset:4096\n\t"
"ds_read_b128 v[16:19], v100 offset:8192\n\t"
"ds_read_b128 v[20:23], v101 offset:8192\n\t"
"ds_read_b128 v[24:27], v100 offset:12288\n\t"
"ds_read_b128 v[28:31], v101 offset:12288\n\t"
"ds_read_b128 v[32:35], v100 offset:32768\n\t"
"ds_read_b128 v[36:39], v101 offset:32768\n\t"
"ds_read_b128 v[40:43], v100 offset:36864\n\t"
"ds_read_b128 v[44:47], v101 offset:36864\n\t"
"ds_read_b128 v[48:51], v100 offset:40960\n\t"
"ds_read_b128 v[52:55], v101 offset:40960\n\t"
"ds_read_b128 v[56:59], v100 offset:45056\n\t"
"ds_read_b128 v[60:63], v101 offset:45056\n\t"
"ds_read_b128 v[64:67], v102 offset:0\n\t"
"ds_read_b128 v[68:71], v103 offset:0\n\t"
"ds_read_b128 v[72:75], v102 offset:4096\n\t"
"ds_read_b128 v[76:79], v103 offset:4096\n\t"
"ds_read_b128 v[80:83], v102 offset:8192\n\t"
"ds_read_b128 v[84:87], v103 offset:8192\n\t"
"ds_read_b128 v[88:91], v102 offset:12288\n\t"
"ds_read_b128 v[92:95], v103 offset:12288\n\t"
"s_waitcnt lgkmcnt(15)\n\t"
"v_pk_add_f32 v[0:1], v[0:1], v[8:9]\n\t"
"v_pk_add_f32 v[16:17], v[16:17], v[24:25]\n\t"
"v_pk_add_f32 v[2:3], v[2:3], v[10:11]\n\t"
"v_pk_add_f32 v[18:19], v[18:19], v[26:27]\n\t"
"v_pk_add_f32 v[4:5], v[4:5], v[12:13]\n\t"
"v_pk_add_f32 v[20:21], v[20:21], v[28:29]\n\t"
"v_pk_add_f32 v[6:7], v[6:7], v[14:15]\n\t"
"v_pk_add_f32 v[22:23], v[22:23], v[30:31]\n\t"
"v_pk_add_f32 v[0:1], v[0:1], v[16:17]\n\t"
"v_pk_add_f32 v[2:3], v[2:3], v[18:19]\n\t"
"v_pk_add_f32 v[4:5], v[4:5], v[20:21]\n\t"
"v_pk_add_f32 v[6:7], v[6:7], v[22:23]\n\t"
"v_cvt_pk_bf16_f32 v8, v0, v1\n\t"
"v_cvt_pk_bf16_f32 v9, v2, v3\n\t"
"v_cvt_pk_bf16_f32 v10, v4, v5\n\t"
"v_cvt_pk_bf16_f32 v11, v6, v7\n\t"
"s_mov_b32 s71, 0\n\t"
"buffer_store_dwordx4 v[8:11], v240, s[36:39], s71 offen offset:0 sc1\n\t"
"s_waitcnt lgkmcnt(8)\n\t"
"v_pk_add_f32 v[32:33], v[32:33], v[40:41]\n\t"
"v_pk_add_f32 v[48:49], v[48:49], v[56:57]\n\t"
"v_pk_add_f32 v[34:35], v[34:35], v[42:43]\n\t"
"v_pk_add_f32 v[50:51], v[50:51], v[58:59]\n\t"
"v_pk_add_f32 v[36:37], v[36:37], v[44:45]\n\t"
"v_pk_add_f32 v[52:53], v[52:53], v[60:61]\n\t"
"v_pk_add_f32 v[38:39], v[38:39], v[46:47]\n\t"
"v_pk_add_f32 v[54:55], v[54:55], v[62:63]\n\t"
"v_pk_add_f32 v[32:33], v[32:33], v[48:49]\n\t"
"v_pk_add_f32 v[34:35], v[34:35], v[50:51]\n\t"
"v_pk_add_f32 v[36:37], v[36:37], v[52:53]\n\t"
"v_pk_add_f32 v[38:39], v[38:39], v[54:55]\n\t"
"v_cvt_pk_bf16_f32 v40, v32, v33\n\t"
"v_cvt_pk_bf16_f32 v41, v34, v35\n\t"
"v_cvt_pk_bf16_f32 v42, v36, v37\n\t"
"v_cvt_pk_bf16_f32 v43, v38, v39\n\t"
"s_mov_b32 s71, 65536\n\t"
"buffer_store_dwordx4 v[40:43], v240, s[36:39], s71 offen offset:0 sc1\n\t"
"s_waitcnt lgkmcnt(0)\n\t"
"v_pk_add_f32 v[64:65], v[64:65], v[72:73]\n\t"
"v_pk_add_f32 v[80:81], v[80:81], v[88:89]\n\t"
"v_pk_add_f32 v[66:67], v[66:67], v[74:75]\n\t"
"v_pk_add_f32 v[82:83], v[82:83], v[90:91]\n\t"
"v_pk_add_f32 v[68:69], v[68:69], v[76:77]\n\t"
"v_pk_add_f32 v[84:85], v[84:85], v[92:93]\n\t"
"v_pk_add_f32 v[70:71], v[70:71], v[78:79]\n\t"
"v_pk_add_f32 v[86:87], v[86:87], v[94:95]\n\t"
"v_pk_add_f32 v[64:65], v[64:65], v[80:81]\n\t"
"v_pk_add_f32 v[66:67], v[66:67], v[82:83]\n\t"
"v_pk_add_f32 v[68:69], v[68:69], v[84:85]\n\t"
"v_pk_add_f32 v[70:71], v[70:71], v[86:87]\n\t"
"v_cvt_pk_bf16_f32 v72, v64, v65\n\t"
"v_cvt_pk_bf16_f32 v73, v66, v67\n\t"
"v_cvt_pk_bf16_f32 v74, v68, v69\n\t"
"v_cvt_pk_bf16_f32 v75, v70, v71\n\t"
"s_mov_b32 s71, 131072\n\t"
"buffer_store_dwordx4 v[72:75], v240, s[36:39], s71 offen offset:0 sc1\n\t"
"s_cmp_lg_u32 s52, 0\n\t"
"s_cbranch_scc1 FIN%=\n\t"
"s_waitcnt vmcnt(3)\n\t"
"v_cmp_eq_u32_e32 vcc, v241, v234\n\t"
"s_cbranch_vccz FIN%=\n\t"
"v_and_b32_e32 v237, 15, v252\n\t"
"v_lshlrev_b32_e32 v237, 2, v237\n\t"
"v_mov_b32_e32 v239, 0\n\t"
"s_mov_b32 s71, 0x10000\n\t"
"ACK%=:\n\t"
"global_load_dword v238, v237, %16 sc1\n\t"
"s_waitcnt vmcnt(0)\n\t"
"v_cmp_eq_u32_e32 vcc, v241, v238\n\t"
"s_cbranch_vccz FIN%=\n\t"
"v_add_u32_e64 v233, 1, v233\n\t"
"s_mov_b64 exec, 1\n\t"
"global_store_dword v239, v233, %12 sc1\n\t"
"s_mov_b64 exec, -1\n\t"
"s_sleep 8\n\t"
"s_sub_u32 s71, s71, 1\n\t"
"s_cmp_lg_u32 s71, 0\n\t"
"s_cbranch_scc1 ACK%=\n\t"
"FIN%=:\n\t"
"s_branch END%=\n\t"
"PC0%=:\n\t"
"s_sleep 1\n\t"
"s_memrealtime s[74:75]\n\t"
"s_waitcnt lgkmcnt(0)\n\t"
"s_sub_u32 s77, s76, s74\n\t"
"s_cmp_gt_i32 s77, 0\n\t"
"s_cbranch_scc1 PC0%=\n\t"
"s_branch PCB0%=\n\t"
"PC1%=:\n\t"
"s_sleep 1\n\t"
"s_memrealtime s[74:75]\n\t"
"s_waitcnt lgkmcnt(0)\n\t"
"s_sub_u32 s77, s76, s74\n\t"
"s_cmp_gt_i32 s77, 0\n\t"
"s_cbranch_scc1 PC1%=\n\t"
"s_branch PCB1%=\n\t"
"LC%=:\n\t"
"s_memtime s[66:67]\n\t"
"s_memrealtime s[74:75]\n\t"
"s_waitcnt lgkmcnt(0)\n\t"
"s_sub_u32 s66, s66, s64\n\t"
"s_sub_u32 s67, s74, s65\n\t"
"s_mul_i32 s66, s66, 10\n\t"
"s_mul_i32 s67, s67, 172\n\t"
"s_cmp_ge_u32 s66, s67\n\t"
"s_cbranch_scc1 LCB%=\n\t"
"s_mov_b32 s62, 122\n\t"
"s_sub_u32 s80, s80, 12\n\t"
"s_branch LCB%=\n\t"
"RC0%=:\n\t"
"v_and_b32_e32 v233, 15, v252\n\t"
"v_lshlrev_b32_e32 v233, 8, v233\n\t"
"v_lshrrev_b32_e64 v234, 4, v252\n\t"
"v_lshl_add_u32 v233, v234, 4, v233\n\t"
"s_sub_u32 s61, s59, 1\n\t"
"s_lshl_b32 s61, s61, 13\n\t"
"v_add_u32_e64 v233, s61, v233\n\t"
"v_add_u32_e32 v234, 0x1000, v233\n\t"
"global_load_dwordx4 a[192:195], v233, %19 offset:0\n\t"
"global_load_dwordx4 a[196:199], v233, %19 offset:64\n\t"
"global_load_dwordx4 a[200:203], v233, %19 offset:128\n\t"
"global_load_dwordx4 a[204:207], v233, %19 offset:192\n\t"
"global_load_dwordx4 a[208:211], v234, %19 offset:0\n\t"
"global_load_dwordx4 a[212:215], v234, %19 offset:64\n\t"
"global_load_dwordx4 a[216:219], v234, %19 offset:128\n\t"
"global_load_dwordx4 a[220:223], v234, %19 offset:192\n\t"
"s_waitcnt vmcnt(0)\n\t"
"v_mfma_f32_16x16x32_bf16 v[0:3], a[192:195], v[100:103], v[192:195]\n\t"
"v_mfma_f32_16x16x32_bf16 v[4:7], a[208:211], v[100:103], v[192:195]\n\t"
"v_mfma_f32_16x16x32_bf16 v[0:3], a[196:199], v[148:151], v[0:3]\n\t"
"v_mfma_f32_16x16x32_bf16 v[4:7], a[212:215], v[148:151], v[4:7]\n\t"
"v_mfma_f32_16x16x32_bf16 v[0:3], a[200:203], v[96:99], v[0:3]\n\t"
"v_mfma_f32_16x16x32_bf16 v[4:7], a[216:219], v[96:99], v[4:7]\n\t"
"v_mfma_f32_16x16x32_bf16 v[0:3], a[204:207], v[144:147], v[0:3]\n\t"
"v_mfma_f32_16x16x32_bf16 v[4:7], a[220:223], v[144:147], v[4:7]\n\t"
"v_mfma_f32_16x16x32_bf16 v[8:11], a[192:195], v[108:111], v[196:199]\n\t"
"v_mfma_f32_16x16x32_bf16 v[12:15], a[208:211], v[108:111], v[196:199]\n\t"
"v_mfma_f32_16x16x32_bf16 v[8:11], a[196:199], v[156:159], v[8:11]\n\t"
"v_mfma_f32_16x16x32_bf16 v[12:15], a[212:215], v[156:159], v[12:15]\n\t"
"v_mfma_f32_16x16x32_bf16 v[8:11], a[200:203], v[104:107], v[8:11]\n\t"
"v_mfma_f32_16x16x32_bf16 v[12:15], a[216:219], v[104:107], v[12:15]\n\t"
"v_mfma_f32_16x16x32_bf16 v[8:11], a[204:207], v[152:155], v[8:11]\n\t"
"v_mfma_f32_16x16x32_bf16 v[12:15], a[220:223], v[152:155], v[12:15]\n\t"
"v_mfma_f32_16x16x32_bf16 v[16:19], a[192:195], v[116:119], v[200:203]\n\t"
"v_mfma_f32_16x16x32_bf16 v[20:23], a[208:211], v[116:119], v[200:203]\n\t"
"v_mfma_f32_16x16x32_bf16 v[16:19], a[196:199], v[164:167], v[16:19]\n\t"
"v_mfma_f32_16x16x32_bf16 v[20:23], a[212:215], v[164:167], v[20:23]\n\t"
"v_mfma_f32_16x16x32_bf16 v[16:19], a[200:203], v[112:115], v[16:19]\n\t"
"v_mfma_f32_16x16x32_bf16 v[20:23], a[216:219], v[112:115], v[20:23]\n\t"
"v_mfma_f32_16x16x32_bf16 v[16:19], a[204:207], v[160:163], v[16:19]\n\t"
"v_mfma_f32_16x16x32_bf16 v[20:23], a[220:223], v[160:163], v[20:23]\n\t"
"v_mfma_f32_16x16x32_bf16 v[24:27], a[192:195], v[124:127], v[204:207]\n\t"
"v_mfma_f32_16x16x32_bf16 v[28:31], a[208:211], v[124:127], v[204:207]\n\t"
"v_mfma_f32_16x16x32_bf16 v[24:27], a[196:199], v[172:175], v[24:27]\n\t"
"v_mfma_f32_16x16x32_bf16 v[28:31], a[212:215], v[172:175], v[28:31]\n\t"
"v_mfma_f32_16x16x32_bf16 v[24:27], a[200:203], v[120:123], v[24:27]\n\t"
"v_mfma_f32_16x16x32_bf16 v[28:31], a[216:219], v[120:123], v[28:31]\n\t"
"v_mfma_f32_16x16x32_bf16 v[24:27], a[204:207], v[168:171], v[24:27]\n\t"
"v_mfma_f32_16x16x32_bf16 v[28:31], a[220:223], v[168:171], v[28:31]\n\t"
"v_mfma_f32_16x16x32_bf16 v[32:35], a[192:195], v[132:135], v[208:211]\n\t"
"v_mfma_f32_16x16x32_bf16 v[36:39], a[208:211], v[132:135], v[208:211]\n\t"
"v_mfma_f32_16x16x32_bf16 v[32:35], a[196:199], v[180:183], v[32:35]\n\t"
"v_mfma_f32_16x16x32_bf16 v[36:39], a[212:215], v[180:183], v[36:39]\n\t"
"v_mfma_f32_16x16x32_bf16 v[32:35], a[200:203], v[128:131], v[32:35]\n\t"
"v_mfma_f32_16x16x32_bf16 v[36:39], a[216:219], v[128:131], v[36:39]\n\t"
"v_mfma_f32_16x16x32_bf16 v[32:35], a[204:207], v[176:179], v[32:35]\n\t"
"v_mfma_f32_16x16x32_bf16 v[36:39], a[220:223], v[176:179], v[36:39]\n\t"
"v_mfma_f32_16x16x32_bf16 v[40:43], a[192:195], v[140:143], v[212:215]\n\t"
"v_mfma_f32_16x16x32_bf16 v[44:47], a[208:211], v[140:143], v[212:215]\n\t"
"v_mfma_f32_16x16x32_bf16 v[40:43], a[196:199], v[188:191], v[40:43]\n\t"
"v_mfma_f32_16x16x32_bf16 v[44:47], a[212:215], v[188:191], v[44:47]\n\t"
"v_mfma_f32_16x16x32_bf16 v[40:43], a[200:203], v[136:139], v[40:43]\n\t"
"v_mfma_f32_16x16x32_bf16 v[44:47], a[216:219], v[136:139], v[44:47]\n\t"
"v_mfma_f32_16x16x32_bf16 v[40:43], a[204:207], v[184:187], v[40:43]\n\t"
"v_mfma_f32_16x16x32_bf16 v[44:47], a[220:223], v[184:187], v[44:47]\n\t"
"s_nop 7\n\t"
"v_max3_f32 v233, v0, v1, v2\n\t"
"v_max3_f32 v234, v3, v4, v5\n\t"
"v_max3_f32 v233, v233, v6, v7\n\t"
"v_max_f32_e32 v233, v233, v234\n\t"
"v_mov_b32_e32 v234, v233\n\t"
"s_nop 1\n\t"
"v_permlane16_swap_b32_e32 v233, v234\n\t"
"s_nop 0\n\t"
"v_max_f32_e32 v233, v233, v234\n\t"
"v_mov_b32_e32 v234, v233\n\t"
"s_nop 1\n\t"
"v_permlane32_swap_b32_e32 v233, v234\n\t"
"s_nop 0\n\t"
"v_max3_f32 v233, v233, v234, 0\n\t"
"v_sub_f32_e32 v0, v0, v233\n\t"
"v_sub_f32_e32 v1, v1, v233\n\t"
"v_sub_f32_e32 v2, v2, v233\n\t"
"v_sub_f32_e32 v3, v3, v233\n\t"
"v_sub_f32_e32 v4, v4, v233\n\t"
"v_sub_f32_e32 v5, v5, v233\n\t"
"v_sub_f32_e32 v6, v6, v233\n\t"
"v_sub_f32_e32 v7, v7, v233\n\t"
"v_sub_f32_e32 v48, v48, v233\n\t"
"v_sub_f32_e32 v49, v49, v233\n\t"
"v_sub_f32_e32 v50, v50, v233\n\t"
"v_sub_f32_e32 v51, v51, v233\n\t"
"v_sub_f32_e32 v52, v52, v233\n\t"
"v_sub_f32_e32 v53, v53, v233\n\t"
"v_sub_f32_e32 v54, v54, v233\n\t"
"v_sub_f32_e32 v55, v55, v233\n\t"
"v_sub_f32_e32 v192, v192, v233\n\t"
"v_sub_f32_e32 v193, v193, v233\n\t"
"v_sub_f32_e32 v194, v194, v233\n\t"
"v_sub_f32_e32 v195, v195, v233\n\t"
"v_exp_f32_e64 v235, -v233\n\t"
"v_exp_f32_e32 v0, v0\n\t"
"v_exp_f32_e32 v1, v1\n\t"
"v_exp_f32_e32 v2, v2\n\t"
"v_exp_f32_e32 v3, v3\n\t"
"v_exp_f32_e32 v4, v4\n\t"
"v_exp_f32_e32 v5, v5\n\t"
"v_exp_f32_e32 v6, v6\n\t"
"v_exp_f32_e32 v7, v7\n\t"
"v_mul_f32_e64 v227, v227, v235\n\t"
"v_cvt_pk_bf16_f32 v0, v0, v1\n\t"
"v_cvt_pk_bf16_f32 v1, v2, v3\n\t"
"v_cvt_pk_bf16_f32 v2, v4, v5\n\t"
"v_cvt_pk_bf16_f32 v3, v6, v7\n\t"
"v_max3_f32 v233, v8, v9, v10\n\t"
"v_max3_f32 v234, v11, v12, v13\n\t"
"v_max3_f32 v233, v233, v14, v15\n\t"
"v_max_f32_e32 v233, v233, v234\n\t"
"v_mov_b32_e32 v234, v233\n\t"
"s_nop 1\n\t"
"v_permlane16_swap_b32_e32 v233, v234\n\t"
"s_nop 0\n\t"
"v_max_f32_e32 v233, v233, v234\n\t"
"v_mov_b32_e32 v234, v233\n\t"
"s_nop 1\n\t"
"v_permlane32_swap_b32_e32 v233, v234\n\t"
"s_nop 0\n\t"
"v_max3_f32 v233, v233, v234, 0\n\t"
"v_sub_f32_e32 v8, v8, v233\n\t"
"v_sub_f32_e32 v9, v9, v233\n\t"
"v_sub_f32_e32 v10, v10, v233\n\t"
"v_sub_f32_e32 v11, v11, v233\n\t"
"v_sub_f32_e32 v12, v12, v233\n\t"
"v_sub_f32_e32 v13, v13, v233\n\t"
"v_sub_f32_e32 v14, v14, v233\n\t"
"v_sub_f32_e32 v15, v15, v233\n\t"
"v_sub_f32_e32 v56, v56, v233\n\t"
"v_sub_f32_e32 v57, v57, v233\n\t"
"v_sub_f32_e32 v58, v58, v233\n\t"
"v_sub_f32_e32 v59, v59, v233\n\t"
"v_sub_f32_e32 v60, v60, v233\n\t"
"v_sub_f32_e32 v61, v61, v233\n\t"
"v_sub_f32_e32 v62, v62, v233\n\t"
"v_sub_f32_e32 v63, v63, v233\n\t"
"v_sub_f32_e32 v196, v196, v233\n\t"
"v_sub_f32_e32 v197, v197, v233\n\t"
"v_sub_f32_e32 v198, v198, v233\n\t"
"v_sub_f32_e32 v199, v199, v233\n\t"
"v_exp_f32_e64 v236, -v233\n\t"
"v_exp_f32_e32 v8, v8\n\t"
"v_exp_f32_e32 v9, v9\n\t"
"v_exp_f32_e32 v10, v10\n\t"
"v_exp_f32_e32 v11, v11\n\t"
"v_exp_f32_e32 v12, v12\n\t"
"v_exp_f32_e32 v13, v13\n\t"
"v_exp_f32_e32 v14, v14\n\t"
"v_exp_f32_e32 v15, v15\n\t"
"v_mul_f32_e64 v228, v228, v236\n\t"
"v_cvt_pk_bf16_f32 v8, v8, v9\n\t"
"v_cvt_pk_bf16_f32 v9, v10, v11\n\t"
"v_cvt_pk_bf16_f32 v10, v12, v13\n\t"
"v_cvt_pk_bf16_f32 v11, v14, v15\n\t"
"v_max3_f32 v233, v16, v17, v18\n\t"
"v_max3_f32 v234, v19, v20, v21\n\t"
"v_max3_f32 v233, v233, v22, v23\n\t"
"v_max_f32_e32 v233, v233, v234\n\t"
"v_mov_b32_e32 v234, v233\n\t"
"s_nop 1\n\t"
"v_permlane16_swap_b32_e32 v233, v234\n\t"
"s_nop 0\n\t"
"v_max_f32_e32 v233, v233, v234\n\t"
"v_mov_b32_e32 v234, v233\n\t"
"s_nop 1\n\t"
"v_permlane32_swap_b32_e32 v233, v234\n\t"
"s_nop 0\n\t"
"v_max3_f32 v233, v233, v234, 0\n\t"
"v_sub_f32_e32 v16, v16, v233\n\t"
"v_sub_f32_e32 v17, v17, v233\n\t"
"v_sub_f32_e32 v18, v18, v233\n\t"
"v_sub_f32_e32 v19, v19, v233\n\t"
"v_sub_f32_e32 v20, v20, v233\n\t"
"v_sub_f32_e32 v21, v21, v233\n\t"
"v_sub_f32_e32 v22, v22, v233\n\t"
"v_sub_f32_e32 v23, v23, v233\n\t"
"v_sub_f32_e32 v64, v64, v233\n\t"
"v_sub_f32_e32 v65, v65, v233\n\t"
"v_sub_f32_e32 v66, v66, v233\n\t"
"v_sub_f32_e32 v67, v67, v233\n\t"
"v_sub_f32_e32 v68, v68, v233\n\t"
"v_sub_f32_e32 v69, v69, v233\n\t"
"v_sub_f32_e32 v70, v70, v233\n\t"
"v_sub_f32_e32 v71, v71, v233\n\t"
"v_sub_f32_e32 v200, v200, v233\n\t"
"v_sub_f32_e32 v201, v201, v233\n\t"
"v_sub_f32_e32 v202, v202, v233\n\t"
"v_sub_f32_e32 v203, v203, v233\n\t"
"v_exp_f32_e64 v237, -v233\n\t"
"v_exp_f32_e32 v16, v16\n\t"
"v_exp_f32_e32 v17, v17\n\t"
"v_exp_f32_e32 v18, v18\n\t"
"v_exp_f32_e32 v19, v19\n\t"
"v_exp_f32_e32 v20, v20\n\t"
"v_exp_f32_e32 v21, v21\n\t"
"v_exp_f32_e32 v22, v22\n\t"
"v_exp_f32_e32 v23, v23\n\t"
"v_mul_f32_e64 v229, v229, v237\n\t"
"v_cvt_pk_bf16_f32 v16, v16, v17\n\t"
"v_cvt_pk_bf16_f32 v17, v18, v19\n\t"
"v_cvt_pk_bf16_f32 v18, v20, v21\n\t"
"v_cvt_pk_bf16_f32 v19, v22, v23\n\t"
"v_max3_f32 v233, v24, v25, v26\n\t"
"v_max3_f32 v234, v27, v28, v29\n\t"
"v_max3_f32 v233, v233, v30, v31\n\t"
"v_max_f32_e32 v233, v233, v234\n\t"
"v_mov_b32_e32 v234, v233\n\t"
"s_nop 1\n\t"
"v_permlane16_swap_b32_e32 v233, v234\n\t"
"s_nop 0\n\t"
"v_max_f32_e32 v233, v233, v234\n\t"
"v_mov_b32_e32 v234, v233\n\t"
"s_nop 1\n\t"
"v_permlane32_swap_b32_e32 v233, v234\n\t"
"s_nop 0\n\t"
"v_max3_f32 v233, v233, v234, 0\n\t"
"v_sub_f32_e32 v24, v24, v233\n\t"
"v_sub_f32_e32 v25, v25, v233\n\t"
"v_sub_f32_e32 v26, v26, v233\n\t"
"v_sub_f32_e32 v27, v27, v233\n\t"
"v_sub_f32_e32 v28, v28, v233\n\t"
"v_sub_f32_e32 v29, v29, v233\n\t"
"v_sub_f32_e32 v30, v30, v233\n\t"
"v_sub_f32_e32 v31, v31, v233\n\t"
"v_sub_f32_e32 v72, v72, v233\n\t"
"v_sub_f32_e32 v73, v73, v233\n\t"
"v_sub_f32_e32 v74, v74, v233\n\t"
"v_sub_f32_e32 v75, v75, v233\n\t"
"v_sub_f32_e32 v76, v76, v233\n\t"
"v_sub_f32_e32 v77, v77, v233\n\t"
"v_sub_f32_e32 v78, v78, v233\n\t"
"v_sub_f32_e32 v79, v79, v233\n\t"
"v_sub_f32_e32 v204, v204, v233\n\t"
"v_sub_f32_e32 v205, v205, v233\n\t"
"v_sub_f32_e32 v206, v206, v233\n\t"
"v_sub_f32_e32 v207, v207, v233\n\t"
"v_exp_f32_e64 v238, -v233\n\t"
"v_exp_f32_e32 v24, v24\n\t"
"v_exp_f32_e32 v25, v25\n\t"
"v_exp_f32_e32 v26, v26\n\t"
"v_exp_f32_e32 v27, v27\n\t"
"v_exp_f32_e32 v28, v28\n\t"
"v_exp_f32_e32 v29, v29\n\t"
"v_exp_f32_e32 v30, v30\n\t"
"v_exp_f32_e32 v31, v31\n\t"
"v_mul_f32_e64 v230, v230, v238\n\t"
"v_cvt_pk_bf16_f32 v24, v24, v25\n\t"
"v_cvt_pk_bf16_f32 v25, v26, v27\n\t"
"v_cvt_pk_bf16_f32 v26, v28, v29\n\t"
"v_cvt_pk_bf16_f32 v27, v30, v31\n\t"
"v_max3_f32 v233, v32, v33, v34\n\t"
"v_max3_f32 v234, v35, v36, v37\n\t"
"v_max3_f32 v233, v233, v38, v39\n\t"
"v_max_f32_e32 v233, v233, v234\n\t"
"v_mov_b32_e32 v234, v233\n\t"
"s_nop 1\n\t"
"v_permlane16_swap_b32_e32 v233, v234\n\t"
"s_nop 0\n\t"
"v_max_f32_e32 v233, v233, v234\n\t"
"v_mov_b32_e32 v234, v233\n\t"
"s_nop 1\n\t"
"v_permlane32_swap_b32_e32 v233, v234\n\t"
"s_nop 0\n\t"
"v_max3_f32 v233, v233, v234, 0\n\t"
"v_sub_f32_e32 v32, v32, v233\n\t"
"v_sub_f32_e32 v33, v33, v233\n\t"
"v_sub_f32_e32 v34, v34, v233\n\t"
"v_sub_f32_e32 v35, v35, v233\n\t"
"v_sub_f32_e32 v36, v36, v233\n\t"
"v_sub_f32_e32 v37, v37, v233\n\t"
"v_sub_f32_e32 v38, v38, v233\n\t"
"v_sub_f32_e32 v39, v39, v233\n\t"
"v_sub_f32_e32 v80, v80, v233\n\t"
"v_sub_f32_e32 v81, v81, v233\n\t"
"v_sub_f32_e32 v82, v82, v233\n\t"
"v_sub_f32_e32 v83, v83, v233\n\t"
"v_sub_f32_e32 v84, v84, v233\n\t"
"v_sub_f32_e32 v85, v85, v233\n\t"
"v_sub_f32_e32 v86, v86, v233\n\t"
"v_sub_f32_e32 v87, v87, v233\n\t"
"v_sub_f32_e32 v208, v208, v233\n\t"
"v_sub_f32_e32 v209, v209, v233\n\t"
"v_sub_f32_e32 v210, v210, v233\n\t"
"v_sub_f32_e32 v211, v211, v233\n\t"
"v_exp_f32_e64 v239, -v233\n\t"
"v_exp_f32_e32 v32, v32\n\t"
"v_exp_f32_e32 v33, v33\n\t"
"v_exp_f32_e32 v34, v34\n\t"
"v_exp_f32_e32 v35, v35\n\t"
"v_exp_f32_e32 v36, v36\n\t"
"v_exp_f32_e32 v37, v37\n\t"
"v_exp_f32_e32 v38, v38\n\t"
"v_exp_f32_e32 v39, v39\n\t"
"v_mul_f32_e64 v231, v231, v239\n\t"
"v_cvt_pk_bf16_f32 v32, v32, v33\n\t"
"v_cvt_pk_bf16_f32 v33, v34, v35\n\t"
"v_cvt_pk_bf16_f32 v34, v36, v37\n\t"
"v_cvt_pk_bf16_f32 v35, v38, v39\n\t"
"v_max3_f32 v233, v40, v41, v42\n\t"
"v_max3_f32 v234, v43, v44, v45\n\t"
"v_max3_f32 v233, v233, v46, v47\n\t"
"v_max_f32_e32 v233, v233, v234\n\t"
"v_mov_b32_e32 v234, v233\n\t"
"s_nop 1\n\t"
"v_permlane16_swap_b32_e32 v233, v234\n\t"
"s_nop 0\n\t"
"v_max_f32_e32 v233, v233, v234\n\t"
"v_mov_b32_e32 v234, v233\n\t"
"s_nop 1\n\t"
"v_permlane32_swap_b32_e32 v233, v234\n\t"
"s_nop 0\n\t"
"v_max3_f32 v233, v233, v234, 0\n\t"
"v_sub_f32_e32 v40, v40, v233\n\t"
"v_sub_f32_e32 v41, v41, v233\n\t"
"v_sub_f32_e32 v42, v42, v233\n\t"
"v_sub_f32_e32 v43, v43, v233\n\t"
"v_sub_f32_e32 v44, v44, v233\n\t"
"v_sub_f32_e32 v45, v45, v233\n\t"
"v_sub_f32_e32 v46, v46, v233\n\t"
"v_sub_f32_e32 v47, v47, v233\n\t"
"v_sub_f32_e32 v88, v88, v233\n\t"
"v_sub_f32_e32 v89, v89, v233\n\t"
"v_sub_f32_e32 v90, v90, v233\n\t"
"v_sub_f32_e32 v91, v91, v233\n\t"
"v_sub_f32_e32 v92, v92, v233\n\t"
"v_sub_f32_e32 v93, v93, v233\n\t"
"v_sub_f32_e32 v94, v94, v233\n\t"
"v_sub_f32_e32 v95, v95, v233\n\t"
"v_sub_f32_e32 v212, v212, v233\n\t"
"v_sub_f32_e32 v213, v213, v233\n\t"
"v_sub_f32_e32 v214, v214, v233\n\t"
"v_sub_f32_e32 v215, v215, v233\n\t"
"v_exp_f32_e64 v240, -v233\n\t"
"v_exp_f32_e32 v40, v40\n\t"
"v_exp_f32_e32 v41, v41\n\t"
"v_exp_f32_e32 v42, v42\n\t"
"v_exp_f32_e32 v43, v43\n\t"
"v_exp_f32_e32 v44, v44\n\t"
"v_exp_f32_e32 v45, v45\n\t"
"v_exp_f32_e32 v46, v46\n\t"
"v_exp_f32_e32 v47, v47\n\t"
"v_mul_f32_e64 v232, v232, v240\n\t"
"v_cvt_pk_bf16_f32 v40, v40, v41\n\t"
"v_cvt_pk_bf16_f32 v41, v42, v43\n\t"
"v_cvt_pk_bf16_f32 v42, v44, v45\n\t"
"v_cvt_pk_bf16_f32 v43, v46, v47\n\t"
"v_mfma_f32_16x16x32_bf16 v[4:7], v[216:219], v[0:3], 0\n\t"
"v_mfma_f32_16x16x32_bf16 v[12:15], v[216:219], v[8:11], 0\n\t"
"v_mfma_f32_16x16x32_bf16 v[20:23], v[216:219], v[16:19], 0\n\t"
"v_mfma_f32_16x16x32_bf16 v[28:31], v[216:219], v[24:27], 0\n\t"
"v_mfma_f32_16x16x32_bf16 v[36:39], v[216:219], v[32:35], 0\n\t"
"v_mfma_f32_16x16x32_bf16 v[44:47], v[216:219], v[40:43], 0\n\t"
"s_branch RESC%=\n\t"
"RC1%=:\n\t"
"v_and_b32_e32 v233, 15, v252\n\t"
"v_lshlrev_b32_e32 v233, 8, v233\n\t"
"v_lshrrev_b32_e32 v234, 4, v252\n\t"
"v_lshl_add_u32 v233, v234, 4, v233\n\t"
"s_sub_u32 s61, s59, 1\n\t"
"s_lshl_b32 s61, s61, 13\n\t"
"v_add_u32_e64 v233, s61, v233\n\t"
"v_add_u32_e32 v234, 0x1000, v233\n\t"
"global_load_dwordx4 a[192:195], v233, %19 offset:0\n\t"
"global_load_dwordx4 a[196:199], v233, %19 offset:64\n\t"
"global_load_dwordx4 a[200:203], v233, %19 offset:128\n\t"
"global_load_dwordx4 a[204:207], v233, %19 offset:192\n\t"
"global_load_dwordx4 a[208:211], v234, %19 offset:0\n\t"
"global_load_dwordx4 a[212:215], v234, %19 offset:64\n\t"
"global_load_dwordx4 a[216:219], v234, %19 offset:128\n\t"
"global_load_dwordx4 a[220:223], v234, %19 offset:192\n\t"
"s_waitcnt vmcnt(0)\n\t"
"v_mfma_f32_16x16x32_bf16 v[48:51], a[192:195], v[100:103], v[192:195]\n\t"
"v_mfma_f32_16x16x32_bf16 v[52:55], a[208:211], v[100:103], v[192:195]\n\t"
"v_mfma_f32_16x16x32_bf16 v[48:51], a[196:199], v[148:151], v[48:51]\n\t"
"v_mfma_f32_16x16x32_bf16 v[52:55], a[212:215], v[148:151], v[52:55]\n\t"
"v_mfma_f32_16x16x32_bf16 v[48:51], a[200:203], v[96:99], v[48:51]\n\t"
"v_mfma_f32_16x16x32_bf16 v[52:55], a[216:219], v[96:99], v[52:55]\n\t"
"v_mfma_f32_16x16x32_bf16 v[48:51], a[204:207], v[144:147], v[48:51]\n\t"
"v_mfma_f32_16x16x32_bf16 v[52:55], a[220:223], v[144:147], v[52:55]\n\t"
"v_mfma_f32_16x16x32_bf16 v[56:59], a[192:195], v[108:111], v[196:199]\n\t"
"v_mfma_f32_16x16x32_bf16 v[60:63], a[208:211], v[108:111], v[196:199]\n\t"
"v_mfma_f32_16x16x32_bf16 v[56:59], a[196:199], v[156:159], v[56:59]\n\t"
"v_mfma_f32_16x16x32_bf16 v[60:63], a[212:215], v[156:159], v[60:63]\n\t"
"v_mfma_f32_16x16x32_bf16 v[56:59], a[200:203], v[104:107], v[56:59]\n\t"
"v_mfma_f32_16x16x32_bf16 v[60:63], a[216:219], v[104:107], v[60:63]\n\t"
"v_mfma_f32_16x16x32_bf16 v[56:59], a[204:207], v[152:155], v[56:59]\n\t"
"v_mfma_f32_16x16x32_bf16 v[60:63], a[220:223], v[152:155], v[60:63]\n\t"
"v_mfma_f32_16x16x32_bf16 v[64:67], a[192:195], v[116:119], v[200:203]\n\t"
"v_mfma_f32_16x16x32_bf16 v[68:71], a[208:211], v[116:119], v[200:203]\n\t"
"v_mfma_f32_16x16x32_bf16 v[64:67], a[196:199], v[164:167], v[64:67]\n\t"
"v_mfma_f32_16x16x32_bf16 v[68:71], a[212:215], v[164:167], v[68:71]\n\t"
"v_mfma_f32_16x16x32_bf16 v[64:67], a[200:203], v[112:115], v[64:67]\n\t"
"v_mfma_f32_16x16x32_bf16 v[68:71], a[216:219], v[112:115], v[68:71]\n\t"
"v_mfma_f32_16x16x32_bf16 v[64:67], a[204:207], v[160:163], v[64:67]\n\t"
"v_mfma_f32_16x16x32_bf16 v[68:71], a[220:223], v[160:163], v[68:71]\n\t"
"v_mfma_f32_16x16x32_bf16 v[72:75], a[192:195], v[124:127], v[204:207]\n\t"
"v_mfma_f32_16x16x32_bf16 v[76:79], a[208:211], v[124:127], v[204:207]\n\t"
"v_mfma_f32_16x16x32_bf16 v[72:75], a[196:199], v[172:175], v[72:75]\n\t"
"v_mfma_f32_16x16x32_bf16 v[76:79], a[212:215], v[172:175], v[76:79]\n\t"
"v_mfma_f32_16x16x32_bf16 v[72:75], a[200:203], v[120:123], v[72:75]\n\t"
"v_mfma_f32_16x16x32_bf16 v[76:79], a[216:219], v[120:123], v[76:79]\n\t"
"v_mfma_f32_16x16x32_bf16 v[72:75], a[204:207], v[168:171], v[72:75]\n\t"
"v_mfma_f32_16x16x32_bf16 v[76:79], a[220:223], v[168:171], v[76:79]\n\t"
"v_mfma_f32_16x16x32_bf16 v[80:83], a[192:195], v[132:135], v[208:211]\n\t"
"v_mfma_f32_16x16x32_bf16 v[84:87], a[208:211], v[132:135], v[208:211]\n\t"
"v_mfma_f32_16x16x32_bf16 v[80:83], a[196:199], v[180:183], v[80:83]\n\t"
"v_mfma_f32_16x16x32_bf16 v[84:87], a[212:215], v[180:183], v[84:87]\n\t"
"v_mfma_f32_16x16x32_bf16 v[80:83], a[200:203], v[128:131], v[80:83]\n\t"
"v_mfma_f32_16x16x32_bf16 v[84:87], a[216:219], v[128:131], v[84:87]\n\t"
"v_mfma_f32_16x16x32_bf16 v[80:83], a[204:207], v[176:179], v[80:83]\n\t"
"v_mfma_f32_16x16x32_bf16 v[84:87], a[220:223], v[176:179], v[84:87]\n\t"
"v_mfma_f32_16x16x32_bf16 v[88:91], a[192:195], v[140:143], v[212:215]\n\t"
"v_mfma_f32_16x16x32_bf16 v[92:95], a[208:211], v[140:143], v[212:215]\n\t"
"v_mfma_f32_16x16x32_bf16 v[88:91], a[196:199], v[188:191], v[88:91]\n\t"
"v_mfma_f32_16x16x32_bf16 v[92:95], a[212:215], v[188:191], v[92:95]\n\t"
"v_mfma_f32_16x16x32_bf16 v[88:91], a[200:203], v[136:139], v[88:91]\n\t"
"v_mfma_f32_16x16x32_bf16 v[92:95], a[216:219], v[136:139], v[92:95]\n\t"
"v_mfma_f32_16x16x32_bf16 v[88:91], a[204:207], v[184:187], v[88:91]\n\t"
"v_mfma_f32_16x16x32_bf16 v[92:95], a[220:223], v[184:187], v[92:95]\n\t"
"s_nop 7\n\t"
"v_max3_f32 v233, v48, v49, v50\n\t"
"v_max3_f32 v234, v51, v52, v53\n\t"
"v_max3_f32 v233, v233, v54, v55\n\t"
"v_max_f32_e32 v233, v233, v234\n\t"
"v_mov_b32_e32 v234, v233\n\t"
"s_nop 1\n\t"
"v_permlane16_swap_b32_e32 v233, v234\n\t"
"s_nop 0\n\t"
"v_max_f32_e32 v233, v233, v234\n\t"
"v_mov_b32_e32 v234, v233\n\t"
"s_nop 1\n\t"
"v_permlane32_swap_b32_e32 v233, v234\n\t"
"s_nop 0\n\t"
"v_max3_f32 v233, v233, v234, 0\n\t"
"v_sub_f32_e32 v48, v48, v233\n\t"
"v_sub_f32_e32 v49, v49, v233\n\t"
"v_sub_f32_e32 v50, v50, v233\n\t"
"v_sub_f32_e32 v51, v51, v233\n\t"
"v_sub_f32_e32 v52, v52, v233\n\t"
"v_sub_f32_e32 v53, v53, v233\n\t"
"v_sub_f32_e32 v54, v54, v233\n\t"
"v_sub_f32_e32 v55, v55, v233\n\t"
"v_sub_f32_e32 v0, v0, v233\n\t"
"v_sub_f32_e32 v1, v1, v233\n\t"
"v_sub_f32_e32 v2, v2, v233\n\t"
"v_sub_f32_e32 v3, v3, v233\n\t"
"v_sub_f32_e32 v4, v4, v233\n\t"
"v_sub_f32_e32 v5, v5, v233\n\t"
"v_sub_f32_e32 v6, v6, v233\n\t"
"v_sub_f32_e32 v7, v7, v233\n\t"
"v_sub_f32_e32 v192, v192, v233\n\t"
"v_sub_f32_e32 v193, v193, v233\n\t"
"v_sub_f32_e32 v194, v194, v233\n\t"
"v_sub_f32_e32 v195, v195, v233\n\t"
"v_exp_f32_e64 v235, -v233\n\t"
"v_exp_f32_e32 v48, v48\n\t"
"v_exp_f32_e32 v49, v49\n\t"
"v_exp_f32_e32 v50, v50\n\t"
"v_exp_f32_e32 v51, v51\n\t"
"v_exp_f32_e32 v52, v52\n\t"
"v_exp_f32_e32 v53, v53\n\t"
"v_exp_f32_e32 v54, v54\n\t"
"v_exp_f32_e32 v55, v55\n\t"
"v_mul_f32_e64 v227, v227, v235\n\t"
"v_cvt_pk_bf16_f32 v48, v48, v49\n\t"
"v_cvt_pk_bf16_f32 v49, v50, v51\n\t"
"v_cvt_pk_bf16_f32 v50, v52, v53\n\t"
"v_cvt_pk_bf16_f32 v51, v54, v55\n\t"
"v_max3_f32 v233, v56, v57, v58\n\t"
"v_max3_f32 v234, v59, v60, v61\n\t"
"v_max3_f32 v233, v233, v62, v63\n\t"
"v_max_f32_e32 v233, v233, v234\n\t"
"v_mov_b32_e32 v234, v233\n\t"
"s_nop 1\n\t"
"v_permlane16_swap_b32_e32 v233, v234\n\t"
"s_nop 0\n\t"
"v_max_f32_e32 v233, v233, v234\n\t"
"v_mov_b32_e32 v234, v233\n\t"
"s_nop 1\n\t"
"v_permlane32_swap_b32_e32 v233, v234\n\t"
"s_nop 0\n\t"
"v_max3_f32 v233, v233, v234, 0\n\t"
"v_sub_f32_e32 v56, v56, v233\n\t"
"v_sub_f32_e32 v57, v57, v233\n\t"
"v_sub_f32_e32 v58, v58, v233\n\t"
"v_sub_f32_e32 v59, v59, v233\n\t"
"v_sub_f32_e32 v60, v60, v233\n\t"
"v_sub_f32_e32 v61, v61, v233\n\t"
"v_sub_f32_e32 v62, v62, v233\n\t"
"v_sub_f32_e32 v63, v63, v233\n\t"
"v_sub_f32_e32 v8, v8, v233\n\t"
"v_sub_f32_e32 v9, v9, v233\n\t"
"v_sub_f32_e32 v10, v10, v233\n\t"
"v_sub_f32_e32 v11, v11, v233\n\t"
"v_sub_f32_e32 v12, v12, v233\n\t"
"v_sub_f32_e32 v13, v13, v233\n\t"
"v_sub_f32_e32 v14, v14, v233\n\t"
"v_sub_f32_e32 v15, v15, v233\n\t"
"v_sub_f32_e32 v196, v196, v233\n\t"
"v_sub_f32_e32 v197, v197, v233\n\t"
"v_sub_f32_e32 v198, v198, v233\n\t"
"v_sub_f32_e32 v199, v199, v233\n\t"
"v_exp_f32_e64 v236, -v233\n\t"
"v_exp_f32_e32 v56, v56\n\t"
"v_exp_f32_e32 v57, v57\n\t"
"v_exp_f32_e32 v58, v58\n\t"
"v_exp_f32_e32 v59, v59\n\t"
"v_exp_f32_e32 v60, v60\n\t"
"v_exp_f32_e32 v61, v61\n\t"
"v_exp_f32_e32 v62, v62\n\t"
"v_exp_f32_e32 v63, v63\n\t"
"v_mul_f32_e64 v228, v228, v236\n\t"
"v_cvt_pk_bf16_f32 v56, v56, v57\n\t"
"v_cvt_pk_bf16_f32 v57, v58, v59\n\t"
"v_cvt_pk_bf16_f32 v58, v60, v61\n\t"
"v_cvt_pk_bf16_f32 v59, v62, v63\n\t"
"v_max3_f32 v233, v64, v65, v66\n\t"
"v_max3_f32 v234, v67, v68, v69\n\t"
"v_max3_f32 v233, v233, v70, v71\n\t"
"v_max_f32_e32 v233, v233, v234\n\t"
"v_mov_b32_e32 v234, v233\n\t"
"s_nop 1\n\t"
"v_permlane16_swap_b32_e32 v233, v234\n\t"
"s_nop 0\n\t"
"v_max_f32_e32 v233, v233, v234\n\t"
"v_mov_b32_e32 v234, v233\n\t"
"s_nop 1\n\t"
"v_permlane32_swap_b32_e32 v233, v234\n\t"
"s_nop 0\n\t"
"v_max3_f32 v233, v233, v234, 0\n\t"
"v_sub_f32_e32 v64, v64, v233\n\t"
"v_sub_f32_e32 v65, v65, v233\n\t"
"v_sub_f32_e32 v66, v66, v233\n\t"
"v_sub_f32_e32 v67, v67, v233\n\t"
"v_sub_f32_e32 v68, v68, v233\n\t"
"v_sub_f32_e32 v69, v69, v233\n\t"
"v_sub_f32_e32 v70, v70, v233\n\t"
"v_sub_f32_e32 v71, v71, v233\n\t"
"v_sub_f32_e32 v16, v16, v233\n\t"
"v_sub_f32_e32 v17, v17, v233\n\t"
"v_sub_f32_e32 v18, v18, v233\n\t"
"v_sub_f32_e32 v19, v19, v233\n\t"
"v_sub_f32_e32 v20, v20, v233\n\t"
"v_sub_f32_e32 v21, v21, v233\n\t"
"v_sub_f32_e32 v22, v22, v233\n\t"
"v_sub_f32_e32 v23, v23, v233\n\t"
"v_sub_f32_e32 v200, v200, v233\n\t"
"v_sub_f32_e32 v201, v201, v233\n\t"
"v_sub_f32_e32 v202, v202, v233\n\t"
"v_sub_f32_e32 v203, v203, v233\n\t"
"v_exp_f32_e64 v237, -v233\n\t"
"v_exp_f32_e32 v64, v64\n\t"
"v_exp_f32_e32 v65, v65\n\t"
"v_exp_f32_e32 v66, v66\n\t"
"v_exp_f32_e32 v67, v67\n\t"
"v_exp_f32_e32 v68, v68\n\t"
"v_exp_f32_e32 v69, v69\n\t"
"v_exp_f32_e32 v70, v70\n\t"
"v_exp_f32_e32 v71, v71\n\t"
"v_mul_f32_e64 v229, v229, v237\n\t"
"v_cvt_pk_bf16_f32 v64, v64, v65\n\t"
"v_cvt_pk_bf16_f32 v65, v66, v67\n\t"
"v_cvt_pk_bf16_f32 v66, v68, v69\n\t"
"v_cvt_pk_bf16_f32 v67, v70, v71\n\t"
"v_max3_f32 v233, v72, v73, v74\n\t"
"v_max3_f32 v234, v75, v76, v77\n\t"
"v_max3_f32 v233, v233, v78, v79\n\t"
"v_max_f32_e32 v233, v233, v234\n\t"
"v_mov_b32_e32 v234, v233\n\t"
"s_nop 1\n\t"
"v_permlane16_swap_b32_e32 v233, v234\n\t"
"s_nop 0\n\t"
"v_max_f32_e32 v233, v233, v234\n\t"
"v_mov_b32_e32 v234, v233\n\t"
"s_nop 1\n\t"
"v_permlane32_swap_b32_e32 v233, v234\n\t"
"s_nop 0\n\t"
"v_max3_f32 v233, v233, v234, 0\n\t"
"v_sub_f32_e32 v72, v72, v233\n\t"
"v_sub_f32_e32 v73, v73, v233\n\t"
"v_sub_f32_e32 v74, v74, v233\n\t"
"v_sub_f32_e32 v75, v75, v233\n\t"
"v_sub_f32_e32 v76, v76, v233\n\t"
"v_sub_f32_e32 v77, v77, v233\n\t"
"v_sub_f32_e32 v78, v78, v233\n\t"
"v_sub_f32_e32 v79, v79, v233\n\t"
"v_sub_f32_e32 v24, v24, v233\n\t"
"v_sub_f32_e32 v25, v25, v233\n\t"
"v_sub_f32_e32 v26, v26, v233\n\t"
"v_sub_f32_e32 v27, v27, v233\n\t"
"v_sub_f32_e32 v28, v28, v233\n\t"
"v_sub_f32_e32 v29, v29, v233\n\t"
"v_sub_f32_e32 v30, v30, v233\n\t"
"v_sub_f32_e32 v31, v31, v233\n\t"
"v_sub_f32_e32 v204, v204, v233\n\t"
"v_sub_f32_e32 v205, v205, v233\n\t"
"v_sub_f32_e32 v206, v206, v233\n\t"
"v_sub_f32_e32 v207, v207, v233\n\t"
"v_exp_f32_e64 v238, -v233\n\t"
"v_exp_f32_e32 v72, v72\n\t"
"v_exp_f32_e32 v73, v73\n\t"
"v_exp_f32_e32 v74, v74\n\t"
"v_exp_f32_e32 v75, v75\n\t"
"v_exp_f32_e32 v76, v76\n\t"
"v_exp_f32_e32 v77, v77\n\t"
"v_exp_f32_e32 v78, v78\n\t"
"v_exp_f32_e32 v79, v79\n\t"
"v_mul_f32_e64 v230, v230, v238\n\t"
"v_cvt_pk_bf16_f32 v72, v72, v73\n\t"
"v_cvt_pk_bf16_f32 v73, v74, v75\n\t"
"v_cvt_pk_bf16_f32 v74, v76, v77\n\t"
"v_cvt_pk_bf16_f32 v75, v78, v79\n\t"
"v_max3_f32 v233, v80, v81, v82\n\t"
"v_max3_f32 v234, v83, v84, v85\n\t"
"v_max3_f32 v233, v233, v86, v87\n\t"
"v_max_f32_e32 v233, v233, v234\n\t"
"v_mov_b32_e32 v234, v233\n\t"
"s_nop 1\n\t"
"v_permlane16_swap_b32_e32 v233, v234\n\t"
"s_nop 0\n\t"
"v_max_f32_e32 v233, v233, v234\n\t"
"v_mov_b32_e32 v234, v233\n\t"
"s_nop 1\n\t"
"v_permlane32_swap_b32_e32 v233, v234\n\t"
"s_nop 0\n\t"
"v_max3_f32 v233, v233, v234, 0\n\t"
"v_sub_f32_e32 v80, v80, v233\n\t"
"v_sub_f32_e32 v81, v81, v233\n\t"
"v_sub_f32_e32 v82, v82, v233\n\t"
"v_sub_f32_e32 v83, v83, v233\n\t"
"v_sub_f32_e32 v84, v84, v233\n\t"
"v_sub_f32_e32 v85, v85, v233\n\t"
"v_sub_f32_e32 v86, v86, v233\n\t"
"v_sub_f32_e32 v87, v87, v233\n\t"
"v_sub_f32_e32 v32, v32, v233\n\t"
"v_sub_f32_e32 v33, v33, v233\n\t"
"v_sub_f32_e32 v34, v34, v233\n\t"
"v_sub_f32_e32 v35, v35, v233\n\t"
"v_sub_f32_e32 v36, v36, v233\n\t"
"v_sub_f32_e32 v37, v37, v233\n\t"
"v_sub_f32_e32 v38, v38, v233\n\t"
"v_sub_f32_e32 v39, v39, v233\n\t"
"v_sub_f32_e32 v208, v208, v233\n\t"
"v_sub_f32_e32 v209, v209, v233\n\t"
"v_sub_f32_e32 v210, v210, v233\n\t"
"v_sub_f32_e32 v211, v211, v233\n\t"
"v_exp_f32_e64 v239, -v233\n\t"
"v_exp_f32_e32 v80, v80\n\t"
"v_exp_f32_e32 v81, v81\n\t"
"v_exp_f32_e32 v82, v82\n\t"
"v_exp_f32_e32 v83, v83\n\t"
"v_exp_f32_e32 v84, v84\n\t"
"v_exp_f32_e32 v85, v85\n\t"
"v_exp_f32_e32 v86, v86\n\t"
"v_exp_f32_e32 v87, v87\n\t"
"v_mul_f32_e64 v231, v231, v239\n\t"
"v_cvt_pk_bf16_f32 v80, v80, v81\n\t"
"v_cvt_pk_bf16_f32 v81, v82, v83\n\t"
"v_cvt_pk_bf16_f32 v82, v84, v85\n\t"
"v_cvt_pk_bf16_f32 v83, v86, v87\n\t"
"v_max3_f32 v233, v88, v89, v90\n\t"
"v_max3_f32 v234, v91, v92, v93\n\t"
"v_max3_f32 v233, v233, v94, v95\n\t"
"v_max_f32_e32 v233, v233, v234\n\t"
"v_mov_b32_e32 v234, v233\n\t"
"s_nop 1\n\t"
"v_permlane16_swap_b32_e32 v233, v234\n\t"
"s_nop 0\n\t"
"v_max_f32_e32 v233, v233, v234\n\t"
"v_mov_b32_e32 v234, v233\n\t"
"s_nop 1\n\t"
"v_permlane32_swap_b32_e32 v233, v234\n\t"
"s_nop 0\n\t"
"v_max3_f32 v233, v233, v234, 0\n\t"
"v_sub_f32_e32 v88, v88, v233\n\t"
"v_sub_f32_e32 v89, v89, v233\n\t"
"v_sub_f32_e32 v90, v90, v233\n\t"
"v_sub_f32_e32 v91, v91, v233\n\t"
"v_sub_f32_e32 v92, v92, v233\n\t"
"v_sub_f32_e32 v93, v93, v233\n\t"
"v_sub_f32_e32 v94, v94, v233\n\t"
"v_sub_f32_e32 v95, v95, v233\n\t"
"v_sub_f32_e32 v40, v40, v233\n\t"
"v_sub_f32_e32 v41, v41, v233\n\t"
"v_sub_f32_e32 v42, v42, v233\n\t"
"v_sub_f32_e32 v43, v43, v233\n\t"
"v_sub_f32_e32 v44, v44, v233\n\t"
"v_sub_f32_e32 v45, v45, v233\n\t"
"v_sub_f32_e32 v46, v46, v233\n\t"
"v_sub_f32_e32 v47, v47, v233\n\t"
"v_sub_f32_e32 v212, v212, v233\n\t"
"v_sub_f32_e32 v213, v213, v233\n\t"
"v_sub_f32_e32 v214, v214, v233\n\t"
"v_sub_f32_e32 v215, v215, v233\n\t"
"v_exp_f32_e64 v240, -v233\n\t"
"v_exp_f32_e32 v88, v88\n\t"
"v_exp_f32_e32 v89, v89\n\t"
"v_exp_f32_e32 v90, v90\n\t"
"v_exp_f32_e32 v91, v91\n\t"
"v_exp_f32_e32 v92, v92\n\t"
"v_exp_f32_e32 v93, v93\n\t"
"v_exp_f32_e32 v94, v94\n\t"
"v_exp_f32_e32 v95, v95\n\t"
"v_mul_f32_e64 v232, v232, v240\n\t"
"v_cvt_pk_bf16_f32 v88, v88, v89\n\t"
"v_cvt_pk_bf16_f32 v89, v90, v91\n\t"
"v_cvt_pk_bf16_f32 v90, v92, v93\n\t"
"v_cvt_pk_bf16_f32 v91, v94, v95\n\t"
"v_mfma_f32_16x16x32_bf16 v[52:55], v[216:219], v[48:51], 0\n\t"
"v_mfma_f32_16x16x32_bf16 v[60:63], v[216:219], v[56:59], 0\n\t"
"v_mfma_f32_16x16x32_bf16 v[68:71], v[216:219], v[64:67], 0\n\t"
"v_mfma_f32_16x16x32_bf16 v[76:79], v[216:219], v[72:75], 0\n\t"
"v_mfma_f32_16x16x32_bf16 v[84:87], v[216:219], v[80:83], 0\n\t"
"v_mfma_f32_16x16x32_bf16 v[92:95], v[216:219], v[88:91], 0\n\t"
"s_branch RESC%=\n\t"
"RC2%=:\n\t"
"v_and_b32_e32 v233, 15, v252\n\t"
"v_lshlrev_b32_e32 v233, 8, v233\n\t"
"v_lshrrev_b32_e32 v234, 4, v252\n\t"
"v_lshl_add_u32 v233, v234, 4, v233\n\t"
"s_sub_u32 s61, s59, 0\n\t"
"s_lshl_b32 s61, s61, 13\n\t"
"v_add_u32_e64 v233, s61, v233\n\t"
"v_add_u32_e32 v234, 0x1000, v233\n\t"
"global_load_dwordx4 a[224:227], v233, %19 offset:0\n\t"
"global_load_dwordx4 a[228:231], v233, %19 offset:64\n\t"
"global_load_dwordx4 a[232:235], v233, %19 offset:128\n\t"
"global_load_dwordx4 a[236:239], v233, %19 offset:192\n\t"
"global_load_dwordx4 a[240:243], v234, %19 offset:0\n\t"
"global_load_dwordx4 a[244:247], v234, %19 offset:64\n\t"
"global_load_dwordx4 a[248:251], v234, %19 offset:128\n\t"
"global_load_dwordx4 a[252:255], v234, %19 offset:192\n\t"
"s_waitcnt vmcnt(0)\n\t"
"v_mfma_f32_16x16x32_bf16 v[48:51], a[224:227], v[100:103], v[192:195]\n\t"
"v_mfma_f32_16x16x32_bf16 v[52:55], a[240:243], v[100:103], v[192:195]\n\t"
"v_mfma_f32_16x16x32_bf16 v[48:51], a[228:231], v[148:151], v[48:51]\n\t"
"v_mfma_f32_16x16x32_bf16 v[52:55], a[244:247], v[148:151], v[52:55]\n\t"
"v_mfma_f32_16x16x32_bf16 v[48:51], a[232:235], v[96:99], v[48:51]\n\t"
"v_mfma_f32_16x16x32_bf16 v[52:55], a[248:251], v[96:99], v[52:55]\n\t"
"v_mfma_f32_16x16x32_bf16 v[48:51], a[236:239], v[144:147], v[48:51]\n\t"
"v_mfma_f32_16x16x32_bf16 v[52:55], a[252:255], v[144:147], v[52:55]\n\t"
"v_mfma_f32_16x16x32_bf16 v[56:59], a[224:227], v[108:111], v[196:199]\n\t"
"v_mfma_f32_16x16x32_bf16 v[60:63], a[240:243], v[108:111], v[196:199]\n\t"
"v_mfma_f32_16x16x32_bf16 v[56:59], a[228:231], v[156:159], v[56:59]\n\t"
"v_mfma_f32_16x16x32_bf16 v[60:63], a[244:247], v[156:159], v[60:63]\n\t"
"v_mfma_f32_16x16x32_bf16 v[56:59], a[232:235], v[104:107], v[56:59]\n\t"
"v_mfma_f32_16x16x32_bf16 v[60:63], a[248:251], v[104:107], v[60:63]\n\t"
"v_mfma_f32_16x16x32_bf16 v[56:59], a[236:239], v[152:155], v[56:59]\n\t"
"v_mfma_f32_16x16x32_bf16 v[60:63], a[252:255], v[152:155], v[60:63]\n\t"
"v_mfma_f32_16x16x32_bf16 v[64:67], a[224:227], v[116:119], v[200:203]\n\t"
"v_mfma_f32_16x16x32_bf16 v[68:71], a[240:243], v[116:119], v[200:203]\n\t"
"v_mfma_f32_16x16x32_bf16 v[64:67], a[228:231], v[164:167], v[64:67]\n\t"
"v_mfma_f32_16x16x32_bf16 v[68:71], a[244:247], v[164:167], v[68:71]\n\t"
"v_mfma_f32_16x16x32_bf16 v[64:67], a[232:235], v[112:115], v[64:67]\n\t"
"v_mfma_f32_16x16x32_bf16 v[68:71], a[248:251], v[112:115], v[68:71]\n\t"
"v_mfma_f32_16x16x32_bf16 v[64:67], a[236:239], v[160:163], v[64:67]\n\t"
"v_mfma_f32_16x16x32_bf16 v[68:71], a[252:255], v[160:163], v[68:71]\n\t"
"v_mfma_f32_16x16x32_bf16 v[72:75], a[224:227], v[124:127], v[204:207]\n\t"
"v_mfma_f32_16x16x32_bf16 v[76:79], a[240:243], v[124:127], v[204:207]\n\t"
"v_mfma_f32_16x16x32_bf16 v[72:75], a[228:231], v[172:175], v[72:75]\n\t"
"v_mfma_f32_16x16x32_bf16 v[76:79], a[244:247], v[172:175], v[76:79]\n\t"
"v_mfma_f32_16x16x32_bf16 v[72:75], a[232:235], v[120:123], v[72:75]\n\t"
"v_mfma_f32_16x16x32_bf16 v[76:79], a[248:251], v[120:123], v[76:79]\n\t"
"v_mfma_f32_16x16x32_bf16 v[72:75], a[236:239], v[168:171], v[72:75]\n\t"
"v_mfma_f32_16x16x32_bf16 v[76:79], a[252:255], v[168:171], v[76:79]\n\t"
"v_mfma_f32_16x16x32_bf16 v[80:83], a[224:227], v[132:135], v[208:211]\n\t"
"v_mfma_f32_16x16x32_bf16 v[84:87], a[240:243], v[132:135], v[208:211]\n\t"
"v_mfma_f32_16x16x32_bf16 v[80:83], a[228:231], v[180:183], v[80:83]\n\t"
"v_mfma_f32_16x16x32_bf16 v[84:87], a[244:247], v[180:183], v[84:87]\n\t"
"v_mfma_f32_16x16x32_bf16 v[80:83], a[232:235], v[128:131], v[80:83]\n\t"
"v_mfma_f32_16x16x32_bf16 v[84:87], a[248:251], v[128:131], v[84:87]\n\t"
"v_mfma_f32_16x16x32_bf16 v[80:83], a[236:239], v[176:179], v[80:83]\n\t"
"v_mfma_f32_16x16x32_bf16 v[84:87], a[252:255], v[176:179], v[84:87]\n\t"
"v_mfma_f32_16x16x32_bf16 v[88:91], a[224:227], v[140:143], v[212:215]\n\t"
"v_mfma_f32_16x16x32_bf16 v[92:95], a[240:243], v[140:143], v[212:215]\n\t"
"v_mfma_f32_16x16x32_bf16 v[88:91], a[228:231], v[188:191], v[88:91]\n\t"
"v_mfma_f32_16x16x32_bf16 v[92:95], a[244:247], v[188:191], v[92:95]\n\t"
"v_mfma_f32_16x16x32_bf16 v[88:91], a[232:235], v[136:139], v[88:91]\n\t"
"v_mfma_f32_16x16x32_bf16 v[92:95], a[248:251], v[136:139], v[92:95]\n\t"
"v_mfma_f32_16x16x32_bf16 v[88:91], a[236:239], v[184:187], v[88:91]\n\t"
"v_mfma_f32_16x16x32_bf16 v[92:95], a[252:255], v[184:187], v[92:95]\n\t"
"s_nop 7\n\t"
"v_max3_f32 v233, v48, v49, v50\n\t"
"v_max3_f32 v234, v51, v52, v53\n\t"
"v_max3_f32 v233, v233, v54, v55\n\t"
"v_max_f32_e32 v233, v233, v234\n\t"
"v_mov_b32_e32 v234, v233\n\t"
"s_nop 1\n\t"
"v_permlane16_swap_b32_e32 v233, v234\n\t"
"s_nop 0\n\t"
"v_max_f32_e32 v233, v233, v234\n\t"
"v_mov_b32_e32 v234, v233\n\t"
"s_nop 1\n\t"
"v_permlane32_swap_b32_e32 v233, v234\n\t"
"s_nop 0\n\t"
"v_max3_f32 v233, v233, v234, 0\n\t"
"v_sub_f32_e32 v48, v48, v233\n\t"
"v_sub_f32_e32 v49, v49, v233\n\t"
"v_sub_f32_e32 v50, v50, v233\n\t"
"v_sub_f32_e32 v51, v51, v233\n\t"
"v_sub_f32_e32 v52, v52, v233\n\t"
"v_sub_f32_e32 v53, v53, v233\n\t"
"v_sub_f32_e32 v54, v54, v233\n\t"
"v_sub_f32_e32 v55, v55, v233\n\t"
"v_sub_f32_e32 v192, v192, v233\n\t"
"v_sub_f32_e32 v193, v193, v233\n\t"
"v_sub_f32_e32 v194, v194, v233\n\t"
"v_sub_f32_e32 v195, v195, v233\n\t"
"v_exp_f32_e64 v235, -v233\n\t"
"v_exp_f32_e32 v48, v48\n\t"
"v_exp_f32_e32 v49, v49\n\t"
"v_exp_f32_e32 v50, v50\n\t"
"v_exp_f32_e32 v51, v51\n\t"
"v_exp_f32_e32 v52, v52\n\t"
"v_exp_f32_e32 v53, v53\n\t"
"v_exp_f32_e32 v54, v54\n\t"
"v_exp_f32_e32 v55, v55\n\t"
"v_mul_f32_e64 v227, v227, v235\n\t"
"v_cvt_pk_bf16_f32 v48, v48, v49\n\t"
"v_cvt_pk_bf16_f32 v49, v50, v51\n\t"
"v_cvt_pk_bf16_f32 v50, v52, v53\n\t"
"v_cvt_pk_bf16_f32 v51, v54, v55\n\t"
"v_max3_f32 v233, v56, v57, v58\n\t"
"v_max3_f32 v234, v59, v60, v61\n\t"
"v_max3_f32 v233, v233, v62, v63\n\t"
"v_max_f32_e32 v233, v233, v234\n\t"
"v_mov_b32_e32 v234, v233\n\t"
"s_nop 1\n\t"
"v_permlane16_swap_b32_e32 v233, v234\n\t"
"s_nop 0\n\t"
"v_max_f32_e32 v233, v233, v234\n\t"
"v_mov_b32_e32 v234, v233\n\t"
"s_nop 1\n\t"
"v_permlane32_swap_b32_e32 v233, v234\n\t"
"s_nop 0\n\t"
"v_max3_f32 v233, v233, v234, 0\n\t"
"v_sub_f32_e32 v56, v56, v233\n\t"
"v_sub_f32_e32 v57, v57, v233\n\t"
"v_sub_f32_e32 v58, v58, v233\n\t"
"v_sub_f32_e32 v59, v59, v233\n\t"
"v_sub_f32_e32 v60, v60, v233\n\t"
"v_sub_f32_e32 v61, v61, v233\n\t"
"v_sub_f32_e32 v62, v62, v233\n\t"
"v_sub_f32_e32 v63, v63, v233\n\t"
"v_sub_f32_e32 v196, v196, v233\n\t"
"v_sub_f32_e32 v197, v197, v233\n\t"
"v_sub_f32_e32 v198, v198, v233\n\t"
"v_sub_f32_e32 v199, v199, v233\n\t"
"v_exp_f32_e64 v236, -v233\n\t"
"v_exp_f32_e32 v56, v56\n\t"
"v_exp_f32_e32 v57, v57\n\t"
"v_exp_f32_e32 v58, v58\n\t"
"v_exp_f32_e32 v59, v59\n\t"
"v_exp_f32_e32 v60, v60\n\t"
"v_exp_f32_e32 v61, v61\n\t"
"v_exp_f32_e32 v62, v62\n\t"
"v_exp_f32_e32 v63, v63\n\t"
"v_mul_f32_e64 v228, v228, v236\n\t"
"v_cvt_pk_bf16_f32 v56, v56, v57\n\t"
"v_cvt_pk_bf16_f32 v57, v58, v59\n\t"
"v_cvt_pk_bf16_f32 v58, v60, v61\n\t"
"v_cvt_pk_bf16_f32 v59, v62, v63\n\t"
"v_max3_f32 v233, v64, v65, v66\n\t"
"v_max3_f32 v234, v67, v68, v69\n\t"
"v_max3_f32 v233, v233, v70, v71\n\t"
"v_max_f32_e32 v233, v233, v234\n\t"
"v_mov_b32_e32 v234, v233\n\t"
"s_nop 1\n\t"
"v_permlane16_swap_b32_e32 v233, v234\n\t"
"s_nop 0\n\t"
"v_max_f32_e32 v233, v233, v234\n\t"
"v_mov_b32_e32 v234, v233\n\t"
"s_nop 1\n\t"
"v_permlane32_swap_b32_e32 v233, v234\n\t"
"s_nop 0\n\t"
"v_max3_f32 v233, v233, v234, 0\n\t"
"v_sub_f32_e32 v64, v64, v233\n\t"
"v_sub_f32_e32 v65, v65, v233\n\t"
"v_sub_f32_e32 v66, v66, v233\n\t"
"v_sub_f32_e32 v67, v67, v233\n\t"
"v_sub_f32_e32 v68, v68, v233\n\t"
"v_sub_f32_e32 v69, v69, v233\n\t"
"v_sub_f32_e32 v70, v70, v233\n\t"
"v_sub_f32_e32 v71, v71, v233\n\t"
"v_sub_f32_e32 v200, v200, v233\n\t"
"v_sub_f32_e32 v201, v201, v233\n\t"
"v_sub_f32_e32 v202, v202, v233\n\t"
"v_sub_f32_e32 v203, v203, v233\n\t"
"v_exp_f32_e64 v237, -v233\n\t"
"v_exp_f32_e32 v64, v64\n\t"
"v_exp_f32_e32 v65, v65\n\t"
"v_exp_f32_e32 v66, v66\n\t"
"v_exp_f32_e32 v67, v67\n\t"
"v_exp_f32_e32 v68, v68\n\t"
"v_exp_f32_e32 v69, v69\n\t"
"v_exp_f32_e32 v70, v70\n\t"
"v_exp_f32_e32 v71, v71\n\t"
"v_mul_f32_e64 v229, v229, v237\n\t"
"v_cvt_pk_bf16_f32 v64, v64, v65\n\t"
"v_cvt_pk_bf16_f32 v65, v66, v67\n\t"
"v_cvt_pk_bf16_f32 v66, v68, v69\n\t"
"v_cvt_pk_bf16_f32 v67, v70, v71\n\t"
"v_max3_f32 v233, v72, v73, v74\n\t"
"v_max3_f32 v234, v75, v76, v77\n\t"
"v_max3_f32 v233, v233, v78, v79\n\t"
"v_max_f32_e32 v233, v233, v234\n\t"
"v_mov_b32_e32 v234, v233\n\t"
"s_nop 1\n\t"
"v_permlane16_swap_b32_e32 v233, v234\n\t"
"s_nop 0\n\t"
"v_max_f32_e32 v233, v233, v234\n\t"
"v_mov_b32_e32 v234, v233\n\t"
"s_nop 1\n\t"
"v_permlane32_swap_b32_e32 v233, v234\n\t"
"s_nop 0\n\t"
"v_max3_f32 v233, v233, v234, 0\n\t"
"v_sub_f32_e32 v72, v72, v233\n\t"
"v_sub_f32_e32 v73, v73, v233\n\t"
"v_sub_f32_e32 v74, v74, v233\n\t"
"v_sub_f32_e32 v75, v75, v233\n\t"
"v_sub_f32_e32 v76, v76, v233\n\t"
"v_sub_f32_e32 v77, v77, v233\n\t"
"v_sub_f32_e32 v78, v78, v233\n\t"
"v_sub_f32_e32 v79, v79, v233\n\t"
"v_sub_f32_e32 v204, v204, v233\n\t"
"v_sub_f32_e32 v205, v205, v233\n\t"
"v_sub_f32_e32 v206, v206, v233\n\t"
"v_sub_f32_e32 v207, v207, v233\n\t"
"v_exp_f32_e64 v238, -v233\n\t"
"v_exp_f32_e32 v72, v72\n\t"
"v_exp_f32_e32 v73, v73\n\t"
"v_exp_f32_e32 v74, v74\n\t"
"v_exp_f32_e32 v75, v75\n\t"
"v_exp_f32_e32 v76, v76\n\t"
"v_exp_f32_e32 v77, v77\n\t"
"v_exp_f32_e32 v78, v78\n\t"
"v_exp_f32_e32 v79, v79\n\t"
"v_mul_f32_e64 v230, v230, v238\n\t"
"v_cvt_pk_bf16_f32 v72, v72, v73\n\t"
"v_cvt_pk_bf16_f32 v73, v74, v75\n\t"
"v_cvt_pk_bf16_f32 v74, v76, v77\n\t"
"v_cvt_pk_bf16_f32 v75, v78, v79\n\t"
"v_max3_f32 v233, v80, v81, v82\n\t"
"v_max3_f32 v234, v83, v84, v85\n\t"
"v_max3_f32 v233, v233, v86, v87\n\t"
"v_max_f32_e32 v233, v233, v234\n\t"
"v_mov_b32_e32 v234, v233\n\t"
"s_nop 1\n\t"
"v_permlane16_swap_b32_e32 v233, v234\n\t"
"s_nop 0\n\t"
"v_max_f32_e32 v233, v233, v234\n\t"
"v_mov_b32_e32 v234, v233\n\t"
"s_nop 1\n\t"
"v_permlane32_swap_b32_e32 v233, v234\n\t"
"s_nop 0\n\t"
"v_max3_f32 v233, v233, v234, 0\n\t"
"v_sub_f32_e32 v80, v80, v233\n\t"
"v_sub_f32_e32 v81, v81, v233\n\t"
"v_sub_f32_e32 v82, v82, v233\n\t"
"v_sub_f32_e32 v83, v83, v233\n\t"
"v_sub_f32_e32 v84, v84, v233\n\t"
"v_sub_f32_e32 v85, v85, v233\n\t"
"v_sub_f32_e32 v86, v86, v233\n\t"
"v_sub_f32_e32 v87, v87, v233\n\t"
"v_sub_f32_e32 v208, v208, v233\n\t"
"v_sub_f32_e32 v209, v209, v233\n\t"
"v_sub_f32_e32 v210, v210, v233\n\t"
"v_sub_f32_e32 v211, v211, v233\n\t"
"v_exp_f32_e64 v239, -v233\n\t"
"v_exp_f32_e32 v80, v80\n\t"
"v_exp_f32_e32 v81, v81\n\t"
"v_exp_f32_e32 v82, v82\n\t"
"v_exp_f32_e32 v83, v83\n\t"
"v_exp_f32_e32 v84, v84\n\t"
"v_exp_f32_e32 v85, v85\n\t"
"v_exp_f32_e32 v86, v86\n\t"
"v_exp_f32_e32 v87, v87\n\t"
"v_mul_f32_e64 v231, v231, v239\n\t"
"v_cvt_pk_bf16_f32 v80, v80, v81\n\t"
"v_cvt_pk_bf16_f32 v81, v82, v83\n\t"
"v_cvt_pk_bf16_f32 v82, v84, v85\n\t"
"v_cvt_pk_bf16_f32 v83, v86, v87\n\t"
"v_max3_f32 v233, v88, v89, v90\n\t"
"v_max3_f32 v234, v91, v92, v93\n\t"
"v_max3_f32 v233, v233, v94, v95\n\t"
"v_max_f32_e32 v233, v233, v234\n\t"
"v_mov_b32_e32 v234, v233\n\t"
"s_nop 1\n\t"
"v_permlane16_swap_b32_e32 v233, v234\n\t"
"s_nop 0\n\t"
"v_max_f32_e32 v233, v233, v234\n\t"
"v_mov_b32_e32 v234, v233\n\t"
"s_nop 1\n\t"
"v_permlane32_swap_b32_e32 v233, v234\n\t"
"s_nop 0\n\t"
"v_max3_f32 v233, v233, v234, 0\n\t"
"v_sub_f32_e32 v88, v88, v233\n\t"
"v_sub_f32_e32 v89, v89, v233\n\t"
"v_sub_f32_e32 v90, v90, v233\n\t"
"v_sub_f32_e32 v91, v91, v233\n\t"
"v_sub_f32_e32 v92, v92, v233\n\t"
"v_sub_f32_e32 v93, v93, v233\n\t"
"v_sub_f32_e32 v94, v94, v233\n\t"
"v_sub_f32_e32 v95, v95, v233\n\t"
"v_sub_f32_e32 v212, v212, v233\n\t"
"v_sub_f32_e32 v213, v213, v233\n\t"
"v_sub_f32_e32 v214, v214, v233\n\t"
"v_sub_f32_e32 v215, v215, v233\n\t"
"v_exp_f32_e64 v240, -v233\n\t"
"v_exp_f32_e32 v88, v88\n\t"
"v_exp_f32_e32 v89, v89\n\t"
"v_exp_f32_e32 v90, v90\n\t"
"v_exp_f32_e32 v91, v91\n\t"
"v_exp_f32_e32 v92, v92\n\t"
"v_exp_f32_e32 v93, v93\n\t"
"v_exp_f32_e32 v94, v94\n\t"
"v_exp_f32_e32 v95, v95\n\t"
"v_mul_f32_e64 v232, v232, v240\n\t"
"v_cvt_pk_bf16_f32 v88, v88, v89\n\t"
"v_cvt_pk_bf16_f32 v89, v90, v91\n\t"
"v_cvt_pk_bf16_f32 v90, v92, v93\n\t"
"v_cvt_pk_bf16_f32 v91, v94, v95\n\t"
"v_mfma_f32_16x16x32_bf16 v[52:55], v[216:219], v[48:51], 0\n\t"
"v_mfma_f32_16x16x32_bf16 v[60:63], v[216:219], v[56:59], 0\n\t"
"v_mfma_f32_16x16x32_bf16 v[68:71], v[216:219], v[64:67], 0\n\t"
"v_mfma_f32_16x16x32_bf16 v[76:79], v[216:219], v[72:75], 0\n\t"
"v_mfma_f32_16x16x32_bf16 v[84:87], v[216:219], v[80:83], 0\n\t"
"v_mfma_f32_16x16x32_bf16 v[92:95], v[216:219], v[88:91], 0\n\t"
"s_branch RESC%=\n\t"
"CS0_%=:\n\t"
"s_call_b64 s[66:67], RC0%=\n\t"
"s_branch CB0_%=\n\t"
"CS1_%=:\n\t"
"s_call_b64 s[66:67], RC1%=\n\t"
"s_branch CB1_%=\n\t"
"CS2_%=:\n\t"
"s_call_b64 s[66:67], RC0%=\n\t"
"s_branch CB2_%=\n\t"
"CS3_%=:\n\t"
"s_call_b64 s[66:67], RC1%=\n\t"
"s_branch CB3_%=\n\t"
"CS4_%=:\n\t"
"s_call_b64 s[66:67], RC0%=\n\t"
"s_branch CB4_%=\n\t"
"CS5_%=:\n\t"
"s_call_b64 s[66:67], RC2%=\n\t"
"s_branch CB5_%=\n\t"
"RESC%=:\n\t"
"s_nop 15\n\t"
"v_accvgpr_read_b32 v233, a0\n\t"
"v_accvgpr_read_b32 v234, a1\n\t"
"v_mul_f32_e32 v233, v233, v235\n\t"
"v_mul_f32_e32 v234, v234, v235\n\t"
"v_accvgpr_write_b32 a0, v233\n\t"
"v_accvgpr_write_b32 a1, v234\n\t"
"v_accvgpr_read_b32 v233, a2\n\t"
"v_accvgpr_read_b32 v234, a3\n\t"
"v_mul_f32_e32 v233, v233, v235\n\t"
"v_mul_f32_e32 v234, v234, v235\n\t"
"v_accvgpr_write_b32 a2, v233\n\t"
"v_accvgpr_write_b32 a3, v234\n\t"
"v_accvgpr_read_b32 v233, a4\n\t"
"v_accvgpr_read_b32 v234, a5\n\t"
"v_mul_f32_e32 v233, v233, v235\n\t"
"v_mul_f32_e32 v234, v234, v235\n\t"
"v_accvgpr_write_b32 a4, v233\n\t"
"v_accvgpr_write_b32 a5, v234\n\t"
"v_accvgpr_read_b32 v233, a6\n\t"
"v_accvgpr_read_b32 v234, a7\n\t"
"v_mul_f32_e32 v233, v233, v235\n\t"
"v_mul_f32_e32 v234, v234, v235\n\t"
"v_accvgpr_write_b32 a6, v233\n\t"
"v_accvgpr_write_b32 a7, v234\n\t"
"v_accvgpr_read_b32 v233, a8\n\t"
"v_accvgpr_read_b32 v234, a9\n\t"
"v_mul_f32_e32 v233, v233, v235\n\t"
"v_mul_f32_e32 v234, v234, v235\n\t"
"v_accvgpr_write_b32 a8, v233\n\t"
"v_accvgpr_write_b32 a9, v234\n\t"
"v_accvgpr_read_b32 v233, a10\n\t"
"v_accvgpr_read_b32 v234, a11\n\t"
"v_mul_f32_e32 v233, v233, v235\n\t"
"v_mul_f32_e32 v234, v234, v235\n\t"
"v_accvgpr_write_b32 a10, v233\n\t"
"v_accvgpr_write_b32 a11, v234\n\t"
"v_accvgpr_read_b32 v233, a12\n\t"
"v_accvgpr_read_b32 v234, a13\n\t"
"v_mul_f32_e32 v233, v233, v235\n\t"
"v_mul_f32_e32 v234, v234, v235\n\t"
"v_accvgpr_write_b32 a12, v233\n\t"
"v_accvgpr_write_b32 a13, v234\n\t"
"v_accvgpr_read_b32 v233, a14\n\t"
"v_accvgpr_read_b32 v234, a15\n\t"
"v_mul_f32_e32 v233, v233, v235\n\t"
"v_mul_f32_e32 v234, v234, v235\n\t"
"v_accvgpr_write_b32 a14, v233\n\t"
"v_accvgpr_write_b32 a15, v234\n\t"
"v_accvgpr_read_b32 v233, a16\n\t"
"v_accvgpr_read_b32 v234, a17\n\t"
"v_mul_f32_e32 v233, v233, v235\n\t"
"v_mul_f32_e32 v234, v234, v235\n\t"
"v_accvgpr_write_b32 a16, v233\n\t"
"v_accvgpr_write_b32 a17, v234\n\t"
"v_accvgpr_read_b32 v233, a18\n\t"
"v_accvgpr_read_b32 v234, a19\n\t"
"v_mul_f32_e32 v233, v233, v235\n\t"
"v_mul_f32_e32 v234, v234, v235\n\t"
"v_accvgpr_write_b32 a18, v233\n\t"
"v_accvgpr_write_b32 a19, v234\n\t"
"v_accvgpr_read_b32 v233, a20\n\t"
"v_accvgpr_read_b32 v234, a21\n\t"
"v_mul_f32_e32 v233, v233, v235\n\t"
"v_mul_f32_e32 v234, v234, v235\n\t"
"v_accvgpr_write_b32 a20, v233\n\t"
"v_accvgpr_write_b32 a21, v234\n\t"
"v_accvgpr_read_b32 v233, a22\n\t"
"v_accvgpr_read_b32 v234, a23\n\t"
"v_mul_f32_e32 v233, v233, v235\n\t"
"v_mul_f32_e32 v234, v234, v235\n\t"
"v_accvgpr_write_b32 a22, v233\n\t"
"v_accvgpr_write_b32 a23, v234\n\t"
"v_accvgpr_read_b32 v233, a24\n\t"
"v_accvgpr_read_b32 v234, a25\n\t"
"v_mul_f32_e32 v233, v233, v235\n\t"
"v_mul_f32_e32 v234, v234, v235\n\t"
"v_accvgpr_write_b32 a24, v233\n\t"
"v_accvgpr_write_b32 a25, v234\n\t"
"v_accvgpr_read_b32 v233, a26\n\t"
"v_accvgpr_read_b32 v234, a27\n\t"
"v_mul_f32_e32 v233, v233, v235\n\t"
"v_mul_f32_e32 v234, v234, v235\n\t"
"v_accvgpr_write_b32 a26, v233\n\t"
"v_accvgpr_write_b32 a27, v234\n\t"
"v_accvgpr_read_b32 v233, a28\n\t"
"v_accvgpr_read_b32 v234, a29\n\t"
"v_mul_f32_e32 v233, v233, v235\n\t"
"v_mul_f32_e32 v234, v234, v235\n\t"
"v_accvgpr_write_b32 a28, v233\n\t"
"v_accvgpr_write_b32 a29, v234\n\t"
"v_accvgpr_read_b32 v233, a30\n\t"
"v_accvgpr_read_b32 v234, a31\n\t"
"v_mul_f32_e32 v233, v233, v235\n\t"
"v_mul_f32_e32 v234, v234, v235\n\t"
"v_accvgpr_write_b32 a30, v233\n\t"
"v_accvgpr_write_b32 a31, v234\n\t"
"v_accvgpr_read_b32 v233, a32\n\t"
"v_accvgpr_read_b32 v234, a33\n\t"
"v_mul_f32_e32 v233, v233, v236\n\t"
"v_mul_f32_e32 v234, v234, v236\n\t"
"v_accvgpr_write_b32 a32, v233\n\t"
"v_accvgpr_write_b32 a33, v234\n\t"
"v_accvgpr_read_b32 v233, a34\n\t"
"v_accvgpr_read_b32 v234, a35\n\t"
"v_mul_f32_e32 v233, v233, v236\n\t"
"v_mul_f32_e32 v234, v234, v236\n\t"
"v_accvgpr_write_b32 a34, v233\n\t"
"v_accvgpr_write_b32 a35, v234\n\t"
"v_accvgpr_read_b32 v233, a36\n\t"
"v_accvgpr_read_b32 v234, a37\n\t"
"v_mul_f32_e32 v233, v233, v236\n\t"
"v_mul_f32_e32 v234, v234, v236\n\t"
"v_accvgpr_write_b32 a36, v233\n\t"
"v_accvgpr_write_b32 a37, v234\n\t"
"v_accvgpr_read_b32 v233, a38\n\t"
"v_accvgpr_read_b32 v234, a39\n\t"
"v_mul_f32_e32 v233, v233, v236\n\t"
"v_mul_f32_e32 v234, v234, v236\n\t"
"v_accvgpr_write_b32 a38, v233\n\t"
"v_accvgpr_write_b32 a39, v234\n\t"
"v_accvgpr_read_b32 v233, a40\n\t"
"v_accvgpr_read_b32 v234, a41\n\t"
"v_mul_f32_e32 v233, v233, v236\n\t"
"v_mul_f32_e32 v234, v234, v236\n\t"
"v_accvgpr_write_b32 a40, v233\n\t"
"v_accvgpr_write_b32 a41, v234\n\t"
"v_accvgpr_read_b32 v233, a42\n\t"
"v_accvgpr_read_b32 v234, a43\n\t"
"v_mul_f32_e32 v233, v233, v236\n\t"
"v_mul_f32_e32 v234, v234, v236\n\t"
"v_accvgpr_write_b32 a42, v233\n\t"
"v_accvgpr_write_b32 a43, v234\n\t"
"v_accvgpr_read_b32 v233, a44\n\t"
"v_accvgpr_read_b32 v234, a45\n\t"
"v_mul_f32_e32 v233, v233, v236\n\t"
"v_mul_f32_e32 v234, v234, v236\n\t"
"v_accvgpr_write_b32 a44, v233\n\t"
"v_accvgpr_write_b32 a45, v234\n\t"
"v_accvgpr_read_b32 v233, a46\n\t"
"v_accvgpr_read_b32 v234, a47\n\t"
"v_mul_f32_e32 v233, v233, v236\n\t"
"v_mul_f32_e32 v234, v234, v236\n\t"
"v_accvgpr_write_b32 a46, v233\n\t"
"v_accvgpr_write_b32 a47, v234\n\t"
"v_accvgpr_read_b32 v233, a48\n\t"
"v_accvgpr_read_b32 v234, a49\n\t"
"v_mul_f32_e32 v233, v233, v236\n\t"
"v_mul_f32_e32 v234, v234, v236\n\t"
"v_accvgpr_write_b32 a48, v233\n\t"
"v_accvgpr_write_b32 a49, v234\n\t"
"v_accvgpr_read_b32 v233, a50\n\t"
"v_accvgpr_read_b32 v234, a51\n\t"
"v_mul_f32_e32 v233, v233, v236\n\t"
"v_mul_f32_e32 v234, v234, v236\n\t"
"v_accvgpr_write_b32 a50, v233\n\t"
"v_accvgpr_write_b32 a51, v234\n\t"
"v_accvgpr_read_b32 v233, a52\n\t"
"v_accvgpr_read_b32 v234, a53\n\t"
"v_mul_f32_e32 v233, v233, v236\n\t"
"v_mul_f32_e32 v234, v234, v236\n\t"
"v_accvgpr_write_b32 a52, v233\n\t"
"v_accvgpr_write_b32 a53, v234\n\t"
"v_accvgpr_read_b32 v233, a54\n\t"
"v_accvgpr_read_b32 v234, a55\n\t"
"v_mul_f32_e32 v233, v233, v236\n\t"
"v_mul_f32_e32 v234, v234, v236\n\t"
"v_accvgpr_write_b32 a54, v233\n\t"
"v_accvgpr_write_b32 a55, v234\n\t"
"v_accvgpr_read_b32 v233, a56\n\t"
"v_accvgpr_read_b32 v234, a57\n\t"
"v_mul_f32_e32 v233, v233, v236\n\t"
"v_mul_f32_e32 v234, v234, v236\n\t"
"v_accvgpr_write_b32 a56, v233\n\t"
"v_accvgpr_write_b32 a57, v234\n\t"
"v_accvgpr_read_b32 v233, a58\n\t"
"v_accvgpr_read_b32 v234, a59\n\t"
"v_mul_f32_e32 v233, v233, v236\n\t"
"v_mul_f32_e32 v234, v234, v236\n\t"
"v_accvgpr_write_b32 a58, v233\n\t"
"v_accvgpr_write_b32 a59, v234\n\t"
"v_accvgpr_read_b32 v233, a60\n\t"
"v_accvgpr_read_b32 v234, a61\n\t"
"v_mul_f32_e32 v233, v233, v236\n\t"
"v_mul_f32_e32 v234, v234, v236\n\t"
"v_accvgpr_write_b32 a60, v233\n\t"
"v_accvgpr_write_b32 a61, v234\n\t"
"v_accvgpr_read_b32 v233, a62\n\t"
"v_accvgpr_read_b32 v234, a63\n\t"
"v_mul_f32_e32 v233, v233, v236\n\t"
"v_mul_f32_e32 v234, v234, v236\n\t"
"v_accvgpr_write_b32 a62, v233\n\t"
"v_accvgpr_write_b32 a63, v234\n\t"
"v_accvgpr_read_b32 v233, a64\n\t"
"v_accvgpr_read_b32 v234, a65\n\t"
"v_mul_f32_e32 v233, v233, v237\n\t"
"v_mul_f32_e32 v234, v234, v237\n\t"
"v_accvgpr_write_b32 a64, v233\n\t"
"v_accvgpr_write_b32 a65, v234\n\t"
"v_accvgpr_read_b32 v233, a66\n\t"
"v_accvgpr_read_b32 v234, a67\n\t"
"v_mul_f32_e32 v233, v233, v237\n\t"
"v_mul_f32_e32 v234, v234, v237\n\t"
"v_accvgpr_write_b32 a66, v233\n\t"
"v_accvgpr_write_b32 a67, v234\n\t"
"v_accvgpr_read_b32 v233, a68\n\t"
"v_accvgpr_read_b32 v234, a69\n\t"
"v_mul_f32_e32 v233, v233, v237\n\t"
"v_mul_f32_e32 v234, v234, v237\n\t"
"v_accvgpr_write_b32 a68, v233\n\t"
"v_accvgpr_write_b32 a69, v234\n\t"
"v_accvgpr_read_b32 v233, a70\n\t"
"v_accvgpr_read_b32 v234, a71\n\t"
"v_mul_f32_e32 v233, v233, v237\n\t"
"v_mul_f32_e32 v234, v234, v237\n\t"
"v_accvgpr_write_b32 a70, v233\n\t"
"v_accvgpr_write_b32 a71, v234\n\t"
"v_accvgpr_read_b32 v233, a72\n\t"
"v_accvgpr_read_b32 v234, a73\n\t"
"v_mul_f32_e32 v233, v233, v237\n\t"
"v_mul_f32_e32 v234, v234, v237\n\t"
"v_accvgpr_write_b32 a72, v233\n\t"
"v_accvgpr_write_b32 a73, v234\n\t"
"v_accvgpr_read_b32 v233, a74\n\t"
"v_accvgpr_read_b32 v234, a75\n\t"
"v_mul_f32_e32 v233, v233, v237\n\t"
"v_mul_f32_e32 v234, v234, v237\n\t"
"v_accvgpr_write_b32 a74, v233\n\t"
"v_accvgpr_write_b32 a75, v234\n\t"
"v_accvgpr_read_b32 v233, a76\n\t"
"v_accvgpr_read_b32 v234, a77\n\t"
"v_mul_f32_e32 v233, v233, v237\n\t"
"v_mul_f32_e32 v234, v234, v237\n\t"
"v_accvgpr_write_b32 a76, v233\n\t"
"v_accvgpr_write_b32 a77, v234\n\t"
"v_accvgpr_read_b32 v233, a78\n\t"
"v_accvgpr_read_b32 v234, a79\n\t"
"v_mul_f32_e32 v233, v233, v237\n\t"
"v_mul_f32_e32 v234, v234, v237\n\t"
"v_accvgpr_write_b32 a78, v233\n\t"
"v_accvgpr_write_b32 a79, v234\n\t"
"v_accvgpr_read_b32 v233, a80\n\t"
"v_accvgpr_read_b32 v234, a81\n\t"
"v_mul_f32_e32 v233, v233, v237\n\t"
"v_mul_f32_e32 v234, v234, v237\n\t"
"v_accvgpr_write_b32 a80, v233\n\t"
"v_accvgpr_write_b32 a81, v234\n\t"
"v_accvgpr_read_b32 v233, a82\n\t"
"v_accvgpr_read_b32 v234, a83\n\t"
"v_mul_f32_e32 v233, v233, v237\n\t"
"v_mul_f32_e32 v234, v234, v237\n\t"
"v_accvgpr_write_b32 a82, v233\n\t"
"v_accvgpr_write_b32 a83, v234\n\t"
"v_accvgpr_read_b32 v233, a84\n\t"
"v_accvgpr_read_b32 v234, a85\n\t"
"v_mul_f32_e32 v233, v233, v237\n\t"
"v_mul_f32_e32 v234, v234, v237\n\t"
"v_accvgpr_write_b32 a84, v233\n\t"
"v_accvgpr_write_b32 a85, v234\n\t"
"v_accvgpr_read_b32 v233, a86\n\t"
"v_accvgpr_read_b32 v234, a87\n\t"
"v_mul_f32_e32 v233, v233, v237\n\t"
"v_mul_f32_e32 v234, v234, v237\n\t"
"v_accvgpr_write_b32 a86, v233\n\t"
"v_accvgpr_write_b32 a87, v234\n\t"
"v_accvgpr_read_b32 v233, a88\n\t"
"v_accvgpr_read_b32 v234, a89\n\t"
"v_mul_f32_e32 v233, v233, v237\n\t"
"v_mul_f32_e32 v234, v234, v237\n\t"
"v_accvgpr_write_b32 a88, v233\n\t"
"v_accvgpr_write_b32 a89, v234\n\t"
"v_accvgpr_read_b32 v233, a90\n\t"
"v_accvgpr_read_b32 v234, a91\n\t"
"v_mul_f32_e32 v233, v233, v237\n\t"
"v_mul_f32_e32 v234, v234, v237\n\t"
"v_accvgpr_write_b32 a90, v233\n\t"
"v_accvgpr_write_b32 a91, v234\n\t"
"v_accvgpr_read_b32 v233, a92\n\t"
"v_accvgpr_read_b32 v234, a93\n\t"
"v_mul_f32_e32 v233, v233, v237\n\t"
"v_mul_f32_e32 v234, v234, v237\n\t"
"v_accvgpr_write_b32 a92, v233\n\t"
"v_accvgpr_write_b32 a93, v234\n\t"
"v_accvgpr_read_b32 v233, a94\n\t"
"v_accvgpr_read_b32 v234, a95\n\t"
"v_mul_f32_e32 v233, v233, v237\n\t"
"v_mul_f32_e32 v234, v234, v237\n\t"
"v_accvgpr_write_b32 a94, v233\n\t"
"v_accvgpr_write_b32 a95, v234\n\t"
"v_accvgpr_read_b32 v233, a96\n\t"
"v_accvgpr_read_b32 v234, a97\n\t"
"v_mul_f32_e32 v233, v233, v238\n\t"
"v_mul_f32_e32 v234, v234, v238\n\t"
"v_accvgpr_write_b32 a96, v233\n\t"
"v_accvgpr_write_b32 a97, v234\n\t"
"v_accvgpr_read_b32 v233, a98\n\t"
"v_accvgpr_read_b32 v234, a99\n\t"
"v_mul_f32_e32 v233, v233, v238\n\t"
"v_mul_f32_e32 v234, v234, v238\n\t"
"v_accvgpr_write_b32 a98, v233\n\t"
"v_accvgpr_write_b32 a99, v234\n\t"
"v_accvgpr_read_b32 v233, a100\n\t"
"v_accvgpr_read_b32 v234, a101\n\t"
"v_mul_f32_e32 v233, v233, v238\n\t"
"v_mul_f32_e32 v234, v234, v238\n\t"
"v_accvgpr_write_b32 a100, v233\n\t"
"v_accvgpr_write_b32 a101, v234\n\t"
"v_accvgpr_read_b32 v233, a102\n\t"
"v_accvgpr_read_b32 v234, a103\n\t"
"v_mul_f32_e32 v233, v233, v238\n\t"
"v_mul_f32_e32 v234, v234, v238\n\t"
"v_accvgpr_write_b32 a102, v233\n\t"
"v_accvgpr_write_b32 a103, v234\n\t"
"v_accvgpr_read_b32 v233, a104\n\t"
"v_accvgpr_read_b32 v234, a105\n\t"
"v_mul_f32_e32 v233, v233, v238\n\t"
"v_mul_f32_e32 v234, v234, v238\n\t"
"v_accvgpr_write_b32 a104, v233\n\t"
"v_accvgpr_write_b32 a105, v234\n\t"
"v_accvgpr_read_b32 v233, a106\n\t"
"v_accvgpr_read_b32 v234, a107\n\t"
"v_mul_f32_e32 v233, v233, v238\n\t"
"v_mul_f32_e32 v234, v234, v238\n\t"
"v_accvgpr_write_b32 a106, v233\n\t"
"v_accvgpr_write_b32 a107, v234\n\t"
"v_accvgpr_read_b32 v233, a108\n\t"
"v_accvgpr_read_b32 v234, a109\n\t"
"v_mul_f32_e32 v233, v233, v238\n\t"
"v_mul_f32_e32 v234, v234, v238\n\t"
"v_accvgpr_write_b32 a108, v233\n\t"
"v_accvgpr_write_b32 a109, v234\n\t"
"v_accvgpr_read_b32 v233, a110\n\t"
"v_accvgpr_read_b32 v234, a111\n\t"
"v_mul_f32_e32 v233, v233, v238\n\t"
"v_mul_f32_e32 v234, v234, v238\n\t"
"v_accvgpr_write_b32 a110, v233\n\t"
"v_accvgpr_write_b32 a111, v234\n\t"
"v_accvgpr_read_b32 v233, a112\n\t"
"v_accvgpr_read_b32 v234, a113\n\t"
"v_mul_f32_e32 v233, v233, v238\n\t"
"v_mul_f32_e32 v234, v234, v238\n\t"
"v_accvgpr_write_b32 a112, v233\n\t"
"v_accvgpr_write_b32 a113, v234\n\t"
"v_accvgpr_read_b32 v233, a114\n\t"
"v_accvgpr_read_b32 v234, a115\n\t"
"v_mul_f32_e32 v233, v233, v238\n\t"
"v_mul_f32_e32 v234, v234, v238\n\t"
"v_accvgpr_write_b32 a114, v233\n\t"
"v_accvgpr_write_b32 a115, v234\n\t"
"v_accvgpr_read_b32 v233, a116\n\t"
"v_accvgpr_read_b32 v234, a117\n\t"
"v_mul_f32_e32 v233, v233, v238\n\t"
"v_mul_f32_e32 v234, v234, v238\n\t"
"v_accvgpr_write_b32 a116, v233\n\t"
"v_accvgpr_write_b32 a117, v234\n\t"
"v_accvgpr_read_b32 v233, a118\n\t"
"v_accvgpr_read_b32 v234, a119\n\t"
"v_mul_f32_e32 v233, v233, v238\n\t"
"v_mul_f32_e32 v234, v234, v238\n\t"
"v_accvgpr_write_b32 a118, v233\n\t"
"v_accvgpr_write_b32 a119, v234\n\t"
"v_accvgpr_read_b32 v233, a120\n\t"
"v_accvgpr_read_b32 v234, a121\n\t"
"v_mul_f32_e32 v233, v233, v238\n\t"
"v_mul_f32_e32 v234, v234, v238\n\t"
"v_accvgpr_write_b32 a120, v233\n\t"
"v_accvgpr_write_b32 a121, v234\n\t"
"v_accvgpr_read_b32 v233, a122\n\t"
"v_accvgpr_read_b32 v234, a123\n\t"
"v_mul_f32_e32 v233, v233, v238\n\t"
"v_mul_f32_e32 v234, v234, v238\n\t"
"v_accvgpr_write_b32 a122, v233\n\t"
"v_accvgpr_write_b32 a123, v234\n\t"
"v_accvgpr_read_b32 v233, a124\n\t"
"v_accvgpr_read_b32 v234, a125\n\t"
"v_mul_f32_e32 v233, v233, v238\n\t"
"v_mul_f32_e32 v234, v234, v238\n\t"
"v_accvgpr_write_b32 a124, v233\n\t"
"v_accvgpr_write_b32 a125, v234\n\t"
"v_accvgpr_read_b32 v233, a126\n\t"
"v_accvgpr_read_b32 v234, a127\n\t"
"v_mul_f32_e32 v233, v233, v238\n\t"
"v_mul_f32_e32 v234, v234, v238\n\t"
"v_accvgpr_write_b32 a126, v233\n\t"
"v_accvgpr_write_b32 a127, v234\n\t"
"v_accvgpr_read_b32 v233, a128\n\t"
"v_accvgpr_read_b32 v234, a129\n\t"
"v_mul_f32_e32 v233, v233, v239\n\t"
"v_mul_f32_e32 v234, v234, v239\n\t"
"v_accvgpr_write_b32 a128, v233\n\t"
"v_accvgpr_write_b32 a129, v234\n\t"
"v_accvgpr_read_b32 v233, a130\n\t"
"v_accvgpr_read_b32 v234, a131\n\t"
"v_mul_f32_e32 v233, v233, v239\n\t"
"v_mul_f32_e32 v234, v234, v239\n\t"
"v_accvgpr_write_b32 a130, v233\n\t"
"v_accvgpr_write_b32 a131, v234\n\t"
"v_accvgpr_read_b32 v233, a132\n\t"
"v_accvgpr_read_b32 v234, a133\n\t"
"v_mul_f32_e32 v233, v233, v239\n\t"
"v_mul_f32_e32 v234, v234, v239\n\t"
"v_accvgpr_write_b32 a132, v233\n\t"
"v_accvgpr_write_b32 a133, v234\n\t"
"v_accvgpr_read_b32 v233, a134\n\t"
"v_accvgpr_read_b32 v234, a135\n\t"
"v_mul_f32_e32 v233, v233, v239\n\t"
"v_mul_f32_e32 v234, v234, v239\n\t"
"v_accvgpr_write_b32 a134, v233\n\t"
"v_accvgpr_write_b32 a135, v234\n\t"
"v_accvgpr_read_b32 v233, a136\n\t"
"v_accvgpr_read_b32 v234, a137\n\t"
"v_mul_f32_e32 v233, v233, v239\n\t"
"v_mul_f32_e32 v234, v234, v239\n\t"
"v_accvgpr_write_b32 a136, v233\n\t"
"v_accvgpr_write_b32 a137, v234\n\t"
"v_accvgpr_read_b32 v233, a138\n\t"
"v_accvgpr_read_b32 v234, a139\n\t"
"v_mul_f32_e32 v233, v233, v239\n\t"
"v_mul_f32_e32 v234, v234, v239\n\t"
"v_accvgpr_write_b32 a138, v233\n\t"
"v_accvgpr_write_b32 a139, v234\n\t"
"v_accvgpr_read_b32 v233, a140\n\t"
"v_accvgpr_read_b32 v234, a141\n\t"
"v_mul_f32_e32 v233, v233, v239\n\t"
"v_mul_f32_e32 v234, v234, v239\n\t"
"v_accvgpr_write_b32 a140, v233\n\t"
"v_accvgpr_write_b32 a141, v234\n\t"
"v_accvgpr_read_b32 v233, a142\n\t"
"v_accvgpr_read_b32 v234, a143\n\t"
"v_mul_f32_e32 v233, v233, v239\n\t"
"v_mul_f32_e32 v234, v234, v239\n\t"
"v_accvgpr_write_b32 a142, v233\n\t"
"v_accvgpr_write_b32 a143, v234\n\t"
"v_accvgpr_read_b32 v233, a144\n\t"
"v_accvgpr_read_b32 v234, a145\n\t"
"v_mul_f32_e32 v233, v233, v239\n\t"
"v_mul_f32_e32 v234, v234, v239\n\t"
"v_accvgpr_write_b32 a144, v233\n\t"
"v_accvgpr_write_b32 a145, v234\n\t"
"v_accvgpr_read_b32 v233, a146\n\t"
"v_accvgpr_read_b32 v234, a147\n\t"
"v_mul_f32_e32 v233, v233, v239\n\t"
"v_mul_f32_e32 v234, v234, v239\n\t"
"v_accvgpr_write_b32 a146, v233\n\t"
"v_accvgpr_write_b32 a147, v234\n\t"
"v_accvgpr_read_b32 v233, a148\n\t"
"v_accvgpr_read_b32 v234, a149\n\t"
"v_mul_f32_e32 v233, v233, v239\n\t"
"v_mul_f32_e32 v234, v234, v239\n\t"
"v_accvgpr_write_b32 a148, v233\n\t"
"v_accvgpr_write_b32 a149, v234\n\t"
"v_accvgpr_read_b32 v233, a150\n\t"
"v_accvgpr_read_b32 v234, a151\n\t"
"v_mul_f32_e32 v233, v233, v239\n\t"
"v_mul_f32_e32 v234, v234, v239\n\t"
"v_accvgpr_write_b32 a150, v233\n\t"
"v_accvgpr_write_b32 a151, v234\n\t"
"v_accvgpr_read_b32 v233, a152\n\t"
"v_accvgpr_read_b32 v234, a153\n\t"
"v_mul_f32_e32 v233, v233, v239\n\t"
"v_mul_f32_e32 v234, v234, v239\n\t"
"v_accvgpr_write_b32 a152, v233\n\t"
"v_accvgpr_write_b32 a153, v234\n\t"
"v_accvgpr_read_b32 v233, a154\n\t"
"v_accvgpr_read_b32 v234, a155\n\t"
"v_mul_f32_e32 v233, v233, v239\n\t"
"v_mul_f32_e32 v234, v234, v239\n\t"
"v_accvgpr_write_b32 a154, v233\n\t"
"v_accvgpr_write_b32 a155, v234\n\t"
"v_accvgpr_read_b32 v233, a156\n\t"
"v_accvgpr_read_b32 v234, a157\n\t"
"v_mul_f32_e32 v233, v233, v239\n\t"
"v_mul_f32_e32 v234, v234, v239\n\t"
"v_accvgpr_write_b32 a156, v233\n\t"
"v_accvgpr_write_b32 a157, v234\n\t"
"v_accvgpr_read_b32 v233, a158\n\t"
"v_accvgpr_read_b32 v234, a159\n\t"
"v_mul_f32_e32 v233, v233, v239\n\t"
"v_mul_f32_e32 v234, v234, v239\n\t"
"v_accvgpr_write_b32 a158, v233\n\t"
"v_accvgpr_write_b32 a159, v234\n\t"
"v_accvgpr_read_b32 v233, a160\n\t"
"v_accvgpr_read_b32 v234, a161\n\t"
"v_mul_f32_e32 v233, v233, v240\n\t"
"v_mul_f32_e32 v234, v234, v240\n\t"
"v_accvgpr_write_b32 a160, v233\n\t"
"v_accvgpr_write_b32 a161, v234\n\t"
"v_accvgpr_read_b32 v233, a162\n\t"
"v_accvgpr_read_b32 v234, a163\n\t"
"v_mul_f32_e32 v233, v233, v240\n\t"
"v_mul_f32_e32 v234, v234, v240\n\t"
"v_accvgpr_write_b32 a162, v233\n\t"
"v_accvgpr_write_b32 a163, v234\n\t"
"v_accvgpr_read_b32 v233, a164\n\t"
"v_accvgpr_read_b32 v234, a165\n\t"
"v_mul_f32_e32 v233, v233, v240\n\t"
"v_mul_f32_e32 v234, v234, v240\n\t"
"v_accvgpr_write_b32 a164, v233\n\t"
"v_accvgpr_write_b32 a165, v234\n\t"
"v_accvgpr_read_b32 v233, a166\n\t"
"v_accvgpr_read_b32 v234, a167\n\t"
"v_mul_f32_e32 v233, v233, v240\n\t"
"v_mul_f32_e32 v234, v234, v240\n\t"
"v_accvgpr_write_b32 a166, v233\n\t"
"v_accvgpr_write_b32 a167, v234\n\t"
"v_accvgpr_read_b32 v233, a168\n\t"
"v_accvgpr_read_b32 v234, a169\n\t"
"v_mul_f32_e32 v233, v233, v240\n\t"
"v_mul_f32_e32 v234, v234, v240\n\t"
"v_accvgpr_write_b32 a168, v233\n\t"
"v_accvgpr_write_b32 a169, v234\n\t"
"v_accvgpr_read_b32 v233, a170\n\t"
"v_accvgpr_read_b32 v234, a171\n\t"
"v_mul_f32_e32 v233, v233, v240\n\t"
"v_mul_f32_e32 v234, v234, v240\n\t"
"v_accvgpr_write_b32 a170, v233\n\t"
"v_accvgpr_write_b32 a171, v234\n\t"
"v_accvgpr_read_b32 v233, a172\n\t"
"v_accvgpr_read_b32 v234, a173\n\t"
"v_mul_f32_e32 v233, v233, v240\n\t"
"v_mul_f32_e32 v234, v234, v240\n\t"
"v_accvgpr_write_b32 a172, v233\n\t"
"v_accvgpr_write_b32 a173, v234\n\t"
"v_accvgpr_read_b32 v233, a174\n\t"
"v_accvgpr_read_b32 v234, a175\n\t"
"v_mul_f32_e32 v233, v233, v240\n\t"
"v_mul_f32_e32 v234, v234, v240\n\t"
"v_accvgpr_write_b32 a174, v233\n\t"
"v_accvgpr_write_b32 a175, v234\n\t"
"v_accvgpr_read_b32 v233, a176\n\t"
"v_accvgpr_read_b32 v234, a177\n\t"
"v_mul_f32_e32 v233, v233, v240\n\t"
"v_mul_f32_e32 v234, v234, v240\n\t"
"v_accvgpr_write_b32 a176, v233\n\t"
"v_accvgpr_write_b32 a177, v234\n\t"
"v_accvgpr_read_b32 v233, a178\n\t"
"v_accvgpr_read_b32 v234, a179\n\t"
"v_mul_f32_e32 v233, v233, v240\n\t"
"v_mul_f32_e32 v234, v234, v240\n\t"
"v_accvgpr_write_b32 a178, v233\n\t"
"v_accvgpr_write_b32 a179, v234\n\t"
"v_accvgpr_read_b32 v233, a180\n\t"
"v_accvgpr_read_b32 v234, a181\n\t"
"v_mul_f32_e32 v233, v233, v240\n\t"
"v_mul_f32_e32 v234, v234, v240\n\t"
"v_accvgpr_write_b32 a180, v233\n\t"
"v_accvgpr_write_b32 a181, v234\n\t"
"v_accvgpr_read_b32 v233, a182\n\t"
"v_accvgpr_read_b32 v234, a183\n\t"
"v_mul_f32_e32 v233, v233, v240\n\t"
"v_mul_f32_e32 v234, v234, v240\n\t"
"v_accvgpr_write_b32 a182, v233\n\t"
"v_accvgpr_write_b32 a183, v234\n\t"
"v_accvgpr_read_b32 v233, a184\n\t"
"v_accvgpr_read_b32 v234, a185\n\t"
"v_mul_f32_e32 v233, v233, v240\n\t"
"v_mul_f32_e32 v234, v234, v240\n\t"
"v_accvgpr_write_b32 a184, v233\n\t"
"v_accvgpr_write_b32 a185, v234\n\t"
"v_accvgpr_read_b32 v233, a186\n\t"
"v_accvgpr_read_b32 v234, a187\n\t"
"v_mul_f32_e32 v233, v233, v240\n\t"
"v_mul_f32_e32 v234, v234, v240\n\t"
"v_accvgpr_write_b32 a186, v233\n\t"
"v_accvgpr_write_b32 a187, v234\n\t"
"v_accvgpr_read_b32 v233, a188\n\t"
"v_accvgpr_read_b32 v234, a189\n\t"
"v_mul_f32_e32 v233, v233, v240\n\t"
"v_mul_f32_e32 v234, v234, v240\n\t"
"v_accvgpr_write_b32 a188, v233\n\t"
"v_accvgpr_write_b32 a189, v234\n\t"
"v_accvgpr_read_b32 v233, a190\n\t"
"v_accvgpr_read_b32 v234, a191\n\t"
"v_mul_f32_e32 v233, v233, v240\n\t"
"v_mul_f32_e32 v234, v234, v240\n\t"
"v_accvgpr_write_b32 a190, v233\n\t"
"v_accvgpr_write_b32 a191, v234\n\t"
"s_nop 1\n\t"
"s_setpc_b64 s[66:67]\n\t"
"END%=:\n\t"
    :: "s"(kvb), "s"(xb), "s"(wb), "s"(ob), "s"(wid), "s"(ldsb), "s"(wid), "s"(wo), "s"(idx), "s"(ol), "s"(wl), "s"(op), "s"(myflag), "s"(pf), "s"(wid), "s"(woff), "s"(myacks), "s"(ackdst), "s"(krot), "s"(khead)
    : "memory", "vcc", "scc", "v0", "v1", "v2", "v3", "v4", "v5", "v6", "v7", "v8", "v9", "v10", "v11", "v12", "v13", "v14", "v15", "v16", "v17", "v18", "v19", "v20", "v21", "v22", "v23", "v24", "v25", "v26", "v27", "v28", "v29", "v30", "v31", "v32", "v33", "v34", "v35", "v36", "v37", "v38", "v39", "v40", "v41", "v42", "v43", "v44", "v45", "v46", "v47", "v48", "v49", "v50", "v51", "v52", "v53", "v54", "v55", "v56", "v57", "v58", "v59", "v60", "v61", "v62", "v63", "v64", "v65", "v66", "v67", "v68", "v69", "v70", "v71", "v72", "v73", "v74", "v75", "v76", "v77", "v78", "v79", "v80", "v81", "v82", "v83", "v84", "v85", "v86", "v87", "v88", "v89", "v90", "v91", "v92", "v93", "v94", "v95", "v96", "v97", "v98", "v99", "v100", "v101", "v102", "v103", "v104", "v105", "v106", "v107", "v108", "v109", "v110", "v111", "v112", "v113", "v114", "v115", "v116", "v117", "v118", "v119", "v120", "v121", "v122", "v123", "v124", "v125", "v126", "v127", "v128", "v129", "v130", "v131", "v132", "v133", "v134", "v135", "v136", "v137", "v138", "v139", "v140", "v141", "v142", "v143", "v144", "v145", "v146", "v147", "v148", "v149", "v150", "v151", "v152", "v153", "v154", "v155", "v156", "v157", "v158", "v159", "v160", "v161", "v162", "v163", "v164", "v165", "v166", "v167", "v168", "v169", "v170", "v171", "v172", "v173", "v174", "v175", "v176", "v177", "v178", "v179", "v180", "v181", "v182", "v183", "v184", "v185", "v186", "v187", "v188", "v189", "v190", "v191", "v192", "v193", "v194", "v195", "v196", "v197", "v198", "v199", "v200", "v201", "v202", "v203", "v204", "v205", "v206", "v207", "v208", "v209", "v210", "v211", "v212", "v213", "v214", "v215", "v216", "v217", "v218", "v219", "v220", "v221", "v222", "v223", "v224", "v225", "v226", "v227", "v228", "v229", "v230", "v231", "v232", "v233", "v234", "v235", "v236", "v237", "v238", "v239", "v240", "v241", "v242", "v243", "v244", "v245", "v246", "v247", "v248", "v249", "v250", "v251", "v252", "v253", "v254", "v255", "a0", "a1", "a2", "a3", "a4", "a5", "a6", "a7", "a8", "a9", "a10", "a11", "a12", "a13", "a14", "a15", "a16", "a17", "a18", "a19", "a20", "a21", "a22", "a23", "a24", "a25", "a26", "a27", "a28", "a29", "a30", "a31", "a32", "a33", "a34", "a35", "a36", "a37", "a38", "a39", "a40", "a41", "a42", "a43", "a44", "a45", "a46", "a47", "a48", "a49", "a50", "a51", "a52", "a53", "a54", "a55", "a56", "a57", "a58", "a59", "a60", "a61", "a62", "a63", "a64", "a65", "a66", "a67", "a68", "a69", "a70", "a71", "a72", "a73", "a74", "a75", "a76", "a77", "a78", "a79", "a80", "a81", "a82", "a83", "a84", "a85", "a86", "a87", "a88", "a89", "a90", "a91", "a92", "a93", "a94", "a95", "a96", "a97", "a98", "a99", "a100", "a101", "a102", "a103", "a104", "a105", "a106", "a107", "a108", "a109", "a110", "a111", "a112", "a113", "a114", "a115", "a116", "a117", "a118", "a119", "a120", "a121", "a122", "a123", "a124", "a125", "a126", "a127", "a128", "a129", "a130", "a131", "a132", "a133", "a134", "a135", "a136", "a137", "a138", "a139", "a140", "a141", "a142", "a143", "a144", "a145", "a146", "a147", "a148", "a149", "a150", "a151", "a152", "a153", "a154", "a155", "a156", "a157", "a158", "a159", "a160", "a161", "a162", "a163", "a164", "a165", "a166", "a167", "a168", "a169", "a170", "a171", "a172", "a173", "a174", "a175", "a176", "a177", "a178", "a179", "a180", "a181", "a182", "a183", "a184", "a185", "a186", "a187", "a188", "a189", "a190", "a191", "a192", "a193", "a194", "a195", "a196", "a197", "a198", "a199", "a200", "a201", "a202", "a203", "a204", "a205", "a206", "a207", "a208", "a209", "a210", "a211", "a212", "a213", "a214", "a215", "a216", "a217", "a218", "a219", "a220", "a221", "a222", "a223", "a224", "a225", "a226", "a227", "a228", "a229", "a230", "a231", "a232", "a233", "a234", "a235", "a236", "a237", "a238", "a239", "a240", "a241", "a242", "a243", "a244", "a245", "a246", "a247", "a248", "a249", "a250", "a251", "a252", "a253", "a254", "a255", "s36", "s37", "s38", "s39", "s40", "s41", "s42", "s43", "s44", "s45", "s46", "s47", "s48", "s49", "s50", "s51", "s52", "s53", "s54", "s55", "s56", "s57", "s58", "s59", "s60", "s61", "s62", "s63", "s64", "s65", "s66", "s67", "s68", "s69", "s70", "s71", "s72", "s73", "s74", "s75", "s76", "s77", "s78", "s79", "s80", "s81", "s82", "s83");
}

extern "C" void kernel_launch(void* const* d_in, const int* in_sizes, int n_in, void* d_out, int out_size, void* d_ws, size_t ws_size, hipStream_t stream) {
  (void)in_sizes; (void)n_in; (void)out_size; (void)ws_size;
  (void)hipFuncSetAttribute((const void*)attn_kernel, hipFuncAttributeMaxDynamicSharedMemorySize, 163840);
  hipLaunchKernelGGL(attn_kernel, dim3(256), dim3(256), 163840, stream,
                     (const char*)d_in[0], (const char*)d_in[1], (const char*)d_in[2], (const char*)d_in[3],
                     (const char*)d_in[5], (const char*)d_in[6], (const char*)d_in[7], (const char*)d_in[8], (char*)d_ws, (const char*)d_in[4], (const char*)d_in[9], (char*)d_out);
}
```
